```python
import torch

def _softmax(x, axis=-1):
    return torch.softmax(x, dim=axis)
def _gelu(x):
    return torch.nn.functional.gelu(x, approximate="tanh")
def _silu(x):
    return torch.nn.functional.silu(x)
def _sigmoid(x):
    return torch.sigmoid(x)
def _layernorm(x, axis=-1, eps=1e-5):
    m = x.mean(dim=axis, keepdim=True)
    v = x.var(dim=axis, keepdim=True, unbiased=False)
    return (x - m) / torch.sqrt(v + eps)
def _scatter_add(tgt, idx, src, axis=0):
    return tgt.clone().index_add_(axis, idx, src)
def _segment_sum(src, seg, n):
    out = torch.zeros((n,) + src.shape[1:], dtype=src.dtype)
    return out.index_add_(0, seg, src)
def _pad(x, pw):
    flat = tuple(v for pair in reversed(pw) for v in pair)
    return torch.nn.functional.pad(x, flat)

kernel_name = 'concat_heads__gqa_block_cached__gqa_block_cached__near_ridge'

def setup_inputs():

    _w = (lambda a: (lambda t: t.to(torch.bfloat16) if t.is_floating_point() else t)(torch.as_tensor(a)))
    def _A_setup():
        B, Sq, D = 8, 160, 576
        Hq, Hkv, Sk, Dh = 32, 16, 1024, 128
        assert Hq % Hkv == 0, (Hq, Hkv)
        return {
            "x": _w((lambda *s: torch.randn(*s, dtype=torch.bfloat16) * 0.8)(B, Sq, D)),
            "k_cache": _w((lambda *s: torch.randn(*s, dtype=torch.bfloat16) * 0.8)(B, Hkv, Sk, Dh)),
            "v_cache": _w((lambda *s: torch.randn(*s, dtype=torch.bfloat16) * 0.05)(B, Hkv, Sk, Dh)),
            "wq": _w((lambda *s: torch.randn(*s, dtype=torch.bfloat16) * 0.8)(D, Hq * Dh) * (D ** -0.5)),
            "wo": _w((lambda *s: torch.randn(*s, dtype=torch.bfloat16) * 0.05)(Hq * Dh, D) * ((Hq * Dh) ** -0.5)),
        }
    _A_in = _A_setup()

    def _B_setup():
        B, Sq, D = 8, 160, 576
        Hq, Hkv, Sk, Dh = 32, 16, 1024, 128
        assert Hq % Hkv == 0, (Hq, Hkv)
        return {
            "x": _w((lambda *s: torch.randn(*s, dtype=torch.bfloat16) * 0.8)(B, Sq, D)),
            "k_cache": _w((lambda *s: torch.randn(*s, dtype=torch.bfloat16) * 0.8)(B, Hkv, Sk, Dh)),
            "v_cache": _w((lambda *s: torch.randn(*s, dtype=torch.bfloat16) * 0.05)(B, Hkv, Sk, Dh)),
            "wq": _w((lambda *s: torch.randn(*s, dtype=torch.bfloat16) * 0.8)(D, Hq * Dh) * (D ** -0.5)),
            "wo": _w((lambda *s: torch.randn(*s, dtype=torch.bfloat16) * 0.05)(Hq * Dh, D) * ((Hq * Dh) ** -0.5)),
        }
    _B_in = _B_setup()

    return {
        "A_x": _A_in["x"],
        "A_k_cache": _A_in["k_cache"],
        "A_v_cache": _A_in["v_cache"],
        "A_wq": _A_in["wq"],
        "A_wo": _A_in["wo"],
        "B_x": _B_in["x"],
        "B_k_cache": _B_in["k_cache"],
        "B_v_cache": _B_in["v_cache"],
        "B_wq": _B_in["wq"],
        "B_wo": _B_in["wo"],
    }

def reference(A_x, A_k_cache, A_v_cache, A_wq, A_wo, B_x, B_k_cache, B_v_cache, B_wq, B_wo):
    def _A_ref(x, k_cache, v_cache, wq, wo):
        B, Sq, D = x.shape
        Hq, Dh = 32, 128
        Hkv = k_cache.shape[1]
        rep = Hq // Hkv
        q = (lambda x, *d: x.permute(*d))((x @ wq).reshape(B, Sq, Hq, Dh), 0, 2, 1, 3)
        k = (lambda x, n, axis: torch.repeat_interleave(x, n, dim=axis))(k_cache, rep, axis=1)
        v = (lambda x, n, axis: torch.repeat_interleave(x, n, dim=axis))(v_cache, rep, axis=1)
        scores = torch.einsum("bhqd,bhkd->bhqk", q, k) * (Dh ** -0.5)
        p = _softmax(scores, axis=-1)
        o = torch.einsum("bhqk,bhkd->bhqd", p, v)
        return (lambda x, *d: x.permute(*d))(o, 0, 2, 1, 3).reshape(B, Sq, Hq * Dh) @ wo

    def _B_ref(x, k_cache, v_cache, wq, wo):
        B, Sq, D = x.shape
        Hq, Dh = 32, 128
        Hkv = k_cache.shape[1]
        rep = Hq // Hkv
        q = (lambda x, *d: x.permute(*d))((x @ wq).reshape(B, Sq, Hq, Dh), 0, 2, 1, 3)
        k = (lambda x, n, axis: torch.repeat_interleave(x, n, dim=axis))(k_cache, rep, axis=1)
        v = (lambda x, n, axis: torch.repeat_interleave(x, n, dim=axis))(v_cache, rep, axis=1)
        scores = torch.einsum("bhqd,bhkd->bhqk", q, k) * (Dh ** -0.5)
        p = _softmax(scores, axis=-1)
        o = torch.einsum("bhqk,bhkd->bhqd", p, v)
        return (lambda x, *d: x.permute(*d))(o, 0, 2, 1, 3).reshape(B, Sq, Hq * Dh) @ wo

    a = _A_ref(A_x, A_k_cache, A_v_cache, A_wq, A_wo)
    b = _B_ref(B_x, B_k_cache, B_v_cache, B_wq, B_wo)
    return torch.concatenate([a, b], axis=-1)
```

```cpp
#include <hip/hip_runtime.h>
#include <hip/hip_bf16.h>
typedef unsigned short u16;
__device__ __forceinline__ float bf2f(u16 x){ return __uint_as_float(((unsigned)x)<<16); }
__device__ __forceinline__ u16 f2bf(float f){ unsigned u=__float_as_uint(f); u += 0x7fffu + ((u>>16)&1u); return (u16)(u>>16); }
constexpr int B=8,SQ=160,D=576,HQ=32,HKV=16,SK=1024,DH=128;
__global__ void k_q(const u16* xA,const u16* wA,const u16* xB,const u16* wB,u16* q){
  int br=blockIdx.z; const u16* x=br?xB:xA; const u16* w=br?wB:wA;
  int row0=blockIdx.y*8; int col=blockIdx.x*256+threadIdx.x;
  __shared__ float xs[8][D];
  for(int i=threadIdx.x;i<8*D;i+=256) xs[i/D][i%D]=bf2f(x[(size_t)(row0+i/D)*D+i%D]);
  __syncthreads();
  float acc[8]={0,0,0,0,0,0,0,0};
  for(int c=0;c<D;++c){ float wv=bf2f(w[(size_t)c*4096+col]);
#pragma unroll
    for(int r=0;r<8;++r) acc[r]+=xs[r][c]*wv; }
#pragma unroll
  for(int r=0;r<8;++r) q[((size_t)br*1280+row0+r)*4096+col]=f2bf(acc[r]);
}
__global__ __launch_bounds__(256) void k_attn(const u16* q,const u16* kA,const u16* vA,const u16* kB,const u16* vB,u16* o){
  int br=blockIdx.z; int bh=blockIdx.y; int b=bh/HQ,h=bh%HQ; int r0=blockIdx.x*4;
  const u16* k=(br?kB:kA)+((size_t)(b*HKV+h/2))*SK*DH; const u16* v=(br?vB:vA)+((size_t)(b*HKV+h/2))*SK*DH;
  __shared__ float qs[4][DH]; __shared__ float s[4][SK]; __shared__ float red[4][2];
  for(int i=threadIdx.x;i<4*DH;i+=256) qs[i/DH][i%DH]=bf2f(q[((size_t)br*1280+b*SQ+r0+i/DH)*4096+h*DH+i%DH]);
  __syncthreads();
  for(int j=threadIdx.x;j<SK;j+=256){ float a[4]={0,0,0,0};
    _Pragma("unroll 1") for(int d=0;d<DH;d+=8){ uint4 kk=*(const uint4*)(k+(size_t)j*DH+d); unsigned w4[4]={kk.x,kk.y,kk.z,kk.w};
#pragma unroll
      for(int e=0;e<4;++e){ float k0=__uint_as_float(w4[e]<<16),k1=__uint_as_float(w4[e]&0xffff0000u);
#pragma unroll
        for(int r=0;r<4;++r) a[r]+=qs[r][d+2*e]*k0+qs[r][d+2*e+1]*k1; } }
#pragma unroll
    for(int r=0;r<4;++r) s[r][j]=a[r]*0.08838834764831845f; }
  __syncthreads();
  int w=threadIdx.x>>6,l=threadIdx.x&63;
  { float m=-INFINITY; for(int j=l;j<SK;j+=64) m=fmaxf(m,s[w][j]);
    for(int o2=32;o2;o2>>=1) m=fmaxf(m,__shfl_xor(m,o2));
    float sum=0; for(int j=l;j<SK;j+=64){ float p=__expf(s[w][j]-m); s[w][j]=p; sum+=p; }
    for(int o2=32;o2;o2>>=1) sum+=__shfl_xor(sum,o2);
    if(l==0) red[w][0]=1.f/sum; }
  __syncthreads();
  { int d=threadIdx.x&127, rr=(threadIdx.x>>7)*2; float a0=0,a1=0;
    for(int j=0;j<SK;++j){ float vv=bf2f(v[(size_t)j*DH+d]); a0+=s[rr][j]*vv; a1+=s[rr+1][j]*vv; }
    o[((size_t)br*1280+b*SQ+r0+rr)*4096+h*DH+d]=f2bf(a0*red[rr][0]);
    o[((size_t)br*1280+b*SQ+r0+rr+1)*4096+h*DH+d]=f2bf(a1*red[rr+1][0]); }
}
__global__ void k_o(const u16* o,const u16* wA,const u16* wB,u16* out){
  int br=blockIdx.z; const u16* w=br?wB:wA; int row0=blockIdx.y*4; int col=blockIdx.x*64+(threadIdx.x&63); int ks=threadIdx.x>>6;
  __shared__ float os[4][4096]; __shared__ float part[4][4][64];
  for(int i=threadIdx.x;i<4*4096;i+=256) os[i>>12][i&4095]=bf2f(o[((size_t)br*1280+row0+(i>>12))*4096+(i&4095)]);
  __syncthreads();
  float a[4]={0,0,0,0};
  for(int c=ks*1024;c<ks*1024+1024;++c){ float wv=bf2f(w[(size_t)c*D+col]);
#pragma unroll
    for(int r=0;r<4;++r) a[r]+=os[r][c]*wv; }
#pragma unroll
  for(int r=0;r<4;++r) part[ks][r][threadIdx.x&63]=a[r];
  __syncthreads();
  int r=threadIdx.x>>6,c=threadIdx.x&63; float t=part[0][r][c]+part[1][r][c]+part[2][r][c]+part[3][r][c];
  out[(size_t)(row0+r)*1152+br*D+blockIdx.x*64+c]=f2bf(t);
}
extern "C" void kernel_launch(void* const* d_in, const int* in_sizes, int n_in, void* d_out, int out_size, void* d_ws, size_t ws_size, hipStream_t stream){
  const u16 *xA=(const u16*)d_in[0],*kA=(const u16*)d_in[1],*vA=(const u16*)d_in[2],*wqA=(const u16*)d_in[3],*woA=(const u16*)d_in[4];
  const u16 *xB=(const u16*)d_in[5],*kB=(const u16*)d_in[6],*vB=(const u16*)d_in[7],*wqB=(const u16*)d_in[8],*woB=(const u16*)d_in[9];
  u16* q=(u16*)d_ws; u16* o=q+(size_t)2*1280*4096;
  k_q<<<dim3(16,160,2),256,0,stream>>>(xA,wqA,xB,wqB,q);
  k_attn<<<dim3(40,256,2),256,0,stream>>>(q,kA,vA,kB,vB,o);
  k_o<<<dim3(9,320,2),256,0,stream>>>(o,woA,woB,(u16*)d_out);
}
```

```cpp
#include <hip/hip_runtime.h>
#include <hip/hip_bf16.h>

__global__ __launch_bounds__(256, 1) void gqa_fused(const char* xA, const char* kA, const char* vA, const char* wqA, const char* woA,
                                                    const char* xB, const char* kB, const char* vB, const char* wqB, const char* woB, char* out, char* ws) {
  const unsigned wid = __builtin_amdgcn_readfirstlane(threadIdx.x >> 6);
  asm volatile(
"Lstart_%=:\n"
"s_bfe_u32 s34, %[bid], 0x10002\n"
"s_mul_i32 s35, s34, 40\n"
"s_mov_b64 s[18:19], %[ka]\n"
"s_add_u32 s18, s18, s35\n"
"s_addc_u32 s19, s19, 0\n"
"s_load_dwordx8 s[8:15], s[18:19], 0x0\n"
"s_load_dwordx2 s[16:17], s[18:19], 0x20\n"
"s_load_dwordx4 s[20:23], %[ka], 0x50\n"
"v_mbcnt_lo_u32_b32 v0, -1, 0\n"
"v_mbcnt_hi_u32_b32 v0, -1, v0\n"
"s_lshr_b32 s24, %[bid], 3\n"
"s_and_b32 s26, %[bid], 3\n"
"s_lshl_b32 s26, s26, 2\n"
"s_and_b32 s27, s24, 3\n"
"s_or_b32 s26, s26, s27\n"
"s_lshr_b32 s27, %[bid], 5\n"
"s_getpc_b64 s[4:5]\n"
"Lpc_%=:\n"
"s_and_b32 s5, s5, 0xffff\n"
"s_mov_b32 s6, Lend_%=-Lpc_%=\n"
"s_mov_b32 s7, 0x20000\n"
"s_lshl_b32 s96, s24, 2\n"
"s_add_u32 s96, s96, %[wid]\n"
"s_and_b32 s96, s96, 63\n"
"s_mul_i32 s96, s96, 1152\n"
"v_min_u32 v61, 9, v0\n"
"v_lshlrev_b32 v61, 7, v61\n"
"buffer_load_dword v60, v61, s[4:7], s96 offen\n"
"v_lshrrev_b32 v12, 5, v0\n"
"v_bfe_u32 v13, v0, 2, 3\n"
"v_and_b32 v14, 3, v0\n"
"v_lshlrev_b32 v14, 4, v14\n"
"v_lshl_add_u32 v14, v12, 13, v14\n"
"v_xor_b32 v13, v13, v12\n"
"v_lshl_add_u32 v22, v13, 6, v14\n"
"v_xor_b32 v23, 0x80, v22\n"
"s_lshl_b32 s96, %[wid], 17\n"
"s_add_u32 s68, s96, 0\n"
"s_add_u32 s69, s96, 15360\n"
"s_add_u32 s70, s96, 30720\n"
"s_add_u32 s71, s96, 46080\n"
"s_add_u32 s72, s96, 65536\n"
"s_add_u32 s73, s96, 80896\n"
"s_add_u32 s74, s96, 96256\n"
"s_add_u32 s75, s96, 111616\n"
"s_mul_i32 s81, %[wid], 8208\n"
"v_lshrrev_b32 v12, 3, v0\n"
"v_mul_u32_u24 v12, 1152, v12\n"
"v_and_b32 v13, 7, v0\n"
"v_lshrrev_b32 v14, 4, v0\n"
"v_xor_b32 v13, v13, v14\n"
"s_and_b32 s96, %[wid], 1\n"
"s_lshl_b32 s96, s96, 2\n"
"v_xor_b32 v13, s96, v13\n"
"v_lshl_add_u32 v24, v13, 4, v12\n"
"v_xor_b32 v13, 4, v13\n"
"v_lshl_add_u32 v25, v13, 4, v12\n"
"s_mul_i32 s96, %[wid], 46080\n"
"s_add_u32 s76, s96, 0\n"
"s_add_u32 s77, s96, 8192\n"
"s_add_u32 s78, s96, 16384\n"
"s_add_u32 s79, s96, 24576\n"
"s_add_u32 s80, s96, 36864\n"
"s_mul_i32 s82, %[wid], 5120\n"
"s_add_u32 s82, s82, 32832\n"
"s_mov_b32 s42, 4710912\n"
"s_mov_b32 s43, 0x20000\n"
"s_mov_b32 s38, 184320\n"
"s_mov_b32 s39, 0x20000\n"
"s_lshl_b32 s28, s26, 9\n"
"s_mul_i32 s29, s27, 184320\n"
"s_waitcnt lgkmcnt(0)\n"
"s_add_u32 s40, s14, s28\n"
"s_addc_u32 s41, s15, 0\n"
"s_and_b32 s41, s41, 0xffff\n"
"s_add_u32 s36, s8, s29\n"
"s_addc_u32 s37, s9, 0\n"
"s_and_b32 s37, s37, 0xffff\n"
"s_add_u32 m0, s81, 0\n"
"s_nop 0\n"
"buffer_load_dwordx4 v22, s[40:43], s68 offen offset:0 nt lds\n"
"buffer_load_dwordx4 v23, s[40:43], s69 offen offset:1024 nt lds\n"
"buffer_load_dwordx4 v22, s[40:43], s70 offen offset:2048 nt lds\n"
"buffer_load_dwordx4 v23, s[40:43], s71 offen offset:3072 nt lds\n"
"s_add_u32 m0, s81, 4104\n"
"s_nop 0\n"
"buffer_load_dwordx4 v22, s[40:43], s72 offen offset:0 nt lds\n"
"buffer_load_dwordx4 v23, s[40:43], s73 offen offset:1024 nt lds\n"
"buffer_load_dwordx4 v22, s[40:43], s74 offen offset:2048 nt lds\n"
"buffer_load_dwordx4 v23, s[40:43], s75 offen offset:3072 nt lds\n"
"s_add_u32 m0, s82, 0\n"
"s_nop 0\n"
"buffer_load_dwordx4 v24, s[36:39], s76 offen offset:0 lds\n"
"buffer_load_dwordx4 v25, s[36:39], s77 offen offset:1024 lds\n"
"buffer_load_dwordx4 v24, s[36:39], s78 offen offset:2048 lds\n"
"buffer_load_dwordx4 v25, s[36:39], s79 offen offset:3072 lds\n"
"s_add_u32 m0, s82, 4096\n"
"s_nop 0\n"
"buffer_load_dwordx4 v24, s[36:39], s80 offen offset:0 lds\n"
"s_add_u32 s40, s40, 524288\n"
"s_addc_u32 s41, s41, 0\n"
"s_sub_u32 s42, s42, 524288\n"
"s_add_u32 s36, s36, 128\n"
"s_addc_u32 s37, s37, 0\n"
"s_sub_u32 s38, s38, 128\n"
"s_lshl_b32 s25, %[bid], 2\n"
"s_add_u32 s25, s25, %[wid]\n"
"s_mov_b64 s[64:65], s[22:23]\n"
"s_add_u32 s64, s64, 25165824\n"
"s_addc_u32 s65, s65, 0\n"
"s_and_b32 s65, s65, 0xffff\n"
"s_mov_b32 s66, 32768\n"
"s_mov_b32 s67, 0x20000\n"
"s_lshl_b32 s96, s25, 3\n"
"s_add_u32 s96, s96, 24576\n"
"v_mov_b32 v7, s96\n"
"s_cmp_ge_u32 s25, 1020\n"
"s_cbranch_scc0 Lld0_%=\n"
"v_mov_b32 v48, 0\n"
"v_mov_b32 v49, 0\n"
"v_mov_b32 v50, 0\n"
"v_mov_b32 v51, 0\n"
"v_lshlrev_b32 v56, 4, v0\n"
"s_lshl_b32 s96, %[wid], 12\n"
"buffer_store_dwordx4 v[48:51], v56, s[64:67], s96 offen offset:0 sc1\n"
"buffer_store_dwordx4 v[48:51], v56, s[64:67], s96 offen offset:1024 sc1\n"
"buffer_store_dwordx4 v[48:51], v56, s[64:67], s96 offen offset:2048 sc1\n"
"buffer_store_dwordx4 v[48:51], v56, s[64:67], s96 offen offset:3072 sc1\n"
"s_lshl_b32 s96, %[wid], 10\n"
"v_add_u32 v56, s96, v56\n"
"v_add_u32 v57, 20480, v56\n"
"buffer_load_dwordx4 v[40:43], v57, s[64:67], 0 offen sc1\n"
"Lld0_%=:\n"
"v_lshrrev_b32 v26, 4, v0\n"
"v_bfe_u32 v27, v0, 2, 2\n"
"v_and_b32 v28, 3, v0\n"
"v_and_b32 v29, 15, v0\n"
"v_lshlrev_b32 v30, 12, v26\n"
"v_lshl_add_u32 v30, v26, 3, v30\n"
"v_lshl_add_u32 v30, v27, 9, v30\n"
"v_lshl_add_u32 v30, v28, 4, v30\n"
"s_lshr_b32 s96, %[wid], 1\n"
"s_lshl_b32 s96, s96, 8\n"
"v_add_u32 v30, s96, v30\n"
"v_xor_b32 v31, 0, v27\n"
"v_lshl_add_u32 v16, v31, 6, v30\n"
"v_xor_b32 v31, 1, v27\n"
"v_lshl_add_u32 v17, v31, 6, v30\n"
"v_xor_b32 v31, 2, v27\n"
"v_lshl_add_u32 v18, v31, 6, v30\n"
"v_xor_b32 v31, 3, v27\n"
"v_lshl_add_u32 v19, v31, 6, v30\n"
"s_and_b32 s96, %[wid], 1\n"
"s_mul_i32 s96, s96, 10240\n"
"s_add_u32 s96, s96, 32832\n"
"v_lshlrev_b32 v30, 7, v29\n"
"v_add_u32 v30, s96, v30\n"
"v_lshrrev_b32 v31, 1, v29\n"
"v_xor_b32 v31, v31, v26\n"
"v_lshl_add_u32 v20, v31, 4, v30\n"
"v_xor_b32 v31, 4, v31\n"
"v_lshl_add_u32 v21, v31, 4, v30\n"
"s_add_u32 m0, s81, 53312\n"
"s_nop 0\n"
"buffer_load_dwordx4 v22, s[40:43], s68 offen offset:0 nt lds\n"
"buffer_load_dwordx4 v23, s[40:43], s69 offen offset:1024 nt lds\n"
"buffer_load_dwordx4 v22, s[40:43], s70 offen offset:2048 nt lds\n"
"buffer_load_dwordx4 v23, s[40:43], s71 offen offset:3072 nt lds\n"
"s_add_u32 m0, s81, 57416\n"
"s_nop 0\n"
"buffer_load_dwordx4 v22, s[40:43], s72 offen offset:0 nt lds\n"
"buffer_load_dwordx4 v23, s[40:43], s73 offen offset:1024 nt lds\n"
"buffer_load_dwordx4 v22, s[40:43], s74 offen offset:2048 nt lds\n"
"buffer_load_dwordx4 v23, s[40:43], s75 offen offset:3072 nt lds\n"
"s_waitcnt vmcnt(8)\n"
"s_barrier\n"
"s_add_u32 m0, s82, 53312\n"
"ds_read_b64_tr_b16 a[152:153], v16 offset:0\n"
"ds_read_b64_tr_b16 a[154:155], v16 offset:2048\n"
"ds_read_b64_tr_b16 a[156:157], v16 offset:16416\n"
"buffer_load_dwordx4 v24, s[36:39], s76 offen offset:0 lds\n"
"ds_read_b64_tr_b16 a[158:159], v16 offset:18464\n"
"ds_read_b128 a[216:219], v20 offset:0\n"
"ds_read_b128 a[220:223], v21 offset:0\n"
"buffer_load_dwordx4 v25, s[36:39], s77 offen offset:1024 lds\n"
"ds_read_b128 a[224:227], v20 offset:2048\n"
"ds_read_b128 a[228:231], v21 offset:2048\n"
"ds_read_b128 a[232:235], v20 offset:4096\n"
"buffer_load_dwordx4 v24, s[36:39], s78 offen offset:2048 lds\n"
"ds_read_b128 a[236:239], v21 offset:4096\n"
"ds_read_b128 a[240:243], v20 offset:6144\n"
"ds_read_b128 a[244:247], v21 offset:6144\n"
"buffer_load_dwordx4 v25, s[36:39], s79 offen offset:3072 lds\n"
"s_add_u32 m0, s82, 57408\n"
"ds_read_b128 a[248:251], v20 offset:8192\n"
"ds_read_b128 a[252:255], v21 offset:8192\n"
"buffer_load_dwordx4 v24, s[36:39], s80 offen offset:0 lds\n"
"s_add_u32 s40, s40, 524288\n"
"s_addc_u32 s41, s41, 0\n"
"s_sub_u32 s42, s42, 524288\n"
"s_add_u32 s36, s36, 128\n"
"s_addc_u32 s37, s37, 0\n"
"s_sub_u32 s38, s38, 128\n"
"s_waitcnt lgkmcnt(8)\n"
"v_mfma_f32_16x16x32_bf16 v[80:83], a[152:155], a[216:219], 0\n"
"ds_read_b64_tr_b16 a[160:161], v16 offset:8\n"
"s_add_u32 m0, s81, 106624\n"
"v_mfma_f32_16x16x32_bf16 v[80:83], a[156:159], a[220:223], v[80:83]\n"
"s_waitcnt lgkmcnt(7)\n"
"buffer_load_dwordx4 v22, s[40:43], s68 offen offset:0 nt lds\n"
"v_mfma_f32_16x16x32_bf16 v[112:115], a[156:159], a[228:231], 0\n"
"ds_read_b64_tr_b16 a[162:163], v16 offset:2056\n"
"v_mfma_f32_16x16x32_bf16 v[112:115], a[152:155], a[224:227], v[112:115]\n"
"s_waitcnt lgkmcnt(6)\n"
"ds_read_b64_tr_b16 a[164:165], v16 offset:16424\n"
"v_mfma_f32_16x16x32_bf16 v[144:147], a[152:155], a[232:235], 0\n"
"buffer_load_dwordx4 v23, s[40:43], s69 offen offset:1024 nt lds\n"
"v_mfma_f32_16x16x32_bf16 v[144:147], a[156:159], a[236:239], v[144:147]\n"
"s_waitcnt lgkmcnt(5)\n"
"ds_read_b64_tr_b16 a[166:167], v16 offset:18472\n"
"v_mfma_f32_16x16x32_bf16 v[176:179], a[156:159], a[244:247], 0\n"
"ds_read_b64_tr_b16 a[168:169], v17 offset:0\n"
"v_mfma_f32_16x16x32_bf16 v[176:179], a[152:155], a[240:243], v[176:179]\n"
"s_waitcnt lgkmcnt(5)\n"
"buffer_load_dwordx4 v22, s[40:43], s70 offen offset:2048 nt lds\n"
"v_mfma_f32_16x16x32_bf16 v[208:211], a[152:155], a[248:251], 0\n"
"ds_read_b64_tr_b16 a[170:171], v17 offset:2048\n"
"v_mfma_f32_16x16x32_bf16 v[208:211], a[156:159], a[252:255], v[208:211]\n"
"s_waitcnt lgkmcnt(2)\n"
"ds_read_b64_tr_b16 a[172:173], v17 offset:16416\n"
"v_mfma_f32_16x16x32_bf16 v[212:215], a[164:167], a[252:255], 0\n"
"buffer_load_dwordx4 v23, s[40:43], s71 offen offset:3072 nt lds\n"
"v_mfma_f32_16x16x32_bf16 v[212:215], a[160:163], a[248:251], v[212:215]\n"
"ds_read_b64_tr_b16 a[174:175], v17 offset:18464\n"
"v_mfma_f32_16x16x32_bf16 v[180:183], a[160:163], a[240:243], 0\n"
"ds_read_b64_tr_b16 a[176:177], v17 offset:8\n"
"s_add_u32 m0, s81, 110728\n"
"v_mfma_f32_16x16x32_bf16 v[180:183], a[164:167], a[244:247], v[180:183]\n"
"buffer_load_dwordx4 v22, s[40:43], s72 offen offset:0 nt lds\n"
"v_mfma_f32_16x16x32_bf16 v[148:151], a[164:167], a[236:239], 0\n"
"ds_read_b64_tr_b16 a[178:179], v17 offset:2056\n"
"v_mfma_f32_16x16x32_bf16 v[148:151], a[160:163], a[232:235], v[148:151]\n"
"ds_read_b64_tr_b16 a[180:181], v17 offset:16424\n"
"v_mfma_f32_16x16x32_bf16 v[116:119], a[160:163], a[224:227], 0\n"
"buffer_load_dwordx4 v23, s[40:43], s73 offen offset:1024 nt lds\n"
"v_mfma_f32_16x16x32_bf16 v[116:119], a[164:167], a[228:231], v[116:119]\n"
"ds_read_b64_tr_b16 a[182:183], v17 offset:18472\n"
"v_mfma_f32_16x16x32_bf16 v[84:87], a[164:167], a[220:223], 0\n"
"ds_read_b64_tr_b16 a[184:185], v18 offset:0\n"
"v_mfma_f32_16x16x32_bf16 v[84:87], a[160:163], a[216:219], v[84:87]\n"
"s_waitcnt lgkmcnt(5)\n"
"buffer_load_dwordx4 v22, s[40:43], s74 offen offset:2048 nt lds\n"
"v_mfma_f32_16x16x32_bf16 v[88:91], a[168:171], a[216:219], 0\n"
"ds_read_b64_tr_b16 a[186:187], v18 offset:2048\n"
"v_mfma_f32_16x16x32_bf16 v[88:91], a[172:175], a[220:223], v[88:91]\n"
"ds_read_b64_tr_b16 a[188:189], v18 offset:16416\n"
"v_mfma_f32_16x16x32_bf16 v[120:123], a[172:175], a[228:231], 0\n"
"buffer_load_dwordx4 v23, s[40:43], s75 offen offset:3072 nt lds\n"
"v_mfma_f32_16x16x32_bf16 v[120:123], a[168:171], a[224:227], v[120:123]\n"
"ds_read_b64_tr_b16 a[190:191], v18 offset:18464\n"
"v_mfma_f32_16x16x32_bf16 v[152:155], a[168:171], a[232:235], 0\n"
"ds_read_b64_tr_b16 a[192:193], v18 offset:8\n"
"s_add_u32 m0, s82, 106624\n"
"v_mfma_f32_16x16x32_bf16 v[152:155], a[172:175], a[236:239], v[152:155]\n"
"buffer_load_dwordx4 v24, s[36:39], s76 offen offset:0 lds\n"
"v_mfma_f32_16x16x32_bf16 v[184:187], a[172:175], a[244:247], 0\n"
"ds_read_b64_tr_b16 a[194:195], v18 offset:2056\n"
"v_mfma_f32_16x16x32_bf16 v[184:187], a[168:171], a[240:243], v[184:187]\n"
"ds_read_b64_tr_b16 a[196:197], v18 offset:16424\n"
"v_mfma_f32_16x16x32_bf16 v[216:219], a[168:171], a[248:251], 0\n"
"buffer_load_dwordx4 v25, s[36:39], s77 offen offset:1024 lds\n"
"v_mfma_f32_16x16x32_bf16 v[216:219], a[172:175], a[252:255], v[216:219]\n"
"s_waitcnt lgkmcnt(7)\n"
"ds_read_b64_tr_b16 a[198:199], v18 offset:18472\n"
"v_mfma_f32_16x16x32_bf16 v[220:223], a[180:183], a[252:255], 0\n"
"ds_read_b64_tr_b16 a[200:201], v19 offset:0\n"
"v_mfma_f32_16x16x32_bf16 v[220:223], a[176:179], a[248:251], v[220:223]\n"
"buffer_load_dwordx4 v24, s[36:39], s78 offen offset:2048 lds\n"
"v_mfma_f32_16x16x32_bf16 v[188:191], a[176:179], a[240:243], 0\n"
"ds_read_b64_tr_b16 a[202:203], v19 offset:2048\n"
"v_mfma_f32_16x16x32_bf16 v[188:191], a[180:183], a[244:247], v[188:191]\n"
"ds_read_b64_tr_b16 a[204:205], v19 offset:16416\n"
"v_mfma_f32_16x16x32_bf16 v[156:159], a[180:183], a[236:239], 0\n"
"buffer_load_dwordx4 v25, s[36:39], s79 offen offset:3072 lds\n"
"v_mfma_f32_16x16x32_bf16 v[156:159], a[176:179], a[232:235], v[156:159]\n"
"ds_read_b64_tr_b16 a[206:207], v19 offset:18464\n"
"v_mfma_f32_16x16x32_bf16 v[124:127], a[176:179], a[224:227], 0\n"
"ds_read_b64_tr_b16 a[208:209], v19 offset:8\n"
"s_add_u32 m0, s82, 110720\n"
"v_mfma_f32_16x16x32_bf16 v[124:127], a[180:183], a[228:231], v[124:127]\n"
"buffer_load_dwordx4 v24, s[36:39], s80 offen offset:0 lds\n"
"v_mfma_f32_16x16x32_bf16 v[92:95], a[180:183], a[220:223], 0\n"
"ds_read_b64_tr_b16 a[210:211], v19 offset:2056\n"
"s_add_u32 s40, s40, 524288\n"
"s_addc_u32 s41, s41, 0\n"
"s_sub_u32 s42, s42, 524288\n"
"v_mfma_f32_16x16x32_bf16 v[92:95], a[176:179], a[216:219], v[92:95]\n"
"s_waitcnt lgkmcnt(10)\n"
"ds_read_b64_tr_b16 a[212:213], v19 offset:16424\n"
"s_add_u32 s36, s36, 128\n"
"s_addc_u32 s37, s37, 0\n"
"s_sub_u32 s38, s38, 128\n"
"v_mfma_f32_16x16x32_bf16 v[96:99], a[184:187], a[216:219], 0\n"
"ds_read_b64_tr_b16 a[214:215], v19 offset:18472\n"
"v_mfma_f32_16x16x32_bf16 v[96:99], a[188:191], a[220:223], v[96:99]\n"
"v_add_u32 v16, 0xd040, v16\n"
"v_add_u32 v17, 0xd040, v17\n"
"v_add_u32 v18, 0xd040, v18\n"
"v_mfma_f32_16x16x32_bf16 v[128:131], a[188:191], a[228:231], 0\n"
"s_waitcnt vmcnt(13)\n"
"s_barrier\n"
"v_add_u32 v19, 0xd040, v19\n"
"v_add_u32 v20, 0xd040, v20\n"
"v_add_u32 v21, 0xd040, v21\n"
"v_mfma_f32_16x16x32_bf16 v[128:131], a[184:187], a[224:227], v[128:131]\n"
"ds_read_b64_tr_b16 a[0:1], v16 offset:0\n"
"v_mfma_f32_16x16x32_bf16 v[160:163], a[184:187], a[232:235], 0\n"
"ds_read_b64_tr_b16 a[2:3], v16 offset:2048\n"
"v_mfma_f32_16x16x32_bf16 v[160:163], a[188:191], a[236:239], v[160:163]\n"
"ds_read_b64_tr_b16 a[4:5], v16 offset:16416\n"
"v_mfma_f32_16x16x32_bf16 v[192:195], a[188:191], a[244:247], 0\n"
"ds_read_b64_tr_b16 a[6:7], v16 offset:18464\n"
"v_mfma_f32_16x16x32_bf16 v[192:195], a[184:187], a[240:243], v[192:195]\n"
"ds_read_b128 a[64:67], v20 offset:0\n"
"v_mfma_f32_16x16x32_bf16 v[224:227], a[184:187], a[248:251], 0\n"
"ds_read_b128 a[68:71], v21 offset:0\n"
"v_mfma_f32_16x16x32_bf16 v[224:227], a[188:191], a[252:255], v[224:227]\n"
"s_waitcnt lgkmcnt(14)\n"
"ds_read_b128 a[72:75], v20 offset:2048\n"
"v_mfma_f32_16x16x32_bf16 v[228:231], a[196:199], a[252:255], 0\n"
"ds_read_b128 a[76:79], v21 offset:2048\n"
"v_mfma_f32_16x16x32_bf16 v[228:231], a[192:195], a[248:251], v[228:231]\n"
"ds_read_b128 a[80:83], v20 offset:4096\n"
"v_mfma_f32_16x16x32_bf16 v[196:199], a[192:195], a[240:243], 0\n"
"ds_read_b128 a[84:87], v21 offset:4096\n"
"v_mfma_f32_16x16x32_bf16 v[196:199], a[196:199], a[244:247], v[196:199]\n"
"ds_read_b128 a[88:91], v20 offset:6144\n"
"v_mfma_f32_16x16x32_bf16 v[164:167], a[196:199], a[236:239], 0\n"
"ds_read_b128 a[92:95], v21 offset:6144\n"
"v_mfma_f32_16x16x32_bf16 v[164:167], a[192:195], a[232:235], v[164:167]\n"
"ds_read_b128 a[96:99], v20 offset:8192\n"
"v_mfma_f32_16x16x32_bf16 v[132:135], a[192:195], a[224:227], 0\n"
"ds_read_b128 a[100:103], v21 offset:8192\n"
"v_mfma_f32_16x16x32_bf16 v[132:135], a[196:199], a[228:231], v[132:135]\n"
"ds_read_b64_tr_b16 a[8:9], v16 offset:8\n"
"v_mfma_f32_16x16x32_bf16 v[100:103], a[196:199], a[220:223], 0\n"
"ds_read_b64_tr_b16 a[10:11], v16 offset:2056\n"
"v_mfma_f32_16x16x32_bf16 v[100:103], a[192:195], a[216:219], v[100:103]\n"
"s_waitcnt lgkmcnt(15)\n"
"ds_read_b64_tr_b16 a[12:13], v16 offset:16424\n"
"v_mfma_f32_16x16x32_bf16 v[104:107], a[200:203], a[216:219], 0\n"
"ds_read_b64_tr_b16 a[14:15], v16 offset:18472\n"
"v_mfma_f32_16x16x32_bf16 v[104:107], a[204:207], a[220:223], v[104:107]\n"
"ds_read_b64_tr_b16 a[16:17], v17 offset:0\n"
"v_mfma_f32_16x16x32_bf16 v[136:139], a[204:207], a[228:231], 0\n"
"ds_read_b64_tr_b16 a[18:19], v17 offset:2048\n"
"v_mfma_f32_16x16x32_bf16 v[136:139], a[200:203], a[224:227], v[136:139]\n"
"ds_read_b64_tr_b16 a[20:21], v17 offset:16416\n"
"v_mfma_f32_16x16x32_bf16 v[168:171], a[200:203], a[232:235], 0\n"
"ds_read_b64_tr_b16 a[22:23], v17 offset:18464\n"
"v_mfma_f32_16x16x32_bf16 v[168:171], a[204:207], a[236:239], v[168:171]\n"
"ds_read_b64_tr_b16 a[24:25], v17 offset:8\n"
"v_mfma_f32_16x16x32_bf16 v[200:203], a[204:207], a[244:247], 0\n"
"ds_read_b64_tr_b16 a[26:27], v17 offset:2056\n"
"v_mfma_f32_16x16x32_bf16 v[200:203], a[200:203], a[240:243], v[200:203]\n"
"ds_read_b64_tr_b16 a[28:29], v17 offset:16424\n"
"v_mfma_f32_16x16x32_bf16 v[232:235], a[200:203], a[248:251], 0\n"
"ds_read_b64_tr_b16 a[30:31], v17 offset:18472\n"
"v_mfma_f32_16x16x32_bf16 v[232:235], a[204:207], a[252:255], v[232:235]\n"
"ds_read_b64_tr_b16 a[32:33], v18 offset:0\n"
"v_mfma_f32_16x16x32_bf16 v[236:239], a[212:215], a[252:255], 0\n"
"ds_read_b64_tr_b16 a[34:35], v18 offset:2048\n"
"v_mfma_f32_16x16x32_bf16 v[236:239], a[208:211], a[248:251], v[236:239]\n"
"ds_read_b64_tr_b16 a[36:37], v18 offset:16416\n"
"v_mfma_f32_16x16x32_bf16 v[204:207], a[208:211], a[240:243], 0\n"
"ds_read_b64_tr_b16 a[38:39], v18 offset:18464\n"
"v_mfma_f32_16x16x32_bf16 v[204:207], a[212:215], a[244:247], v[204:207]\n"
"ds_read_b64_tr_b16 a[40:41], v18 offset:8\n"
"v_mfma_f32_16x16x32_bf16 v[172:175], a[212:215], a[236:239], 0\n"
"ds_read_b64_tr_b16 a[42:43], v18 offset:2056\n"
"v_mfma_f32_16x16x32_bf16 v[172:175], a[208:211], a[232:235], v[172:175]\n"
"ds_read_b64_tr_b16 a[44:45], v18 offset:16424\n"
"v_mfma_f32_16x16x32_bf16 v[140:143], a[208:211], a[224:227], 0\n"
"ds_read_b64_tr_b16 a[46:47], v18 offset:18472\n"
"v_mfma_f32_16x16x32_bf16 v[140:143], a[212:215], a[228:231], v[140:143]\n"
"ds_read_b64_tr_b16 a[48:49], v19 offset:0\n"
"v_mfma_f32_16x16x32_bf16 v[108:111], a[212:215], a[220:223], 0\n"
"ds_read_b64_tr_b16 a[50:51], v19 offset:2048\n"
"v_mfma_f32_16x16x32_bf16 v[108:111], a[208:211], a[216:219], v[108:111]\n"
"s_waitcnt lgkmcnt(15)\n"
"ds_read_b64_tr_b16 a[52:53], v19 offset:16416\n"
"ds_read_b64_tr_b16 a[54:55], v19 offset:18464\n"
"ds_read_b64_tr_b16 a[56:57], v19 offset:8\n"
"ds_read_b64_tr_b16 a[58:59], v19 offset:2056\n"
"ds_read_b64_tr_b16 a[60:61], v19 offset:16424\n"
"ds_read_b64_tr_b16 a[62:63], v19 offset:18472\n"
"v_mfma_f32_16x16x32_bf16 v[80:83], a[0:3], a[64:67], v[80:83]\n"
"v_mfma_f32_16x16x32_bf16 v[80:83], a[4:7], a[68:71], v[80:83]\n"
"v_mfma_f32_16x16x32_bf16 v[112:115], a[4:7], a[76:79], v[112:115]\n"
"v_mfma_f32_16x16x32_bf16 v[112:115], a[0:3], a[72:75], v[112:115]\n"
"v_mfma_f32_16x16x32_bf16 v[144:147], a[0:3], a[80:83], v[144:147]\n"
"v_mfma_f32_16x16x32_bf16 v[144:147], a[4:7], a[84:87], v[144:147]\n"
"s_waitcnt vmcnt(0) lgkmcnt(0)\n"
"s_barrier\n"
"v_add_u32 v16, 0xd040, v16\n"
"v_add_u32 v17, 0xd040, v17\n"
"v_mfma_f32_16x16x32_bf16 v[176:179], a[4:7], a[92:95], v[176:179]\n"
"s_add_u32 m0, s81, 0\n"
"v_add_u32 v18, 0xd040, v18\n"
"v_add_u32 v19, 0xd040, v19\n"
"v_mfma_f32_16x16x32_bf16 v[176:179], a[0:3], a[88:91], v[176:179]\n"
"buffer_load_dwordx4 v22, s[40:43], s68 offen offset:0 nt lds\n"
"v_mfma_f32_16x16x32_bf16 v[208:211], a[0:3], a[96:99], v[208:211]\n"
"v_add_u32 v20, 0xd040, v20\n"
"v_add_u32 v21, 0xd040, v21\n"
"s_lshl_b32 s35, s27, 4\n"
"v_mfma_f32_16x16x32_bf16 v[208:211], a[4:7], a[100:103], v[208:211]\n"
"buffer_load_dwordx4 v23, s[40:43], s69 offen offset:1024 nt lds\n"
"s_add_u32 s35, s35, s26\n"
"s_lshl_b32 s8, s35, 18\n"
"v_mfma_f32_16x16x32_bf16 v[212:215], a[12:15], a[100:103], v[212:215]\n"
"ds_read_b64_tr_b16 a[152:153], v16 offset:0\n"
"s_add_u32 s10, s10, s8\n"
"s_addc_u32 s11, s11, 0\n"
"v_mfma_f32_16x16x32_bf16 v[212:215], a[8:11], a[96:99], v[212:215]\n"
"buffer_load_dwordx4 v22, s[40:43], s70 offen offset:2048 nt lds\n"
"s_add_u32 s12, s12, s8\n"
"s_addc_u32 s13, s13, 0\n"
"v_mfma_f32_16x16x32_bf16 v[180:183], a[8:11], a[88:91], v[180:183]\n"
"ds_read_b64_tr_b16 a[154:155], v16 offset:2048\n"
"s_mul_i32 s8, s35, 36864\n"
"v_mfma_f32_16x16x32_bf16 v[180:183], a[12:15], a[92:95], v[180:183]\n"
"buffer_load_dwordx4 v23, s[40:43], s71 offen offset:3072 nt lds\n"
"s_add_u32 s16, s16, s8\n"
"s_addc_u32 s17, s17, 0\n"
"v_mfma_f32_16x16x32_bf16 v[148:151], a[12:15], a[84:87], v[148:151]\n"
"ds_read_b64_tr_b16 a[156:157], v16 offset:16416\n"
"s_add_u32 m0, s81, 4104\n"
"s_mov_b64 s[44:45], s[10:11]\n"
"v_mfma_f32_16x16x32_bf16 v[148:151], a[8:11], a[80:83], v[148:151]\n"
"buffer_load_dwordx4 v22, s[40:43], s72 offen offset:0 nt lds\n"
"s_and_b32 s45, s45, 0xffff\n"
"s_mov_b32 s46, 262144\n"
"v_mfma_f32_16x16x32_bf16 v[116:119], a[8:11], a[72:75], v[116:119]\n"
"ds_read_b64_tr_b16 a[158:159], v16 offset:18464\n"
"s_mov_b32 s47, 0x20000\n"
"s_mov_b64 s[48:49], s[12:13]\n"
"v_mfma_f32_16x16x32_bf16 v[116:119], a[12:15], a[76:79], v[116:119]\n"
"buffer_load_dwordx4 v23, s[40:43], s73 offen offset:1024 nt lds\n"
"s_sub_u32 s48, s48, 1024\n"
"s_subb_u32 s49, s49, 0\n"
"v_mfma_f32_16x16x32_bf16 v[84:87], a[12:15], a[68:71], v[84:87]\n"
"ds_read_b128 a[216:219], v20 offset:0\n"
"s_and_b32 s49, s49, 0xffff\n"
"s_mov_b32 s50, 263168\n"
"v_mfma_f32_16x16x32_bf16 v[84:87], a[8:11], a[64:67], v[84:87]\n"
"buffer_load_dwordx4 v22, s[40:43], s74 offen offset:2048 nt lds\n"
"s_mov_b32 s51, 0x20000\n"
"s_and_b32 s8, %[wid], 1\n"
"v_mfma_f32_16x16x32_bf16 v[88:91], a[16:19], a[64:67], v[88:91]\n"
"ds_read_b128 a[220:223], v21 offset:0\n"
"s_lshr_b32 s9, %[wid], 1\n"
"s_mul_i32 s10, s34, 768\n"
"v_mfma_f32_16x16x32_bf16 v[88:91], a[20:23], a[68:71], v[88:91]\n"
"buffer_load_dwordx4 v23, s[40:43], s75 offen offset:3072 nt lds\n"
"s_add_u32 s10, s10, s35\n"
"s_mul_i32 s28, s10, 6144\n"
"v_mfma_f32_16x16x32_bf16 v[120:123], a[20:23], a[76:79], v[120:123]\n"
"ds_read_b128 a[224:227], v20 offset:2048\n"
"s_add_u32 m0, s82, 0\n"
"s_add_u32 s28, s28, 27262976\n"
"v_mfma_f32_16x16x32_bf16 v[120:123], a[16:19], a[72:75], v[120:123]\n"
"buffer_load_dwordx4 v24, s[36:39], s76 offen offset:0 lds\n"
"s_lshl_b32 s10, s34, 7\n"
"s_add_u32 s10, s10, s35\n"
"v_mfma_f32_16x16x32_bf16 v[152:155], a[16:19], a[80:83], v[152:155]\n"
"ds_read_b128 a[228:231], v21 offset:2048\n"
"s_lshl_b32 s10, s10, 2\n"
"s_add_u32 s10, s10, %[wid]\n"
"v_mfma_f32_16x16x32_bf16 v[152:155], a[20:23], a[84:87], v[152:155]\n"
"buffer_load_dwordx4 v25, s[36:39], s77 offen offset:1024 lds\n"
"s_lshl_b32 s10, s10, 2\n"
"s_add_u32 s30, s10, 8192\n"
"v_mfma_f32_16x16x32_bf16 v[184:187], a[20:23], a[92:95], v[184:187]\n"
"ds_read_b128 a[232:235], v20 offset:4096\n"
"s_lshl_b32 s10, s34, 3\n"
"s_add_u32 s10, s10, s27\n"
"v_mfma_f32_16x16x32_bf16 v[184:187], a[16:19], a[88:91], v[184:187]\n"
"buffer_load_dwordx4 v24, s[36:39], s78 offen offset:2048 lds\n"
"s_lshl_b32 s10, s10, 1\n"
"s_add_u32 s10, s10, s8\n"
"v_mfma_f32_16x16x32_bf16 v[216:219], a[16:19], a[96:99], v[216:219]\n"
"ds_read_b128 a[236:239], v21 offset:4096\n"
"s_lshl_b32 s11, s10, 2\n"
"s_lshr_b32 s12, s26, 2\n"
"v_mfma_f32_16x16x32_bf16 v[216:219], a[20:23], a[100:103], v[216:219]\n"
"buffer_load_dwordx4 v25, s[36:39], s79 offen offset:3072 lds\n"
"s_add_u32 s11, s11, s12\n"
"s_lshl_b32 s11, s11, 4\n"
"v_mfma_f32_16x16x32_bf16 v[220:223], a[28:31], a[100:103], v[220:223]\n"
"ds_read_b128 a[240:243], v20 offset:6144\n"
"s_add_u32 m0, s82, 4096\n"
"s_and_b32 s12, s26, 3\n"
"v_mfma_f32_16x16x32_bf16 v[220:223], a[24:27], a[96:99], v[220:223]\n"
"buffer_load_dwordx4 v24, s[36:39], s80 offen offset:0 lds\n"
"s_lshl_b32 s12, s12, 1\n"
"s_add_u32 s11, s11, s12\n"
"v_mfma_f32_16x16x32_bf16 v[188:191], a[24:27], a[88:91], v[188:191]\n"
"ds_read_b128 a[244:247], v21 offset:6144\n"
"s_add_u32 s40, s40, 524288\n"
"s_addc_u32 s41, s41, 0\n"
"s_sub_u32 s42, s42, 524288\n"
"v_mfma_f32_16x16x32_bf16 v[188:191], a[28:31], a[92:95], v[188:191]\n"
"ds_read_b128 a[248:251], v20 offset:8192\n"
"s_add_u32 s36, s36, 128\n"
"s_addc_u32 s37, s37, 0\n"
"s_sub_u32 s38, s38, 128\n"
"s_add_u32 m0, s81, 53312\n"
"v_mfma_f32_16x16x32_bf16 v[156:159], a[28:31], a[84:87], v[156:159]\n"
"buffer_load_dwordx4 v22, s[40:43], s68 offen offset:0 nt lds\n"
"s_add_u32 s11, s11, s9\n"
"s_lshl_b32 s15, s11, 2\n"
"v_mfma_f32_16x16x32_bf16 v[156:159], a[24:27], a[80:83], v[156:159]\n"
"ds_read_b128 a[252:255], v21 offset:8192\n"
"s_mul_i32 s10, s10, 160\n"
"s_lshl_b32 s11, s26, 1\n"
"v_mfma_f32_16x16x32_bf16 v[124:127], a[24:27], a[72:75], v[124:127]\n"
"ds_read_b64_tr_b16 a[160:161], v16 offset:8\n"
"s_add_u32 s10, s10, s11\n"
"s_add_u32 s10, s10, s9\n"
"v_mfma_f32_16x16x32_bf16 v[124:127], a[28:31], a[76:79], v[124:127]\n"
"buffer_load_dwordx4 v23, s[40:43], s69 offen offset:1024 nt lds\n"
"s_lshl_b32 s26, s10, 12\n"
"s_add_u32 s26, s26, 0\n"
"v_mfma_f32_16x16x32_bf16 v[92:95], a[28:31], a[68:71], v[92:95]\n"
"ds_read_b64_tr_b16 a[162:163], v16 offset:2056\n"
"s_cmp_lt_u32 s24, 30\n"
"s_cselect_b32 s8, s24, 0\n"
"v_mfma_f32_16x16x32_bf16 v[92:95], a[24:27], a[64:67], v[92:95]\n"
"ds_read_b64_tr_b16 a[164:165], v16 offset:16424\n"
"s_mul_i32 s9, s8, 43\n"
"s_lshr_b32 s9, s9, 8\n"
"v_mfma_f32_16x16x32_bf16 v[96:99], a[32:35], a[64:67], v[96:99]\n"
"ds_read_b64_tr_b16 a[166:167], v16 offset:18472\n"
"s_mul_i32 s10, s9, 6\n"
"s_sub_u32 s8, s8, s10\n"
"v_mfma_f32_16x16x32_bf16 v[96:99], a[36:39], a[68:71], v[96:99]\n"
"buffer_load_dwordx4 v22, s[40:43], s70 offen offset:2048 nt lds\n"
"s_bfe_u32 s10, %[bid], 0x10001\n"
"s_mul_i32 s10, s10, 5\n"
"v_mfma_f32_16x16x32_bf16 v[128:131], a[36:39], a[76:79], v[128:131]\n"
"ds_read_b64_tr_b16 a[168:169], v17 offset:0\n"
"s_add_u32 s9, s9, s10\n"
"s_lshl_b32 s9, s9, 3\n"
"v_mfma_f32_16x16x32_bf16 v[128:131], a[32:35], a[72:75], v[128:131]\n"
"ds_read_b64_tr_b16 a[170:171], v17 offset:2048\n"
"s_and_b32 s10, %[bid], 1\n"
"s_lshl_b32 s11, s10, 2\n"
"v_mfma_f32_16x16x32_bf16 v[160:163], a[32:35], a[80:83], v[160:163]\n"
"buffer_load_dwordx4 v23, s[40:43], s71 offen offset:3072 nt lds\n"
"s_add_u32 s11, s11, %[wid]\n"
"s_lshl_b32 s12, s11, 4\n"
"v_mfma_f32_16x16x32_bf16 v[160:163], a[36:39], a[84:87], v[160:163]\n"
"ds_read_b64_tr_b16 a[172:173], v17 offset:16416\n"
"s_mul_i32 s13, s34, 6\n"
"s_add_u32 s13, s13, s8\n"
"v_mfma_f32_16x16x32_bf16 v[192:195], a[36:39], a[92:95], v[192:195]\n"
"ds_read_b64_tr_b16 a[174:175], v17 offset:18464\n"
"s_lshl_b32 s13, s13, 7\n"
"s_add_u32 s13, s13, s12\n"
"v_mfma_f32_16x16x32_bf16 v[192:195], a[32:35], a[88:91], v[192:195]\n"
"ds_read_b64_tr_b16 a[176:177], v17 offset:8\n"
"s_add_u32 m0, s81, 57416\n"
"s_mul_i32 s29, s13, 6144\n"
"v_mfma_f32_16x16x32_bf16 v[224:227], a[32:35], a[96:99], v[224:227]\n"
"buffer_load_dwordx4 v22, s[40:43], s72 offen offset:0 nt lds\n"
"s_add_u32 s29, s29, 27262976\n"
"s_lshl_b32 s13, s34, 7\n"
"v_mfma_f32_16x16x32_bf16 v[224:227], a[36:39], a[100:103], v[224:227]\n"
"ds_read_b64_tr_b16 a[178:179], v17 offset:2056\n"
"s_add_u32 s13, s13, s12\n"
"s_lshl_b32 s13, s13, 4\n"
"v_mfma_f32_16x16x32_bf16 v[228:231], a[44:47], a[100:103], v[228:231]\n"
"ds_read_b64_tr_b16 a[180:181], v17 offset:16424\n"
"s_add_u32 s31, s13, 8192\n"
"s_mul_i32 s13, s34, 80\n"
"v_mfma_f32_16x16x32_bf16 v[228:231], a[40:43], a[96:99], v[228:231]\n"
"buffer_load_dwordx4 v23, s[40:43], s73 offen offset:1024 nt lds\n"
"s_add_u32 s13, s13, s9\n"
"s_lshl_b32 s13, s13, 7\n"
"v_mfma_f32_16x16x32_bf16 v[196:199], a[40:43], a[88:91], v[196:199]\n"
"ds_read_b64_tr_b16 a[182:183], v17 offset:18472\n"
"s_add_u32 s13, s13, s12\n"
"s_lshl_b32 s27, s13, 10\n"
"v_mfma_f32_16x16x32_bf16 v[196:199], a[44:47], a[92:95], v[196:199]\n"
"ds_read_b64_tr_b16 a[184:185], v18 offset:0\n"
"s_add_u32 s27, s27, 0\n"
"s_add_u32 s13, s9, s11\n"
"v_mfma_f32_16x16x32_bf16 v[164:167], a[44:47], a[84:87], v[164:167]\n"
"ds_read_b64_tr_b16 a[186:187], v18 offset:2048\n"
"s_mul_i32 s13, s13, 36864\n"
"s_mul_i32 s14, s34, 1152\n"
"v_mfma_f32_16x16x32_bf16 v[164:167], a[40:43], a[80:83], v[164:167]\n"
"buffer_load_dwordx4 v22, s[40:43], s74 offen offset:2048 nt lds\n"
"s_add_u32 s13, s13, s14\n"
"s_mul_i32 s14, s8, 192\n"
"v_mfma_f32_16x16x32_bf16 v[132:135], a[40:43], a[72:75], v[132:135]\n"
"ds_read_b64_tr_b16 a[188:189], v18 offset:16416\n"
"s_add_u32 s13, s13, s14\n"
"v_mfma_f32_16x16x32_bf16 v[132:135], a[44:47], a[76:79], v[132:135]\n"
"ds_read_b64_tr_b16 a[190:191], v18 offset:18464\n"
"s_add_u32 s20, s20, s13\n"
"s_addc_u32 s21, s21, 0\n"
"v_mfma_f32_16x16x32_bf16 v[100:103], a[44:47], a[68:71], v[100:103]\n"
"buffer_load_dwordx4 v23, s[40:43], s75 offen offset:3072 nt lds\n"
"s_mul_i32 s13, s9, 205\n"
"s_lshr_b32 s13, s13, 10\n"
"v_mfma_f32_16x16x32_bf16 v[100:103], a[40:43], a[64:67], v[100:103]\n"
"ds_read_b64_tr_b16 a[192:193], v18 offset:8\n"
"s_add_u32 s14, s9, 7\n"
"s_mul_i32 s14, s14, 205\n"
"v_mfma_f32_16x16x32_bf16 v[104:107], a[48:51], a[64:67], v[104:107]\n"
"ds_read_b64_tr_b16 a[194:195], v18 offset:2056\n"
"s_lshr_b32 s14, s14, 10\n"
"s_sub_u32 s18, s14, s13\n"
"v_mfma_f32_16x16x32_bf16 v[104:107], a[52:55], a[68:71], v[104:107]\n"
"ds_read_b64_tr_b16 a[196:197], v18 offset:16424\n"
"s_add_u32 m0, s82, 53312\n"
"s_lshl_b32 s14, s34, 4\n"
"v_mfma_f32_16x16x32_bf16 v[136:139], a[52:55], a[76:79], v[136:139]\n"
"buffer_load_dwordx4 v24, s[36:39], s76 offen offset:0 lds\n"
"s_add_u32 s13, s13, s14\n"
"s_lshl_b32 s13, s13, 3\n"
"v_mfma_f32_16x16x32_bf16 v[136:139], a[48:51], a[72:75], v[136:139]\n"
"ds_read_b64_tr_b16 a[198:199], v18 offset:18472\n"
"s_add_u32 s13, s13, s11\n"
"s_lshr_b32 s14, s13, 1\n"
"v_mfma_f32_16x16x32_bf16 v[168:171], a[48:51], a[80:83], v[168:171]\n"
"ds_read_b64_tr_b16 a[200:201], v19 offset:0\n"
"s_lshl_b32 s14, s14, 6\n"
"s_and_b32 s13, s13, 1\n"
"v_mfma_f32_16x16x32_bf16 v[168:171], a[52:55], a[84:87], v[168:171]\n"
"buffer_load_dwordx4 v25, s[36:39], s77 offen offset:1024 lds\n"
"s_lshl_b32 s13, s13, 4\n"
"s_add_u32 s35, s14, s13\n"
"v_mfma_f32_16x16x32_bf16 v[200:203], a[52:55], a[92:95], v[200:203]\n"
"ds_read_b64_tr_b16 a[202:203], v19 offset:2048\n"
"s_mov_b32 s34, s15\n"
"s_mov_b64 s[52:53], s[22:23]\n"
"v_mfma_f32_16x16x32_bf16 v[200:203], a[48:51], a[88:91], v[200:203]\n"
"ds_read_b64_tr_b16 a[204:205], v19 offset:16416\n"
"s_add_u32 s52, s52, s26\n"
"s_addc_u32 s53, s53, 0\n"
"v_mfma_f32_16x16x32_bf16 v[232:235], a[48:51], a[96:99], v[232:235]\n"
"ds_read_b64_tr_b16 a[206:207], v19 offset:18464\n"
"s_and_b32 s53, s53, 0xffff\n"
"s_mov_b32 s54, 528384\n"
"v_mfma_f32_16x16x32_bf16 v[232:235], a[52:55], a[100:103], v[232:235]\n"
"buffer_load_dwordx4 v24, s[36:39], s78 offen offset:2048 lds\n"
"s_mov_b32 s55, 0x20000\n"
"s_lshl_b32 s83, %[wid], 12\n"
"v_mfma_f32_16x16x32_bf16 v[236:239], a[60:63], a[100:103], v[236:239]\n"
"ds_read_b64_tr_b16 a[208:209], v19 offset:8\n"
"s_add_u32 s84, s83, 65536\n"
"s_mov_b32 s85, s83\n"
"v_mfma_f32_16x16x32_bf16 v[236:239], a[56:59], a[96:99], v[236:239]\n"
"ds_read_b64_tr_b16 a[210:211], v19 offset:2056\n"
"s_add_u32 s86, s85, 1024\n"
"s_add_u32 s87, s85, 128\n"
"v_mfma_f32_16x16x32_bf16 v[204:207], a[56:59], a[88:91], v[204:207]\n"
"buffer_load_dwordx4 v25, s[36:39], s79 offen offset:3072 lds\n"
"v_lshlrev_b32 v30, 8, v29\n"
"v_mfma_f32_16x16x32_bf16 v[204:207], a[60:63], a[92:95], v[204:207]\n"
"ds_read_b64_tr_b16 a[212:213], v19 offset:16424\n"
"v_xor_b32 v31, v26, v29\n"
"v_mfma_f32_16x16x32_bf16 v[172:175], a[60:63], a[84:87], v[172:175]\n"
"ds_read_b64_tr_b16 a[214:215], v19 offset:18472\n"
"v_xor_b32 v35, 0, v31\n"
"v_mfma_f32_16x16x32_bf16 v[172:175], a[56:59], a[80:83], v[172:175]\n"
"s_add_u32 m0, s82, 57408\n"
"v_lshl_add_u32 v1, v35, 4, v30\n"
"v_mfma_f32_16x16x32_bf16 v[140:143], a[56:59], a[72:75], v[140:143]\n"
"buffer_load_dwordx4 v24, s[36:39], s80 offen offset:0 lds\n"
"v_xor_b32 v35, 4, v31\n"
"v_mfma_f32_16x16x32_bf16 v[140:143], a[60:63], a[76:79], v[140:143]\n"
"s_add_u32 s40, s40, 524288\n"
"s_addc_u32 s41, s41, 0\n"
"s_sub_u32 s42, s42, 524288\n"
"v_mfma_f32_16x16x32_bf16 v[108:111], a[60:63], a[68:71], v[108:111]\n"
"s_add_u32 s36, s36, 128\n"
"s_addc_u32 s37, s37, 0\n"
"s_sub_u32 s38, s38, 128\n"
"v_mfma_f32_16x16x32_bf16 v[108:111], a[56:59], a[64:67], v[108:111]\n"
"buffer_load_dword v14, v7, s[64:67], 0 offen sc1\n"
"s_waitcnt lgkmcnt(0)\n"
"v_mfma_f32_16x16x32_bf16 v[80:83], a[152:155], a[216:219], v[80:83]\n"
"s_waitcnt vmcnt(14)\n"
"s_barrier\n"
"v_add_u32 v16, 0xfffe5f80, v16\n"
"v_add_u32 v17, 0xfffe5f80, v17\n"
"v_mfma_f32_16x16x32_bf16 v[80:83], a[156:159], a[220:223], v[80:83]\n"
"s_add_u32 m0, s81, 106624\n"
"v_add_u32 v18, 0xfffe5f80, v18\n"
"v_add_u32 v19, 0xfffe5f80, v19\n"
"v_mfma_f32_16x16x32_bf16 v[112:115], a[156:159], a[228:231], v[112:115]\n"
"buffer_load_dwordx4 v22, s[40:43], s68 offen offset:0 nt lds\n"
"v_mfma_f32_16x16x32_bf16 v[112:115], a[152:155], a[224:227], v[112:115]\n"
"v_add_u32 v20, 0xfffe5f80, v20\n"
"v_add_u32 v21, 0xfffe5f80, v21\n"
"v_mfma_f32_16x16x32_bf16 v[144:147], a[152:155], a[232:235], v[144:147]\n"
"ds_read_b64_tr_b16 a[0:1], v16 offset:0\n"
"v_lshl_add_u32 v2, v35, 4, v30\n"
"v_mfma_f32_16x16x32_bf16 v[144:147], a[156:159], a[236:239], v[144:147]\n"
"ds_read_b64_tr_b16 a[2:3], v16 offset:2048\n"
"v_xor_b32 v35, 8, v31\n"
"v_mfma_f32_16x16x32_bf16 v[176:179], a[156:159], a[244:247], v[176:179]\n"
"ds_read_b64_tr_b16 a[4:5], v16 offset:16416\n"
"v_lshl_add_u32 v3, v35, 4, v30\n"
"v_mfma_f32_16x16x32_bf16 v[176:179], a[152:155], a[240:243], v[176:179]\n"
"ds_read_b64_tr_b16 a[6:7], v16 offset:18464\n"
"v_xor_b32 v35, 12, v31\n"
"v_mfma_f32_16x16x32_bf16 v[208:211], a[152:155], a[248:251], v[208:211]\n"
"buffer_load_dwordx4 v23, s[40:43], s69 offen offset:1024 nt lds\n"
"v_lshl_add_u32 v4, v35, 4, v30\n"
"v_mfma_f32_16x16x32_bf16 v[208:211], a[156:159], a[252:255], v[208:211]\n"
"ds_read_b128 a[64:67], v20 offset:0\n"
"v_lshrrev_b32 v30, 1, v26\n"
"v_mfma_f32_16x16x32_bf16 v[212:215], a[164:167], a[252:255], v[212:215]\n"
"ds_read_b128 a[68:71], v21 offset:0\n"
"v_lshlrev_b32 v30, 11, v30\n"
"v_mfma_f32_16x16x32_bf16 v[212:215], a[160:163], a[248:251], v[212:215]\n"
"ds_read_b128 a[72:75], v20 offset:2048\n"
"v_and_b32 v31, 1, v26\n"
"v_mfma_f32_16x16x32_bf16 v[180:183], a[160:163], a[240:243], v[180:183]\n"
"ds_read_b128 a[76:79], v21 offset:2048\n"
"v_lshl_add_u32 v30, v31, 8, v30\n"
"v_mfma_f32_16x16x32_bf16 v[180:183], a[164:167], a[244:247], v[180:183]\n"
"ds_read_b128 a[80:83], v20 offset:4096\n"
"v_lshl_add_u32 v30, v27, 6, v30\n"
"v_mfma_f32_16x16x32_bf16 v[148:151], a[164:167], a[236:239], v[148:151]\n"
"buffer_load_dwordx4 v22, s[40:43], s70 offen offset:2048 nt lds\n"
"v_lshl_add_u32 v30, v28, 3, v30\n"
"v_mfma_f32_16x16x32_bf16 v[148:151], a[160:163], a[232:235], v[148:151]\n"
"ds_read_b128 a[84:87], v21 offset:4096\n"
"v_lshl_add_u32 v30, v31, 5, v30\n"
"v_mfma_f32_16x16x32_bf16 v[116:119], a[160:163], a[224:227], v[116:119]\n"
"ds_read_b128 a[88:91], v20 offset:6144\n"
"v_add_u32 v5, 0x10000, v30\n"
"v_mfma_f32_16x16x32_bf16 v[116:119], a[164:167], a[228:231], v[116:119]\n"
"ds_read_b128 a[92:95], v21 offset:6144\n"
"v_xor_b32 v6, 32, v5\n"
"v_mfma_f32_16x16x32_bf16 v[84:87], a[164:167], a[220:223], v[84:87]\n"
"ds_read_b128 a[96:99], v20 offset:8192\n"
"v_lshlrev_b32 v30, 8, v26\n"
"v_mfma_f32_16x16x32_bf16 v[84:87], a[160:163], a[216:219], v[84:87]\n"
"ds_read_b128 a[100:103], v21 offset:8192\n"
"v_xor_b32 v31, v26, v29\n"
"v_mfma_f32_16x16x32_bf16 v[88:91], a[168:171], a[216:219], v[88:91]\n"
"buffer_load_dwordx4 v23, s[40:43], s71 offen offset:3072 nt lds\n"
"v_lshl_add_u32 v240, v31, 4, v30\n"
"v_mfma_f32_16x16x32_bf16 v[88:91], a[172:175], a[220:223], v[88:91]\n"
"ds_read_b64_tr_b16 a[8:9], v16 offset:8\n"
"v_xor_b32 v241, 64, v240\n"
"v_mfma_f32_16x16x32_bf16 v[120:123], a[172:175], a[228:231], v[120:123]\n"
"ds_read_b64_tr_b16 a[10:11], v16 offset:2056\n"
"v_xor_b32 v242, 128, v240\n"
"v_mfma_f32_16x16x32_bf16 v[120:123], a[168:171], a[224:227], v[120:123]\n"
"ds_read_b64_tr_b16 a[12:13], v16 offset:16424\n"
"v_xor_b32 v243, 192, v240\n"
"v_mfma_f32_16x16x32_bf16 v[152:155], a[168:171], a[232:235], v[152:155]\n"
"ds_read_b64_tr_b16 a[14:15], v16 offset:18472\n"
"v_bfe_u32 v30, v0, 2, 3\n"
"v_mfma_f32_16x16x32_bf16 v[152:155], a[172:175], a[236:239], v[152:155]\n"
"ds_read_b64_tr_b16 a[16:17], v17 offset:0\n"
"s_add_u32 m0, s81, 110728\n"
"v_mfma_f32_16x16x32_bf16 v[184:187], a[172:175], a[244:247], v[184:187]\n"
"buffer_load_dwordx4 v22, s[40:43], s72 offen offset:0 nt lds\n"
"v_lshrrev_b32 v31, 5, v0\n"
"v_mfma_f32_16x16x32_bf16 v[184:187], a[168:171], a[240:243], v[184:187]\n"
"ds_read_b64_tr_b16 a[18:19], v17 offset:2048\n"
"v_lshlrev_b32 v31, 6, v31\n"
"v_mfma_f32_16x16x32_bf16 v[216:219], a[168:171], a[248:251], v[216:219]\n"
"ds_read_b64_tr_b16 a[20:21], v17 offset:16416\n"
"v_lshl_add_u32 v31, v30, 8, v31\n"
"v_mfma_f32_16x16x32_bf16 v[216:219], a[172:175], a[252:255], v[216:219]\n"
"ds_read_b64_tr_b16 a[22:23], v17 offset:18464\n"
"v_lshrrev_b32 v30, 2, v30\n"
"v_mfma_f32_16x16x32_bf16 v[220:223], a[180:183], a[252:255], v[220:223]\n"
"ds_read_b64_tr_b16 a[24:25], v17 offset:8\n"
"v_lshlrev_b32 v30, 1, v30\n"
"v_mfma_f32_16x16x32_bf16 v[220:223], a[176:179], a[248:251], v[220:223]\n"
"ds_read_b64_tr_b16 a[26:27], v17 offset:2056\n"
"v_xor_b32 v30, v30, v28\n"
"v_mfma_f32_16x16x32_bf16 v[188:191], a[176:179], a[240:243], v[188:191]\n"
"buffer_load_dwordx4 v23, s[40:43], s73 offen offset:1024 nt lds\n"
"v_lshl_add_u32 v8, v30, 4, v31\n"
"v_mfma_f32_16x16x32_bf16 v[188:191], a[180:183], a[244:247], v[188:191]\n"
"ds_read_b64_tr_b16 a[28:29], v17 offset:16424\n"
"v_lshlrev_b32 v9, 4, v0\n"
"v_mfma_f32_16x16x32_bf16 v[156:159], a[180:183], a[236:239], v[156:159]\n"
"ds_read_b64_tr_b16 a[30:31], v17 offset:18472\n"
"v_mov_b32 v10, 0x3e0293ee\n"
"v_mfma_f32_16x16x32_bf16 v[156:159], a[176:179], a[232:235], v[156:159]\n"
"ds_read_b64_tr_b16 a[32:33], v18 offset:0\n"
"v_mov_b32 v11, 0x3e0293ee\n"
"v_mfma_f32_16x16x32_bf16 v[124:127], a[176:179], a[224:227], v[124:127]\n"
"ds_read_b64_tr_b16 a[34:35], v18 offset:2048\n"
"v_mfma_f32_16x16x32_bf16 v[124:127], a[180:183], a[228:231], v[124:127]\n"
"ds_read_b64_tr_b16 a[36:37], v18 offset:16416\n"
"v_mfma_f32_16x16x32_bf16 v[92:95], a[180:183], a[220:223], v[92:95]\n"
"buffer_load_dwordx4 v22, s[40:43], s74 offen offset:2048 nt lds\n"
"v_mfma_f32_16x16x32_bf16 v[92:95], a[176:179], a[216:219], v[92:95]\n"
"ds_read_b64_tr_b16 a[38:39], v18 offset:18464\n"
"v_mfma_f32_16x16x32_bf16 v[96:99], a[184:187], a[216:219], v[96:99]\n"
"ds_read_b64_tr_b16 a[40:41], v18 offset:8\n"
"v_mfma_f32_16x16x32_bf16 v[96:99], a[188:191], a[220:223], v[96:99]\n"
"ds_read_b64_tr_b16 a[42:43], v18 offset:2056\n"
"v_mfma_f32_16x16x32_bf16 v[128:131], a[188:191], a[228:231], v[128:131]\n"
"ds_read_b64_tr_b16 a[44:45], v18 offset:16424\n"
"v_mfma_f32_16x16x32_bf16 v[128:131], a[184:187], a[224:227], v[128:131]\n"
"ds_read_b64_tr_b16 a[46:47], v18 offset:18472\n"
"v_mfma_f32_16x16x32_bf16 v[160:163], a[184:187], a[232:235], v[160:163]\n"
"buffer_load_dwordx4 v23, s[40:43], s75 offen offset:3072 nt lds\n"
"v_mfma_f32_16x16x32_bf16 v[160:163], a[188:191], a[236:239], v[160:163]\n"
"ds_read_b64_tr_b16 a[48:49], v19 offset:0\n"
"v_mfma_f32_16x16x32_bf16 v[192:195], a[188:191], a[244:247], v[192:195]\n"
"ds_read_b64_tr_b16 a[50:51], v19 offset:2048\n"
"v_mfma_f32_16x16x32_bf16 v[192:195], a[184:187], a[240:243], v[192:195]\n"
"ds_read_b64_tr_b16 a[52:53], v19 offset:16416\n"
"v_mfma_f32_16x16x32_bf16 v[224:227], a[184:187], a[248:251], v[224:227]\n"
"ds_read_b64_tr_b16 a[54:55], v19 offset:18464\n"
"v_mfma_f32_16x16x32_bf16 v[224:227], a[188:191], a[252:255], v[224:227]\n"
"ds_read_b64_tr_b16 a[56:57], v19 offset:8\n"
"s_add_u32 m0, s82, 106624\n"
"v_mfma_f32_16x16x32_bf16 v[228:231], a[196:199], a[252:255], v[228:231]\n"
"buffer_load_dwordx4 v24, s[36:39], s76 offen offset:0 lds\n"
"v_mfma_f32_16x16x32_bf16 v[228:231], a[192:195], a[248:251], v[228:231]\n"
"ds_read_b64_tr_b16 a[58:59], v19 offset:2056\n"
"v_mfma_f32_16x16x32_bf16 v[196:199], a[192:195], a[240:243], v[196:199]\n"
"ds_read_b64_tr_b16 a[60:61], v19 offset:16424\n"
"v_mfma_f32_16x16x32_bf16 v[196:199], a[196:199], a[244:247], v[196:199]\n"
"ds_read_b64_tr_b16 a[62:63], v19 offset:18472\n"
"v_mfma_f32_16x16x32_bf16 v[164:167], a[196:199], a[236:239], v[164:167]\n"
"v_mfma_f32_16x16x32_bf16 v[164:167], a[192:195], a[232:235], v[164:167]\n"
"v_mfma_f32_16x16x32_bf16 v[132:135], a[192:195], a[224:227], v[132:135]\n"
"buffer_load_dwordx4 v25, s[36:39], s77 offen offset:1024 lds\n"
"v_mfma_f32_16x16x32_bf16 v[132:135], a[196:199], a[228:231], v[132:135]\n"
"v_mfma_f32_16x16x32_bf16 v[100:103], a[196:199], a[220:223], v[100:103]\n"
"v_mfma_f32_16x16x32_bf16 v[100:103], a[192:195], a[216:219], v[100:103]\n"
"v_mfma_f32_16x16x32_bf16 v[104:107], a[200:203], a[216:219], v[104:107]\n"
"v_mfma_f32_16x16x32_bf16 v[104:107], a[204:207], a[220:223], v[104:107]\n"
"v_mfma_f32_16x16x32_bf16 v[136:139], a[204:207], a[228:231], v[136:139]\n"
"buffer_load_dwordx4 v24, s[36:39], s78 offen offset:2048 lds\n"
"v_mfma_f32_16x16x32_bf16 v[136:139], a[200:203], a[224:227], v[136:139]\n"
"v_mfma_f32_16x16x32_bf16 v[168:171], a[200:203], a[232:235], v[168:171]\n"
"v_mfma_f32_16x16x32_bf16 v[168:171], a[204:207], a[236:239], v[168:171]\n"
"v_mfma_f32_16x16x32_bf16 v[200:203], a[204:207], a[244:247], v[200:203]\n"
"v_mfma_f32_16x16x32_bf16 v[200:203], a[200:203], a[240:243], v[200:203]\n"
"v_mfma_f32_16x16x32_bf16 v[232:235], a[200:203], a[248:251], v[232:235]\n"
"buffer_load_dwordx4 v25, s[36:39], s79 offen offset:3072 lds\n"
"v_mfma_f32_16x16x32_bf16 v[232:235], a[204:207], a[252:255], v[232:235]\n"
"v_mfma_f32_16x16x32_bf16 v[236:239], a[212:215], a[252:255], v[236:239]\n"
"v_mfma_f32_16x16x32_bf16 v[236:239], a[208:211], a[248:251], v[236:239]\n"
"v_mfma_f32_16x16x32_bf16 v[204:207], a[208:211], a[240:243], v[204:207]\n"
"v_mfma_f32_16x16x32_bf16 v[204:207], a[212:215], a[244:247], v[204:207]\n"
"s_add_u32 m0, s82, 110720\n"
"v_mfma_f32_16x16x32_bf16 v[172:175], a[212:215], a[236:239], v[172:175]\n"
"buffer_load_dwordx4 v24, s[36:39], s80 offen offset:0 lds\n"
"v_mfma_f32_16x16x32_bf16 v[172:175], a[208:211], a[232:235], v[172:175]\n"
"s_add_u32 s40, s40, 524288\n"
"s_addc_u32 s41, s41, 0\n"
"s_sub_u32 s42, s42, 524288\n"
"v_mfma_f32_16x16x32_bf16 v[140:143], a[208:211], a[224:227], v[140:143]\n"
"s_add_u32 s36, s36, 128\n"
"s_addc_u32 s37, s37, 0\n"
"s_sub_u32 s38, s38, 128\n"
"v_mfma_f32_16x16x32_bf16 v[140:143], a[212:215], a[228:231], v[140:143]\n"
"v_mfma_f32_16x16x32_bf16 v[108:111], a[212:215], a[220:223], v[108:111]\n"
"v_mfma_f32_16x16x32_bf16 v[108:111], a[208:211], a[216:219], v[108:111]\n"
"s_waitcnt lgkmcnt(0)\n"
"v_mfma_f32_16x16x32_bf16 v[80:83], a[0:3], a[64:67], v[80:83]\n"
"s_waitcnt vmcnt(14)\n"
"s_barrier\n"
"v_add_u32 v16, 0xd040, v16\n"
"v_add_u32 v17, 0xd040, v17\n"
"v_mfma_f32_16x16x32_bf16 v[80:83], a[4:7], a[68:71], v[80:83]\n"
"s_add_u32 m0, s81, 0\n"
"v_add_u32 v18, 0xd040, v18\n"
"v_add_u32 v19, 0xd040, v19\n"
"v_mfma_f32_16x16x32_bf16 v[112:115], a[4:7], a[76:79], v[112:115]\n"
"buffer_load_dwordx4 v22, s[40:43], s68 offen offset:0 nt lds\n"
"v_mfma_f32_16x16x32_bf16 v[112:115], a[0:3], a[72:75], v[112:115]\n"
"v_add_u32 v20, 0xd040, v20\n"
"v_add_u32 v21, 0xd040, v21\n"
"v_mfma_f32_16x16x32_bf16 v[144:147], a[0:3], a[80:83], v[144:147]\n"
"ds_read_b64_tr_b16 a[152:153], v16 offset:0\n"
"v_mfma_f32_16x16x32_bf16 v[144:147], a[4:7], a[84:87], v[144:147]\n"
"ds_read_b64_tr_b16 a[154:155], v16 offset:2048\n"
"v_mfma_f32_16x16x32_bf16 v[176:179], a[4:7], a[92:95], v[176:179]\n"
"ds_read_b64_tr_b16 a[156:157], v16 offset:16416\n"
"v_mfma_f32_16x16x32_bf16 v[176:179], a[0:3], a[88:91], v[176:179]\n"
"ds_read_b64_tr_b16 a[158:159], v16 offset:18464\n"
"v_mfma_f32_16x16x32_bf16 v[208:211], a[0:3], a[96:99], v[208:211]\n"
"buffer_load_dwordx4 v23, s[40:43], s69 offen offset:1024 nt lds\n"
"v_mfma_f32_16x16x32_bf16 v[208:211], a[4:7], a[100:103], v[208:211]\n"
"ds_read_b128 a[216:219], v20 offset:0\n"
"v_mfma_f32_16x16x32_bf16 v[212:215], a[12:15], a[100:103], v[212:215]\n"
"ds_read_b128 a[220:223], v21 offset:0\n"
"v_mfma_f32_16x16x32_bf16 v[212:215], a[8:11], a[96:99], v[212:215]\n"
"ds_read_b128 a[224:227], v20 offset:2048\n"
"v_mfma_f32_16x16x32_bf16 v[180:183], a[8:11], a[88:91], v[180:183]\n"
"ds_read_b128 a[228:231], v21 offset:2048\n"
"v_mfma_f32_16x16x32_bf16 v[180:183], a[12:15], a[92:95], v[180:183]\n"
"ds_read_b128 a[232:235], v20 offset:4096\n"
"v_mfma_f32_16x16x32_bf16 v[148:151], a[12:15], a[84:87], v[148:151]\n"
"buffer_load_dwordx4 v22, s[40:43], s70 offen offset:2048 nt lds\n"
"v_mfma_f32_16x16x32_bf16 v[148:151], a[8:11], a[80:83], v[148:151]\n"
"ds_read_b128 a[236:239], v21 offset:4096\n"
"v_mfma_f32_16x16x32_bf16 v[116:119], a[8:11], a[72:75], v[116:119]\n"
"ds_read_b128 a[240:243], v20 offset:6144\n"
"v_mfma_f32_16x16x32_bf16 v[116:119], a[12:15], a[76:79], v[116:119]\n"
"ds_read_b128 a[244:247], v21 offset:6144\n"
"v_mfma_f32_16x16x32_bf16 v[84:87], a[12:15], a[68:71], v[84:87]\n"
"ds_read_b128 a[248:251], v20 offset:8192\n"
"v_mfma_f32_16x16x32_bf16 v[84:87], a[8:11], a[64:67], v[84:87]\n"
"ds_read_b128 a[252:255], v21 offset:8192\n"
"v_mfma_f32_16x16x32_bf16 v[88:91], a[16:19], a[64:67], v[88:91]\n"
"buffer_load_dwordx4 v23, s[40:43], s71 offen offset:3072 nt lds\n"
"v_mfma_f32_16x16x32_bf16 v[88:91], a[20:23], a[68:71], v[88:91]\n"
"ds_read_b64_tr_b16 a[160:161], v16 offset:8\n"
"v_mfma_f32_16x16x32_bf16 v[120:123], a[20:23], a[76:79], v[120:123]\n"
"ds_read_b64_tr_b16 a[162:163], v16 offset:2056\n"
"v_mfma_f32_16x16x32_bf16 v[120:123], a[16:19], a[72:75], v[120:123]\n"
"ds_read_b64_tr_b16 a[164:165], v16 offset:16424\n"
"v_mfma_f32_16x16x32_bf16 v[152:155], a[16:19], a[80:83], v[152:155]\n"
"ds_read_b64_tr_b16 a[166:167], v16 offset:18472\n"
"v_mfma_f32_16x16x32_bf16 v[152:155], a[20:23], a[84:87], v[152:155]\n"
"ds_read_b64_tr_b16 a[168:169], v17 offset:0\n"
"s_add_u32 m0, s81, 4104\n"
"v_mfma_f32_16x16x32_bf16 v[184:187], a[20:23], a[92:95], v[184:187]\n"
"buffer_load_dwordx4 v22, s[40:43], s72 offen offset:0 nt lds\n"
"v_mfma_f32_16x16x32_bf16 v[184:187], a[16:19], a[88:91], v[184:187]\n"
"ds_read_b64_tr_b16 a[170:171], v17 offset:2048\n"
"v_mfma_f32_16x16x32_bf16 v[216:219], a[16:19], a[96:99], v[216:219]\n"
"ds_read_b64_tr_b16 a[172:173], v17 offset:16416\n"
"v_mfma_f32_16x16x32_bf16 v[216:219], a[20:23], a[100:103], v[216:219]\n"
"ds_read_b64_tr_b16 a[174:175], v17 offset:18464\n"
"v_mfma_f32_16x16x32_bf16 v[220:223], a[28:31], a[100:103], v[220:223]\n"
"ds_read_b64_tr_b16 a[176:177], v17 offset:8\n"
"v_mfma_f32_16x16x32_bf16 v[220:223], a[24:27], a[96:99], v[220:223]\n"
"ds_read_b64_tr_b16 a[178:179], v17 offset:2056\n"
"v_mfma_f32_16x16x32_bf16 v[188:191], a[24:27], a[88:91], v[188:191]\n"
"buffer_load_dwordx4 v23, s[40:43], s73 offen offset:1024 nt lds\n"
"v_mfma_f32_16x16x32_bf16 v[188:191], a[28:31], a[92:95], v[188:191]\n"
"ds_read_b64_tr_b16 a[180:181], v17 offset:16424\n"
"v_mfma_f32_16x16x32_bf16 v[156:159], a[28:31], a[84:87], v[156:159]\n"
"ds_read_b64_tr_b16 a[182:183], v17 offset:18472\n"
"v_mfma_f32_16x16x32_bf16 v[156:159], a[24:27], a[80:83], v[156:159]\n"
"ds_read_b64_tr_b16 a[184:185], v18 offset:0\n"
"v_mfma_f32_16x16x32_bf16 v[124:127], a[24:27], a[72:75], v[124:127]\n"
"ds_read_b64_tr_b16 a[186:187], v18 offset:2048\n"
"v_mfma_f32_16x16x32_bf16 v[124:127], a[28:31], a[76:79], v[124:127]\n"
"ds_read_b64_tr_b16 a[188:189], v18 offset:16416\n"
"v_mfma_f32_16x16x32_bf16 v[92:95], a[28:31], a[68:71], v[92:95]\n"
"buffer_load_dwordx4 v22, s[40:43], s74 offen offset:2048 nt lds\n"
"v_mfma_f32_16x16x32_bf16 v[92:95], a[24:27], a[64:67], v[92:95]\n"
"ds_read_b64_tr_b16 a[190:191], v18 offset:18464\n"
"v_mfma_f32_16x16x32_bf16 v[96:99], a[32:35], a[64:67], v[96:99]\n"
"ds_read_b64_tr_b16 a[192:193], v18 offset:8\n"
"v_mfma_f32_16x16x32_bf16 v[96:99], a[36:39], a[68:71], v[96:99]\n"
"ds_read_b64_tr_b16 a[194:195], v18 offset:2056\n"
"v_mfma_f32_16x16x32_bf16 v[128:131], a[36:39], a[76:79], v[128:131]\n"
"ds_read_b64_tr_b16 a[196:197], v18 offset:16424\n"
"v_mfma_f32_16x16x32_bf16 v[128:131], a[32:35], a[72:75], v[128:131]\n"
"ds_read_b64_tr_b16 a[198:199], v18 offset:18472\n"
"v_mfma_f32_16x16x32_bf16 v[160:163], a[32:35], a[80:83], v[160:163]\n"
"buffer_load_dwordx4 v23, s[40:43], s75 offen offset:3072 nt lds\n"
"v_mfma_f32_16x16x32_bf16 v[160:163], a[36:39], a[84:87], v[160:163]\n"
"ds_read_b64_tr_b16 a[200:201], v19 offset:0\n"
"v_mfma_f32_16x16x32_bf16 v[192:195], a[36:39], a[92:95], v[192:195]\n"
"ds_read_b64_tr_b16 a[202:203], v19 offset:2048\n"
"v_mfma_f32_16x16x32_bf16 v[192:195], a[32:35], a[88:91], v[192:195]\n"
"ds_read_b64_tr_b16 a[204:205], v19 offset:16416\n"
"v_mfma_f32_16x16x32_bf16 v[224:227], a[32:35], a[96:99], v[224:227]\n"
"ds_read_b64_tr_b16 a[206:207], v19 offset:18464\n"
"v_mfma_f32_16x16x32_bf16 v[224:227], a[36:39], a[100:103], v[224:227]\n"
"ds_read_b64_tr_b16 a[208:209], v19 offset:8\n"
"s_add_u32 m0, s82, 0\n"
"v_mfma_f32_16x16x32_bf16 v[228:231], a[44:47], a[100:103], v[228:231]\n"
"buffer_load_dwordx4 v24, s[36:39], s76 offen offset:0 lds\n"
"v_mfma_f32_16x16x32_bf16 v[228:231], a[40:43], a[96:99], v[228:231]\n"
"ds_read_b64_tr_b16 a[210:211], v19 offset:2056\n"
"v_mfma_f32_16x16x32_bf16 v[196:199], a[40:43], a[88:91], v[196:199]\n"
"ds_read_b64_tr_b16 a[212:213], v19 offset:16424\n"
"v_mfma_f32_16x16x32_bf16 v[196:199], a[44:47], a[92:95], v[196:199]\n"
"ds_read_b64_tr_b16 a[214:215], v19 offset:18472\n"
"v_mfma_f32_16x16x32_bf16 v[164:167], a[44:47], a[84:87], v[164:167]\n"
"v_mfma_f32_16x16x32_bf16 v[164:167], a[40:43], a[80:83], v[164:167]\n"
"v_mfma_f32_16x16x32_bf16 v[132:135], a[40:43], a[72:75], v[132:135]\n"
"buffer_load_dwordx4 v25, s[36:39], s77 offen offset:1024 lds\n"
"v_mfma_f32_16x16x32_bf16 v[132:135], a[44:47], a[76:79], v[132:135]\n"
"v_mfma_f32_16x16x32_bf16 v[100:103], a[44:47], a[68:71], v[100:103]\n"
"v_mfma_f32_16x16x32_bf16 v[100:103], a[40:43], a[64:67], v[100:103]\n"
"v_mfma_f32_16x16x32_bf16 v[104:107], a[48:51], a[64:67], v[104:107]\n"
"v_mfma_f32_16x16x32_bf16 v[104:107], a[52:55], a[68:71], v[104:107]\n"
"v_mfma_f32_16x16x32_bf16 v[136:139], a[52:55], a[76:79], v[136:139]\n"
"buffer_load_dwordx4 v24, s[36:39], s78 offen offset:2048 lds\n"
"v_mfma_f32_16x16x32_bf16 v[136:139], a[48:51], a[72:75], v[136:139]\n"
"v_mfma_f32_16x16x32_bf16 v[168:171], a[48:51], a[80:83], v[168:171]\n"
"v_mfma_f32_16x16x32_bf16 v[168:171], a[52:55], a[84:87], v[168:171]\n"
"v_mfma_f32_16x16x32_bf16 v[200:203], a[52:55], a[92:95], v[200:203]\n"
"v_mfma_f32_16x16x32_bf16 v[200:203], a[48:51], a[88:91], v[200:203]\n"
"v_mfma_f32_16x16x32_bf16 v[232:235], a[48:51], a[96:99], v[232:235]\n"
"buffer_load_dwordx4 v25, s[36:39], s79 offen offset:3072 lds\n"
"v_mfma_f32_16x16x32_bf16 v[232:235], a[52:55], a[100:103], v[232:235]\n"
"v_mfma_f32_16x16x32_bf16 v[236:239], a[60:63], a[100:103], v[236:239]\n"
"v_mfma_f32_16x16x32_bf16 v[236:239], a[56:59], a[96:99], v[236:239]\n"
"v_mfma_f32_16x16x32_bf16 v[204:207], a[56:59], a[88:91], v[204:207]\n"
"v_mfma_f32_16x16x32_bf16 v[204:207], a[60:63], a[92:95], v[204:207]\n"
"s_add_u32 m0, s82, 4096\n"
"v_mfma_f32_16x16x32_bf16 v[172:175], a[60:63], a[84:87], v[172:175]\n"
"buffer_load_dwordx4 v24, s[36:39], s80 offen offset:0 lds\n"
"v_mfma_f32_16x16x32_bf16 v[172:175], a[56:59], a[80:83], v[172:175]\n"
"s_add_u32 s40, s40, 524288\n"
"s_addc_u32 s41, s41, 0\n"
"s_sub_u32 s42, s42, 524288\n"
"v_mfma_f32_16x16x32_bf16 v[140:143], a[56:59], a[72:75], v[140:143]\n"
"s_add_u32 s36, s36, 128\n"
"s_addc_u32 s37, s37, 0\n"
"s_sub_u32 s38, s38, 128\n"
"v_mfma_f32_16x16x32_bf16 v[140:143], a[60:63], a[76:79], v[140:143]\n"
"v_mfma_f32_16x16x32_bf16 v[108:111], a[60:63], a[68:71], v[108:111]\n"
"v_mfma_f32_16x16x32_bf16 v[108:111], a[56:59], a[64:67], v[108:111]\n"
"s_waitcnt lgkmcnt(0)\n"
"v_mfma_f32_16x16x32_bf16 v[80:83], a[152:155], a[216:219], v[80:83]\n"
"s_waitcnt vmcnt(13)\n"
"s_barrier\n"
"v_add_u32 v16, 0xd040, v16\n"
"v_add_u32 v17, 0xd040, v17\n"
"v_mfma_f32_16x16x32_bf16 v[80:83], a[156:159], a[220:223], v[80:83]\n"
"s_add_u32 m0, s81, 53312\n"
"v_add_u32 v18, 0xd040, v18\n"
"v_add_u32 v19, 0xd040, v19\n"
"v_mfma_f32_16x16x32_bf16 v[112:115], a[156:159], a[228:231], v[112:115]\n"
"buffer_load_dwordx4 v22, s[40:43], s68 offen offset:0 nt lds\n"
"v_mfma_f32_16x16x32_bf16 v[112:115], a[152:155], a[224:227], v[112:115]\n"
"v_add_u32 v20, 0xd040, v20\n"
"v_add_u32 v21, 0xd040, v21\n"
"v_mfma_f32_16x16x32_bf16 v[144:147], a[152:155], a[232:235], v[144:147]\n"
"ds_read_b64_tr_b16 a[0:1], v16 offset:0\n"
"v_mfma_f32_16x16x32_bf16 v[144:147], a[156:159], a[236:239], v[144:147]\n"
"ds_read_b64_tr_b16 a[2:3], v16 offset:2048\n"
"v_mfma_f32_16x16x32_bf16 v[176:179], a[156:159], a[244:247], v[176:179]\n"
"ds_read_b64_tr_b16 a[4:5], v16 offset:16416\n"
"v_mfma_f32_16x16x32_bf16 v[176:179], a[152:155], a[240:243], v[176:179]\n"
"ds_read_b64_tr_b16 a[6:7], v16 offset:18464\n"
"v_mfma_f32_16x16x32_bf16 v[208:211], a[152:155], a[248:251], v[208:211]\n"
"buffer_load_dwordx4 v23, s[40:43], s69 offen offset:1024 nt lds\n"
"v_mfma_f32_16x16x32_bf16 v[208:211], a[156:159], a[252:255], v[208:211]\n"
"ds_read_b128 a[64:67], v20 offset:0\n"
"v_mfma_f32_16x16x32_bf16 v[212:215], a[164:167], a[252:255], v[212:215]\n"
"ds_read_b128 a[68:71], v21 offset:0\n"
"v_mfma_f32_16x16x32_bf16 v[212:215], a[160:163], a[248:251], v[212:215]\n"
"ds_read_b128 a[72:75], v20 offset:2048\n"
"v_mfma_f32_16x16x32_bf16 v[180:183], a[160:163], a[240:243], v[180:183]\n"
"ds_read_b128 a[76:79], v21 offset:2048\n"
"v_mfma_f32_16x16x32_bf16 v[180:183], a[164:167], a[244:247], v[180:183]\n"
"ds_read_b128 a[80:83], v20 offset:4096\n"
"v_mfma_f32_16x16x32_bf16 v[148:151], a[164:167], a[236:239], v[148:151]\n"
"buffer_load_dwordx4 v22, s[40:43], s70 offen offset:2048 nt lds\n"
"v_mfma_f32_16x16x32_bf16 v[148:151], a[160:163], a[232:235], v[148:151]\n"
"ds_read_b128 a[84:87], v21 offset:4096\n"
"v_mfma_f32_16x16x32_bf16 v[116:119], a[160:163], a[224:227], v[116:119]\n"
"ds_read_b128 a[88:91], v20 offset:6144\n"
"v_mfma_f32_16x16x32_bf16 v[116:119], a[164:167], a[228:231], v[116:119]\n"
"ds_read_b128 a[92:95], v21 offset:6144\n"
"v_mfma_f32_16x16x32_bf16 v[84:87], a[164:167], a[220:223], v[84:87]\n"
"ds_read_b128 a[96:99], v20 offset:8192\n"
"v_mfma_f32_16x16x32_bf16 v[84:87], a[160:163], a[216:219], v[84:87]\n"
"ds_read_b128 a[100:103], v21 offset:8192\n"
"v_mfma_f32_16x16x32_bf16 v[88:91], a[168:171], a[216:219], v[88:91]\n"
"buffer_load_dwordx4 v23, s[40:43], s71 offen offset:3072 nt lds\n"
"v_mfma_f32_16x16x32_bf16 v[88:91], a[172:175], a[220:223], v[88:91]\n"
"ds_read_b64_tr_b16 a[8:9], v16 offset:8\n"
"v_mfma_f32_16x16x32_bf16 v[120:123], a[172:175], a[228:231], v[120:123]\n"
"ds_read_b64_tr_b16 a[10:11], v16 offset:2056\n"
"v_mfma_f32_16x16x32_bf16 v[120:123], a[168:171], a[224:227], v[120:123]\n"
"ds_read_b64_tr_b16 a[12:13], v16 offset:16424\n"
"v_mfma_f32_16x16x32_bf16 v[152:155], a[168:171], a[232:235], v[152:155]\n"
"ds_read_b64_tr_b16 a[14:15], v16 offset:18472\n"
"v_mfma_f32_16x16x32_bf16 v[152:155], a[172:175], a[236:239], v[152:155]\n"
"ds_read_b64_tr_b16 a[16:17], v17 offset:0\n"
"s_add_u32 m0, s81, 57416\n"
"v_mfma_f32_16x16x32_bf16 v[184:187], a[172:175], a[244:247], v[184:187]\n"
"buffer_load_dwordx4 v22, s[40:43], s72 offen offset:0 nt lds\n"
"v_mfma_f32_16x16x32_bf16 v[184:187], a[168:171], a[240:243], v[184:187]\n"
"ds_read_b64_tr_b16 a[18:19], v17 offset:2048\n"
"v_mfma_f32_16x16x32_bf16 v[216:219], a[168:171], a[248:251], v[216:219]\n"
"ds_read_b64_tr_b16 a[20:21], v17 offset:16416\n"
"v_mfma_f32_16x16x32_bf16 v[216:219], a[172:175], a[252:255], v[216:219]\n"
"ds_read_b64_tr_b16 a[22:23], v17 offset:18464\n"
"v_mfma_f32_16x16x32_bf16 v[220:223], a[180:183], a[252:255], v[220:223]\n"
"ds_read_b64_tr_b16 a[24:25], v17 offset:8\n"
"v_mfma_f32_16x16x32_bf16 v[220:223], a[176:179], a[248:251], v[220:223]\n"
"ds_read_b64_tr_b16 a[26:27], v17 offset:2056\n"
"v_mfma_f32_16x16x32_bf16 v[188:191], a[176:179], a[240:243], v[188:191]\n"
"buffer_load_dwordx4 v23, s[40:43], s73 offen offset:1024 nt lds\n"
"v_mfma_f32_16x16x32_bf16 v[188:191], a[180:183], a[244:247], v[188:191]\n"
"ds_read_b64_tr_b16 a[28:29], v17 offset:16424\n"
"v_mfma_f32_16x16x32_bf16 v[156:159], a[180:183], a[236:239], v[156:159]\n"
"ds_read_b64_tr_b16 a[30:31], v17 offset:18472\n"
"v_mfma_f32_16x16x32_bf16 v[156:159], a[176:179], a[232:235], v[156:159]\n"
"ds_read_b64_tr_b16 a[32:33], v18 offset:0\n"
"v_mfma_f32_16x16x32_bf16 v[124:127], a[176:179], a[224:227], v[124:127]\n"
"ds_read_b64_tr_b16 a[34:35], v18 offset:2048\n"
"v_mfma_f32_16x16x32_bf16 v[124:127], a[180:183], a[228:231], v[124:127]\n"
"ds_read_b64_tr_b16 a[36:37], v18 offset:16416\n"
"v_mfma_f32_16x16x32_bf16 v[92:95], a[180:183], a[220:223], v[92:95]\n"
"buffer_load_dwordx4 v22, s[40:43], s74 offen offset:2048 nt lds\n"
"v_mfma_f32_16x16x32_bf16 v[92:95], a[176:179], a[216:219], v[92:95]\n"
"ds_read_b64_tr_b16 a[38:39], v18 offset:18464\n"
"v_mfma_f32_16x16x32_bf16 v[96:99], a[184:187], a[216:219], v[96:99]\n"
"ds_read_b64_tr_b16 a[40:41], v18 offset:8\n"
"v_mfma_f32_16x16x32_bf16 v[96:99], a[188:191], a[220:223], v[96:99]\n"
"ds_read_b64_tr_b16 a[42:43], v18 offset:2056\n"
"v_mfma_f32_16x16x32_bf16 v[128:131], a[188:191], a[228:231], v[128:131]\n"
"ds_read_b64_tr_b16 a[44:45], v18 offset:16424\n"
"v_mfma_f32_16x16x32_bf16 v[128:131], a[184:187], a[224:227], v[128:131]\n"
"ds_read_b64_tr_b16 a[46:47], v18 offset:18472\n"
"v_mfma_f32_16x16x32_bf16 v[160:163], a[184:187], a[232:235], v[160:163]\n"
"buffer_load_dwordx4 v23, s[40:43], s75 offen offset:3072 nt lds\n"
"v_mfma_f32_16x16x32_bf16 v[160:163], a[188:191], a[236:239], v[160:163]\n"
"ds_read_b64_tr_b16 a[48:49], v19 offset:0\n"
"v_mfma_f32_16x16x32_bf16 v[192:195], a[188:191], a[244:247], v[192:195]\n"
"ds_read_b64_tr_b16 a[50:51], v19 offset:2048\n"
"v_mfma_f32_16x16x32_bf16 v[192:195], a[184:187], a[240:243], v[192:195]\n"
"ds_read_b64_tr_b16 a[52:53], v19 offset:16416\n"
"v_mfma_f32_16x16x32_bf16 v[224:227], a[184:187], a[248:251], v[224:227]\n"
"ds_read_b64_tr_b16 a[54:55], v19 offset:18464\n"
"v_mfma_f32_16x16x32_bf16 v[224:227], a[188:191], a[252:255], v[224:227]\n"
"ds_read_b64_tr_b16 a[56:57], v19 offset:8\n"
"s_add_u32 m0, s82, 53312\n"
"v_mfma_f32_16x16x32_bf16 v[228:231], a[196:199], a[252:255], v[228:231]\n"
"buffer_load_dwordx4 v24, s[36:39], s76 offen offset:0 lds\n"
"v_mfma_f32_16x16x32_bf16 v[228:231], a[192:195], a[248:251], v[228:231]\n"
"ds_read_b64_tr_b16 a[58:59], v19 offset:2056\n"
"v_mfma_f32_16x16x32_bf16 v[196:199], a[192:195], a[240:243], v[196:199]\n"
"ds_read_b64_tr_b16 a[60:61], v19 offset:16424\n"
"v_mfma_f32_16x16x32_bf16 v[196:199], a[196:199], a[244:247], v[196:199]\n"
"ds_read_b64_tr_b16 a[62:63], v19 offset:18472\n"
"v_mfma_f32_16x16x32_bf16 v[164:167], a[196:199], a[236:239], v[164:167]\n"
"v_mfma_f32_16x16x32_bf16 v[164:167], a[192:195], a[232:235], v[164:167]\n"
"v_mfma_f32_16x16x32_bf16 v[132:135], a[192:195], a[224:227], v[132:135]\n"
"buffer_load_dwordx4 v25, s[36:39], s77 offen offset:1024 lds\n"
"v_mfma_f32_16x16x32_bf16 v[132:135], a[196:199], a[228:231], v[132:135]\n"
"v_mfma_f32_16x16x32_bf16 v[100:103], a[196:199], a[220:223], v[100:103]\n"
"v_mfma_f32_16x16x32_bf16 v[100:103], a[192:195], a[216:219], v[100:103]\n"
"v_mfma_f32_16x16x32_bf16 v[104:107], a[200:203], a[216:219], v[104:107]\n"
"v_mfma_f32_16x16x32_bf16 v[104:107], a[204:207], a[220:223], v[104:107]\n"
"v_mfma_f32_16x16x32_bf16 v[136:139], a[204:207], a[228:231], v[136:139]\n"
"buffer_load_dwordx4 v24, s[36:39], s78 offen offset:2048 lds\n"
"v_mfma_f32_16x16x32_bf16 v[136:139], a[200:203], a[224:227], v[136:139]\n"
"v_mfma_f32_16x16x32_bf16 v[168:171], a[200:203], a[232:235], v[168:171]\n"
"v_mfma_f32_16x16x32_bf16 v[168:171], a[204:207], a[236:239], v[168:171]\n"
"v_mfma_f32_16x16x32_bf16 v[200:203], a[204:207], a[244:247], v[200:203]\n"
"v_mfma_f32_16x16x32_bf16 v[200:203], a[200:203], a[240:243], v[200:203]\n"
"v_mfma_f32_16x16x32_bf16 v[232:235], a[200:203], a[248:251], v[232:235]\n"
"buffer_load_dwordx4 v25, s[36:39], s79 offen offset:3072 lds\n"
"v_mfma_f32_16x16x32_bf16 v[232:235], a[204:207], a[252:255], v[232:235]\n"
"v_mfma_f32_16x16x32_bf16 v[236:239], a[212:215], a[252:255], v[236:239]\n"
"v_mfma_f32_16x16x32_bf16 v[236:239], a[208:211], a[248:251], v[236:239]\n"
"v_mfma_f32_16x16x32_bf16 v[204:207], a[208:211], a[240:243], v[204:207]\n"
"v_mfma_f32_16x16x32_bf16 v[204:207], a[212:215], a[244:247], v[204:207]\n"
"s_add_u32 m0, s82, 57408\n"
"v_mfma_f32_16x16x32_bf16 v[172:175], a[212:215], a[236:239], v[172:175]\n"
"buffer_load_dwordx4 v24, s[36:39], s80 offen offset:0 lds\n"
"v_mfma_f32_16x16x32_bf16 v[172:175], a[208:211], a[232:235], v[172:175]\n"
"s_add_u32 s40, s40, 524288\n"
"s_addc_u32 s41, s41, 0\n"
"s_sub_u32 s42, s42, 524288\n"
"v_mfma_f32_16x16x32_bf16 v[140:143], a[208:211], a[224:227], v[140:143]\n"
"s_add_u32 s36, s36, 128\n"
"s_addc_u32 s37, s37, 0\n"
"s_sub_u32 s38, s38, 128\n"
"v_mfma_f32_16x16x32_bf16 v[140:143], a[212:215], a[228:231], v[140:143]\n"
"v_mfma_f32_16x16x32_bf16 v[108:111], a[212:215], a[220:223], v[108:111]\n"
"v_mfma_f32_16x16x32_bf16 v[108:111], a[208:211], a[216:219], v[108:111]\n"
"s_lshl_b32 s96, s25, 2\n"
"s_add_u32 s96, s96, 16384\n"
"v_add_u32 v14, 1, v14\n"
"v_mov_b32 v12, s96\n"
"buffer_store_dword v14, v12, s[64:67], 0 offen sc1\n"
"s_waitcnt lgkmcnt(0)\n"
"v_mfma_f32_16x16x32_bf16 v[80:83], a[0:3], a[64:67], v[80:83]\n"
"s_waitcnt vmcnt(14)\n"
"s_barrier\n"
"v_add_u32 v16, 0xfffe5f80, v16\n"
"v_add_u32 v17, 0xfffe5f80, v17\n"
"v_mfma_f32_16x16x32_bf16 v[80:83], a[4:7], a[68:71], v[80:83]\n"
"s_add_u32 m0, s81, 106624\n"
"v_add_u32 v18, 0xfffe5f80, v18\n"
"v_add_u32 v19, 0xfffe5f80, v19\n"
"v_mfma_f32_16x16x32_bf16 v[112:115], a[4:7], a[76:79], v[112:115]\n"
"buffer_load_dwordx4 v22, s[40:43], s68 offen offset:0 nt lds\n"
"v_mfma_f32_16x16x32_bf16 v[112:115], a[0:3], a[72:75], v[112:115]\n"
"v_add_u32 v20, 0xfffe5f80, v20\n"
"v_add_u32 v21, 0xfffe5f80, v21\n"
"v_mfma_f32_16x16x32_bf16 v[144:147], a[0:3], a[80:83], v[144:147]\n"
"ds_read_b64_tr_b16 a[152:153], v16 offset:0\n"
"v_mfma_f32_16x16x32_bf16 v[144:147], a[4:7], a[84:87], v[144:147]\n"
"ds_read_b64_tr_b16 a[154:155], v16 offset:2048\n"
"v_mfma_f32_16x16x32_bf16 v[176:179], a[4:7], a[92:95], v[176:179]\n"
"ds_read_b64_tr_b16 a[156:157], v16 offset:16416\n"
"v_mfma_f32_16x16x32_bf16 v[176:179], a[0:3], a[88:91], v[176:179]\n"
"ds_read_b64_tr_b16 a[158:159], v16 offset:18464\n"
"v_mfma_f32_16x16x32_bf16 v[208:211], a[0:3], a[96:99], v[208:211]\n"
"buffer_load_dwordx4 v23, s[40:43], s69 offen offset:1024 nt lds\n"
"v_mfma_f32_16x16x32_bf16 v[208:211], a[4:7], a[100:103], v[208:211]\n"
"ds_read_b128 a[216:219], v20 offset:0\n"
"v_mfma_f32_16x16x32_bf16 v[212:215], a[12:15], a[100:103], v[212:215]\n"
"ds_read_b128 a[220:223], v21 offset:0\n"
"v_mfma_f32_16x16x32_bf16 v[212:215], a[8:11], a[96:99], v[212:215]\n"
"ds_read_b128 a[224:227], v20 offset:2048\n"
"v_mfma_f32_16x16x32_bf16 v[180:183], a[8:11], a[88:91], v[180:183]\n"
"ds_read_b128 a[228:231], v21 offset:2048\n"
"v_mfma_f32_16x16x32_bf16 v[180:183], a[12:15], a[92:95], v[180:183]\n"
"ds_read_b128 a[232:235], v20 offset:4096\n"
"v_mfma_f32_16x16x32_bf16 v[148:151], a[12:15], a[84:87], v[148:151]\n"
"buffer_load_dwordx4 v22, s[40:43], s70 offen offset:2048 nt lds\n"
"v_mfma_f32_16x16x32_bf16 v[148:151], a[8:11], a[80:83], v[148:151]\n"
"ds_read_b128 a[236:239], v21 offset:4096\n"
"v_mfma_f32_16x16x32_bf16 v[116:119], a[8:11], a[72:75], v[116:119]\n"
"ds_read_b128 a[240:243], v20 offset:6144\n"
"v_mfma_f32_16x16x32_bf16 v[116:119], a[12:15], a[76:79], v[116:119]\n"
"ds_read_b128 a[244:247], v21 offset:6144\n"
"v_mfma_f32_16x16x32_bf16 v[84:87], a[12:15], a[68:71], v[84:87]\n"
"ds_read_b128 a[248:251], v20 offset:8192\n"
"v_mfma_f32_16x16x32_bf16 v[84:87], a[8:11], a[64:67], v[84:87]\n"
"ds_read_b128 a[252:255], v21 offset:8192\n"
"v_mfma_f32_16x16x32_bf16 v[88:91], a[16:19], a[64:67], v[88:91]\n"
"buffer_load_dwordx4 v23, s[40:43], s71 offen offset:3072 nt lds\n"
"v_mfma_f32_16x16x32_bf16 v[88:91], a[20:23], a[68:71], v[88:91]\n"
"ds_read_b64_tr_b16 a[160:161], v16 offset:8\n"
"v_mfma_f32_16x16x32_bf16 v[120:123], a[20:23], a[76:79], v[120:123]\n"
"ds_read_b64_tr_b16 a[162:163], v16 offset:2056\n"
"v_mfma_f32_16x16x32_bf16 v[120:123], a[16:19], a[72:75], v[120:123]\n"
"ds_read_b64_tr_b16 a[164:165], v16 offset:16424\n"
"v_mfma_f32_16x16x32_bf16 v[152:155], a[16:19], a[80:83], v[152:155]\n"
"ds_read_b64_tr_b16 a[166:167], v16 offset:18472\n"
"v_mfma_f32_16x16x32_bf16 v[152:155], a[20:23], a[84:87], v[152:155]\n"
"ds_read_b64_tr_b16 a[168:169], v17 offset:0\n"
"s_add_u32 m0, s81, 110728\n"
"v_mfma_f32_16x16x32_bf16 v[184:187], a[20:23], a[92:95], v[184:187]\n"
"buffer_load_dwordx4 v22, s[40:43], s72 offen offset:0 nt lds\n"
"v_mfma_f32_16x16x32_bf16 v[184:187], a[16:19], a[88:91], v[184:187]\n"
"ds_read_b64_tr_b16 a[170:171], v17 offset:2048\n"
"v_mfma_f32_16x16x32_bf16 v[216:219], a[16:19], a[96:99], v[216:219]\n"
"ds_read_b64_tr_b16 a[172:173], v17 offset:16416\n"
"v_mfma_f32_16x16x32_bf16 v[216:219], a[20:23], a[100:103], v[216:219]\n"
"ds_read_b64_tr_b16 a[174:175], v17 offset:18464\n"
"v_mfma_f32_16x16x32_bf16 v[220:223], a[28:31], a[100:103], v[220:223]\n"
"ds_read_b64_tr_b16 a[176:177], v17 offset:8\n"
"v_mfma_f32_16x16x32_bf16 v[220:223], a[24:27], a[96:99], v[220:223]\n"
"ds_read_b64_tr_b16 a[178:179], v17 offset:2056\n"
"v_mfma_f32_16x16x32_bf16 v[188:191], a[24:27], a[88:91], v[188:191]\n"
"buffer_load_dwordx4 v23, s[40:43], s73 offen offset:1024 nt lds\n"
"v_mfma_f32_16x16x32_bf16 v[188:191], a[28:31], a[92:95], v[188:191]\n"
"ds_read_b64_tr_b16 a[180:181], v17 offset:16424\n"
"v_mfma_f32_16x16x32_bf16 v[156:159], a[28:31], a[84:87], v[156:159]\n"
"ds_read_b64_tr_b16 a[182:183], v17 offset:18472\n"
"v_mfma_f32_16x16x32_bf16 v[156:159], a[24:27], a[80:83], v[156:159]\n"
"ds_read_b64_tr_b16 a[184:185], v18 offset:0\n"
"v_mfma_f32_16x16x32_bf16 v[124:127], a[24:27], a[72:75], v[124:127]\n"
"ds_read_b64_tr_b16 a[186:187], v18 offset:2048\n"
"v_mfma_f32_16x16x32_bf16 v[124:127], a[28:31], a[76:79], v[124:127]\n"
"ds_read_b64_tr_b16 a[188:189], v18 offset:16416\n"
"v_mfma_f32_16x16x32_bf16 v[92:95], a[28:31], a[68:71], v[92:95]\n"
"buffer_load_dwordx4 v22, s[40:43], s74 offen offset:2048 nt lds\n"
"v_mfma_f32_16x16x32_bf16 v[92:95], a[24:27], a[64:67], v[92:95]\n"
"ds_read_b64_tr_b16 a[190:191], v18 offset:18464\n"
"v_mfma_f32_16x16x32_bf16 v[96:99], a[32:35], a[64:67], v[96:99]\n"
"ds_read_b64_tr_b16 a[192:193], v18 offset:8\n"
"v_mfma_f32_16x16x32_bf16 v[96:99], a[36:39], a[68:71], v[96:99]\n"
"ds_read_b64_tr_b16 a[194:195], v18 offset:2056\n"
"v_mfma_f32_16x16x32_bf16 v[128:131], a[36:39], a[76:79], v[128:131]\n"
"ds_read_b64_tr_b16 a[196:197], v18 offset:16424\n"
"v_mfma_f32_16x16x32_bf16 v[128:131], a[32:35], a[72:75], v[128:131]\n"
"ds_read_b64_tr_b16 a[198:199], v18 offset:18472\n"
"v_mfma_f32_16x16x32_bf16 v[160:163], a[32:35], a[80:83], v[160:163]\n"
"buffer_load_dwordx4 v23, s[40:43], s75 offen offset:3072 nt lds\n"
"v_mfma_f32_16x16x32_bf16 v[160:163], a[36:39], a[84:87], v[160:163]\n"
"ds_read_b64_tr_b16 a[200:201], v19 offset:0\n"
"v_mfma_f32_16x16x32_bf16 v[192:195], a[36:39], a[92:95], v[192:195]\n"
"ds_read_b64_tr_b16 a[202:203], v19 offset:2048\n"
"v_mfma_f32_16x16x32_bf16 v[192:195], a[32:35], a[88:91], v[192:195]\n"
"ds_read_b64_tr_b16 a[204:205], v19 offset:16416\n"
"v_mfma_f32_16x16x32_bf16 v[224:227], a[32:35], a[96:99], v[224:227]\n"
"ds_read_b64_tr_b16 a[206:207], v19 offset:18464\n"
"v_mfma_f32_16x16x32_bf16 v[224:227], a[36:39], a[100:103], v[224:227]\n"
"ds_read_b64_tr_b16 a[208:209], v19 offset:8\n"
"s_add_u32 m0, s82, 106624\n"
"v_mfma_f32_16x16x32_bf16 v[228:231], a[44:47], a[100:103], v[228:231]\n"
"buffer_load_dwordx4 v24, s[36:39], s76 offen offset:0 lds\n"
"v_mfma_f32_16x16x32_bf16 v[228:231], a[40:43], a[96:99], v[228:231]\n"
"ds_read_b64_tr_b16 a[210:211], v19 offset:2056\n"
"v_mfma_f32_16x16x32_bf16 v[196:199], a[40:43], a[88:91], v[196:199]\n"
"ds_read_b64_tr_b16 a[212:213], v19 offset:16424\n"
"v_mfma_f32_16x16x32_bf16 v[196:199], a[44:47], a[92:95], v[196:199]\n"
"ds_read_b64_tr_b16 a[214:215], v19 offset:18472\n"
"v_mfma_f32_16x16x32_bf16 v[164:167], a[44:47], a[84:87], v[164:167]\n"
"v_mfma_f32_16x16x32_bf16 v[164:167], a[40:43], a[80:83], v[164:167]\n"
"v_mfma_f32_16x16x32_bf16 v[132:135], a[40:43], a[72:75], v[132:135]\n"
"buffer_load_dwordx4 v25, s[36:39], s77 offen offset:1024 lds\n"
"v_mfma_f32_16x16x32_bf16 v[132:135], a[44:47], a[76:79], v[132:135]\n"
"v_mfma_f32_16x16x32_bf16 v[100:103], a[44:47], a[68:71], v[100:103]\n"
"v_mfma_f32_16x16x32_bf16 v[100:103], a[40:43], a[64:67], v[100:103]\n"
"v_mfma_f32_16x16x32_bf16 v[104:107], a[48:51], a[64:67], v[104:107]\n"
"v_mfma_f32_16x16x32_bf16 v[104:107], a[52:55], a[68:71], v[104:107]\n"
"v_mfma_f32_16x16x32_bf16 v[136:139], a[52:55], a[76:79], v[136:139]\n"
"buffer_load_dwordx4 v24, s[36:39], s78 offen offset:2048 lds\n"
"v_mfma_f32_16x16x32_bf16 v[136:139], a[48:51], a[72:75], v[136:139]\n"
"v_mfma_f32_16x16x32_bf16 v[168:171], a[48:51], a[80:83], v[168:171]\n"
"v_mfma_f32_16x16x32_bf16 v[168:171], a[52:55], a[84:87], v[168:171]\n"
"v_mfma_f32_16x16x32_bf16 v[200:203], a[52:55], a[92:95], v[200:203]\n"
"v_mfma_f32_16x16x32_bf16 v[200:203], a[48:51], a[88:91], v[200:203]\n"
"v_mfma_f32_16x16x32_bf16 v[232:235], a[48:51], a[96:99], v[232:235]\n"
"buffer_load_dwordx4 v25, s[36:39], s79 offen offset:3072 lds\n"
"v_mfma_f32_16x16x32_bf16 v[232:235], a[52:55], a[100:103], v[232:235]\n"
"v_mfma_f32_16x16x32_bf16 v[236:239], a[60:63], a[100:103], v[236:239]\n"
"v_mfma_f32_16x16x32_bf16 v[236:239], a[56:59], a[96:99], v[236:239]\n"
"v_mfma_f32_16x16x32_bf16 v[204:207], a[56:59], a[88:91], v[204:207]\n"
"v_mfma_f32_16x16x32_bf16 v[204:207], a[60:63], a[92:95], v[204:207]\n"
"s_add_u32 m0, s82, 110720\n"
"v_mfma_f32_16x16x32_bf16 v[172:175], a[60:63], a[84:87], v[172:175]\n"
"buffer_load_dwordx4 v24, s[36:39], s80 offen offset:0 lds\n"
"v_mfma_f32_16x16x32_bf16 v[172:175], a[56:59], a[80:83], v[172:175]\n"
"s_add_u32 s40, s40, 524288\n"
"s_addc_u32 s41, s41, 0\n"
"s_sub_u32 s42, s42, 524288\n"
"v_mfma_f32_16x16x32_bf16 v[140:143], a[56:59], a[72:75], v[140:143]\n"
"s_add_u32 s36, s36, 128\n"
"s_addc_u32 s37, s37, 0\n"
"s_sub_u32 s38, s38, 128\n"
"v_mfma_f32_16x16x32_bf16 v[140:143], a[60:63], a[76:79], v[140:143]\n"
"v_mfma_f32_16x16x32_bf16 v[108:111], a[60:63], a[68:71], v[108:111]\n"
"v_mfma_f32_16x16x32_bf16 v[108:111], a[56:59], a[64:67], v[108:111]\n"
"s_waitcnt lgkmcnt(0)\n"
"v_mfma_f32_16x16x32_bf16 v[80:83], a[152:155], a[216:219], v[80:83]\n"
"s_waitcnt vmcnt(14)\n"
"s_barrier\n"
"v_add_u32 v16, 0xd040, v16\n"
"v_add_u32 v17, 0xd040, v17\n"
"v_mfma_f32_16x16x32_bf16 v[80:83], a[156:159], a[220:223], v[80:83]\n"
"s_add_u32 m0, s83, 0\n"
"v_add_u32 v18, 0xd040, v18\n"
"v_add_u32 v19, 0xd040, v19\n"
"v_mfma_f32_16x16x32_bf16 v[112:115], a[156:159], a[228:231], v[112:115]\n"
"buffer_load_dwordx4 v240, s[44:47], s85 offen offset:0 nt lds\n"
"v_mfma_f32_16x16x32_bf16 v[112:115], a[152:155], a[224:227], v[112:115]\n"
"v_add_u32 v20, 0xd040, v20\n"
"v_add_u32 v21, 0xd040, v21\n"
"v_mfma_f32_16x16x32_bf16 v[144:147], a[152:155], a[232:235], v[144:147]\n"
"ds_read_b64_tr_b16 a[0:1], v16 offset:0\n"
"s_lshl_b32 s9, s24, 2\n"
"v_mfma_f32_16x16x32_bf16 v[144:147], a[156:159], a[236:239], v[144:147]\n"
"ds_read_b64_tr_b16 a[2:3], v16 offset:2048\n"
"s_add_u32 s9, s9, %[wid]\n"
"v_mfma_f32_16x16x32_bf16 v[176:179], a[156:159], a[244:247], v[176:179]\n"
"ds_read_b64_tr_b16 a[4:5], v16 offset:16416\n"
"s_and_b32 s9, s9, 31\n"
"v_mfma_f32_16x16x32_bf16 v[176:179], a[152:155], a[240:243], v[176:179]\n"
"ds_read_b64_tr_b16 a[6:7], v16 offset:18464\n"
"s_lshl_b32 s9, s9, 7\n"
"v_mfma_f32_16x16x32_bf16 v[208:211], a[152:155], a[248:251], v[208:211]\n"
"ds_read_b128 a[64:67], v20 offset:0\n"
"s_add_u32 s9, s9, Ltrans_%=-Lpc_%=\n"
"v_mfma_f32_16x16x32_bf16 v[208:211], a[156:159], a[252:255], v[208:211]\n"
"ds_read_b128 a[68:71], v21 offset:0\n"
"v_mov_b32 v35, 0x3f803f80\n"
"v_mfma_f32_16x16x32_bf16 v[212:215], a[164:167], a[252:255], v[212:215]\n"
"ds_read_b128 a[72:75], v20 offset:2048\n"
"s_lshl_b32 s94, %[wid], 13\n"
"v_mfma_f32_16x16x32_bf16 v[212:215], a[160:163], a[248:251], v[212:215]\n"
"buffer_load_dwordx4 v241, s[44:47], s85 offen offset:1024 nt lds\n"
"s_add_u32 s94, s94, 131072\n"
"v_mfma_f32_16x16x32_bf16 v[180:183], a[160:163], a[240:243], v[180:183]\n"
"ds_read_b128 a[76:79], v21 offset:2048\n"
"s_lshl_b32 s68, %[wid], 3\n"
"v_mfma_f32_16x16x32_bf16 v[180:183], a[164:167], a[244:247], v[180:183]\n"
"ds_read_b128 a[80:83], v20 offset:4096\n"
"s_add_u32 s68, s68, 0\n"
"v_mfma_f32_16x16x32_bf16 v[148:151], a[164:167], a[236:239], v[148:151]\n"
"ds_read_b128 a[84:87], v21 offset:4096\n"
"s_mul_i32 s96, s68, 43\n"
"v_mfma_f32_16x16x32_bf16 v[148:151], a[160:163], a[232:235], v[148:151]\n"
"ds_read_b128 a[88:91], v20 offset:6144\n"
"s_lshr_b32 s96, s96, 8\n"
"v_mfma_f32_16x16x32_bf16 v[116:119], a[160:163], a[224:227], v[116:119]\n"
"ds_read_b128 a[92:95], v21 offset:6144\n"
"s_mul_i32 s96, s96, 780288\n"
"v_mfma_f32_16x16x32_bf16 v[116:119], a[164:167], a[228:231], v[116:119]\n"
"ds_read_b128 a[96:99], v20 offset:8192\n"
"s_lshl_b32 s68, s68, 10\n"
"v_mfma_f32_16x16x32_bf16 v[84:87], a[164:167], a[220:223], v[84:87]\n"
"ds_read_b128 a[100:103], v21 offset:8192\n"
"s_add_u32 s68, s68, s96\n"
"v_mfma_f32_16x16x32_bf16 v[84:87], a[160:163], a[216:219], v[84:87]\n"
"ds_read_b64_tr_b16 a[8:9], v16 offset:8\n"
"s_lshl_b32 s69, %[wid], 3\n"
"v_mfma_f32_16x16x32_bf16 v[88:91], a[168:171], a[216:219], v[88:91]\n"
"buffer_load_dwordx4 v242, s[44:47], s85 offen offset:2048 nt lds\n"
"s_add_u32 s69, s69, 1\n"
"v_mfma_f32_16x16x32_bf16 v[88:91], a[172:175], a[220:223], v[88:91]\n"
"ds_read_b64_tr_b16 a[10:11], v16 offset:2056\n"
"s_mul_i32 s96, s69, 43\n"
"v_mfma_f32_16x16x32_bf16 v[120:123], a[172:175], a[228:231], v[120:123]\n"
"ds_read_b64_tr_b16 a[12:13], v16 offset:16424\n"
"s_lshr_b32 s96, s96, 8\n"
"v_mfma_f32_16x16x32_bf16 v[120:123], a[168:171], a[224:227], v[120:123]\n"
"ds_read_b64_tr_b16 a[14:15], v16 offset:18472\n"
"s_mul_i32 s96, s96, 780288\n"
"v_mfma_f32_16x16x32_bf16 v[152:155], a[168:171], a[232:235], v[152:155]\n"
"ds_read_b64_tr_b16 a[16:17], v17 offset:0\n"
"s_lshl_b32 s69, s69, 10\n"
"v_mfma_f32_16x16x32_bf16 v[152:155], a[172:175], a[236:239], v[152:155]\n"
"ds_read_b64_tr_b16 a[18:19], v17 offset:2048\n"
"s_add_u32 s69, s69, s96\n"
"v_mfma_f32_16x16x32_bf16 v[184:187], a[172:175], a[244:247], v[184:187]\n"
"ds_read_b64_tr_b16 a[20:21], v17 offset:16416\n"
"s_lshl_b32 s70, %[wid], 3\n"
"v_mfma_f32_16x16x32_bf16 v[184:187], a[168:171], a[240:243], v[184:187]\n"
"ds_read_b64_tr_b16 a[22:23], v17 offset:18464\n"
"s_add_u32 s70, s70, 2\n"
"v_mfma_f32_16x16x32_bf16 v[216:219], a[168:171], a[248:251], v[216:219]\n"
"ds_read_b64_tr_b16 a[24:25], v17 offset:8\n"
"s_mul_i32 s96, s70, 43\n"
"v_mfma_f32_16x16x32_bf16 v[216:219], a[172:175], a[252:255], v[216:219]\n"
"buffer_load_dwordx4 v243, s[44:47], s85 offen offset:3072 nt lds\n"
"s_lshr_b32 s96, s96, 8\n"
"v_mfma_f32_16x16x32_bf16 v[220:223], a[180:183], a[252:255], v[220:223]\n"
"ds_read_b64_tr_b16 a[26:27], v17 offset:2056\n"
"s_add_u32 s85, s85, 16384\n"
"v_mfma_f32_16x16x32_bf16 v[220:223], a[176:179], a[248:251], v[220:223]\n"
"ds_read_b64_tr_b16 a[28:29], v17 offset:16424\n"
"s_mul_i32 s96, s96, 780288\n"
"v_mfma_f32_16x16x32_bf16 v[188:191], a[176:179], a[240:243], v[188:191]\n"
"ds_read_b64_tr_b16 a[30:31], v17 offset:18472\n"
"s_lshl_b32 s70, s70, 10\n"
"v_mfma_f32_16x16x32_bf16 v[188:191], a[180:183], a[244:247], v[188:191]\n"
"ds_read_b64_tr_b16 a[32:33], v18 offset:0\n"
"s_add_u32 s70, s70, s96\n"
"v_mfma_f32_16x16x32_bf16 v[156:159], a[180:183], a[236:239], v[156:159]\n"
"ds_read_b64_tr_b16 a[34:35], v18 offset:2048\n"
"s_lshl_b32 s71, %[wid], 3\n"
"v_mfma_f32_16x16x32_bf16 v[156:159], a[176:179], a[232:235], v[156:159]\n"
"ds_read_b64_tr_b16 a[36:37], v18 offset:16416\n"
"s_add_u32 s71, s71, 3\n"
"v_mfma_f32_16x16x32_bf16 v[124:127], a[176:179], a[224:227], v[124:127]\n"
"ds_read_b64_tr_b16 a[38:39], v18 offset:18464\n"
"s_mul_i32 s96, s71, 43\n"
"v_mfma_f32_16x16x32_bf16 v[124:127], a[180:183], a[228:231], v[124:127]\n"
"ds_read_b64_tr_b16 a[40:41], v18 offset:8\n"
"s_add_u32 m0, s83, 16384\n"
"v_mfma_f32_16x16x32_bf16 v[92:95], a[180:183], a[220:223], v[92:95]\n"
"buffer_load_dwordx4 v240, s[44:47], s85 offen offset:0 nt lds\n"
"s_lshr_b32 s96, s96, 8\n"
"v_mfma_f32_16x16x32_bf16 v[92:95], a[176:179], a[216:219], v[92:95]\n"
"ds_read_b64_tr_b16 a[42:43], v18 offset:2056\n"
"s_mul_i32 s96, s96, 780288\n"
"v_mfma_f32_16x16x32_bf16 v[96:99], a[184:187], a[216:219], v[96:99]\n"
"ds_read_b64_tr_b16 a[44:45], v18 offset:16424\n"
"s_lshl_b32 s71, s71, 10\n"
"v_mfma_f32_16x16x32_bf16 v[96:99], a[188:191], a[220:223], v[96:99]\n"
"ds_read_b64_tr_b16 a[46:47], v18 offset:18472\n"
"s_add_u32 s71, s71, s96\n"
"v_mfma_f32_16x16x32_bf16 v[128:131], a[188:191], a[228:231], v[128:131]\n"
"ds_read_b64_tr_b16 a[48:49], v19 offset:0\n"
"s_lshl_b32 s72, %[wid], 3\n"
"v_mfma_f32_16x16x32_bf16 v[128:131], a[184:187], a[224:227], v[128:131]\n"
"ds_read_b64_tr_b16 a[50:51], v19 offset:2048\n"
"s_add_u32 s72, s72, 4\n"
"v_mfma_f32_16x16x32_bf16 v[160:163], a[184:187], a[232:235], v[160:163]\n"
"ds_read_b64_tr_b16 a[52:53], v19 offset:16416\n"
"s_mul_i32 s96, s72, 43\n"
"v_mfma_f32_16x16x32_bf16 v[160:163], a[188:191], a[236:239], v[160:163]\n"
"ds_read_b64_tr_b16 a[54:55], v19 offset:18464\n"
"s_lshr_b32 s96, s96, 8\n"
"v_mfma_f32_16x16x32_bf16 v[192:195], a[188:191], a[244:247], v[192:195]\n"
"ds_read_b64_tr_b16 a[56:57], v19 offset:8\n"
"s_mul_i32 s96, s96, 780288\n"
"v_mfma_f32_16x16x32_bf16 v[192:195], a[184:187], a[240:243], v[192:195]\n"
"buffer_load_dwordx4 v241, s[44:47], s85 offen offset:1024 nt lds\n"
"s_lshl_b32 s72, s72, 10\n"
"v_mfma_f32_16x16x32_bf16 v[224:227], a[184:187], a[248:251], v[224:227]\n"
"ds_read_b64_tr_b16 a[58:59], v19 offset:2056\n"
"s_add_u32 s72, s72, s96\n"
"v_mfma_f32_16x16x32_bf16 v[224:227], a[188:191], a[252:255], v[224:227]\n"
"ds_read_b64_tr_b16 a[60:61], v19 offset:16424\n"
"s_lshl_b32 s73, %[wid], 3\n"
"v_mfma_f32_16x16x32_bf16 v[228:231], a[196:199], a[252:255], v[228:231]\n"
"ds_read_b64_tr_b16 a[62:63], v19 offset:18472\n"
"s_add_u32 s73, s73, 5\n"
"v_mfma_f32_16x16x32_bf16 v[228:231], a[192:195], a[248:251], v[228:231]\n"
"s_mul_i32 s96, s73, 43\n"
"s_lshr_b32 s96, s96, 8\n"
"v_mfma_f32_16x16x32_bf16 v[196:199], a[192:195], a[240:243], v[196:199]\n"
"s_mul_i32 s96, s96, 780288\n"
"s_lshl_b32 s73, s73, 10\n"
"v_mfma_f32_16x16x32_bf16 v[196:199], a[196:199], a[244:247], v[196:199]\n"
"s_add_u32 s73, s73, s96\n"
"s_lshl_b32 s74, %[wid], 3\n"
"v_mfma_f32_16x16x32_bf16 v[164:167], a[196:199], a[236:239], v[164:167]\n"
"s_add_u32 s74, s74, 6\n"
"s_mul_i32 s96, s74, 43\n"
"v_mfma_f32_16x16x32_bf16 v[164:167], a[192:195], a[232:235], v[164:167]\n"
"s_lshr_b32 s96, s96, 8\n"
"s_mul_i32 s96, s96, 780288\n"
"v_mfma_f32_16x16x32_bf16 v[132:135], a[192:195], a[224:227], v[132:135]\n"
"buffer_load_dwordx4 v242, s[44:47], s85 offen offset:2048 nt lds\n"
"s_lshl_b32 s74, s74, 10\n"
"v_mfma_f32_16x16x32_bf16 v[132:135], a[196:199], a[228:231], v[132:135]\n"
"s_add_u32 s74, s74, s96\n"
"s_lshl_b32 s75, %[wid], 3\n"
"v_mfma_f32_16x16x32_bf16 v[100:103], a[196:199], a[220:223], v[100:103]\n"
"s_add_u32 s75, s75, 7\n"
"s_mul_i32 s96, s75, 43\n"
"v_mfma_f32_16x16x32_bf16 v[100:103], a[192:195], a[216:219], v[100:103]\n"
"s_lshr_b32 s96, s96, 8\n"
"s_mul_i32 s96, s96, 780288\n"
"v_mfma_f32_16x16x32_bf16 v[104:107], a[200:203], a[216:219], v[104:107]\n"
"s_lshl_b32 s75, s75, 10\n"
"s_add_u32 s75, s75, s96\n"
"v_mfma_f32_16x16x32_bf16 v[104:107], a[204:207], a[220:223], v[104:107]\n"
"s_add_u32 s76, %[wid], 32\n"
"s_mul_i32 s96, s76, 43\n"
"v_mfma_f32_16x16x32_bf16 v[136:139], a[204:207], a[228:231], v[136:139]\n"
"s_lshr_b32 s96, s96, 8\n"
"s_mul_i32 s96, s96, 780288\n"
"v_mfma_f32_16x16x32_bf16 v[136:139], a[200:203], a[224:227], v[136:139]\n"
"s_lshl_b32 s76, s76, 10\n"
"s_add_u32 s76, s76, s96\n"
"v_mfma_f32_16x16x32_bf16 v[168:171], a[200:203], a[232:235], v[168:171]\n"
"s_mov_b64 s[56:57], s[22:23]\n"
"s_add_u32 s56, s56, s28\n"
"s_addc_u32 s57, s57, 0\n"
"v_mfma_f32_16x16x32_bf16 v[168:171], a[204:207], a[236:239], v[168:171]\n"
"buffer_load_dwordx4 v243, s[44:47], s85 offen offset:3072 nt lds\n"
"s_and_b32 s57, s57, 0xffff\n"
"v_mfma_f32_16x16x32_bf16 v[200:203], a[204:207], a[244:247], v[200:203]\n"
"s_add_u32 s85, s85, 16384\n"
"s_mov_b32 s58, 3938304\n"
"v_mfma_f32_16x16x32_bf16 v[200:203], a[200:203], a[240:243], v[200:203]\n"
"s_mov_b32 s59, 0x20000\n"
"s_mov_b64 s[36:37], s[16:17]\n"
"v_mfma_f32_16x16x32_bf16 v[232:235], a[200:203], a[248:251], v[232:235]\n"
"s_sub_u32 s36, s36, 4096\n"
"s_subb_u32 s37, s37, 0\n"
"v_mfma_f32_16x16x32_bf16 v[232:235], a[204:207], a[252:255], v[232:235]\n"
"s_and_b32 s37, s37, 0xffff\n"
"s_mov_b32 s38, 40960\n"
"v_mfma_f32_16x16x32_bf16 v[236:239], a[212:215], a[252:255], v[236:239]\n"
"s_mov_b32 s39, 0x20000\n"
"v_bfe_u32 v252, v0, 2, 3\n"
"v_mfma_f32_16x16x32_bf16 v[236:239], a[208:211], a[248:251], v[236:239]\n"
"v_mul_u32_u24 v252, 896, v252\n"
"v_mfma_f32_16x16x32_bf16 v[204:207], a[208:211], a[240:243], v[204:207]\n"
"v_add_u32 v252, v252, v8\n"
"v_mfma_f32_16x16x32_bf16 v[204:207], a[212:215], a[244:247], v[204:207]\n"
"v_lshrrev_b32 v253, 1, v0\n"
"v_mfma_f32_16x16x32_bf16 v[172:175], a[212:215], a[236:239], v[172:175]\n"
"v_mul_u32_u24 v253, 1152, v253\n"
"v_mfma_f32_16x16x32_bf16 v[172:175], a[208:211], a[232:235], v[172:175]\n"
"v_and_b32 v254, 1, v0\n"
"v_mfma_f32_16x16x32_bf16 v[140:143], a[208:211], a[224:227], v[140:143]\n"
"v_lshl_add_u32 v253, v254, 4, v253\n"
"s_lshl_b32 s97, %[wid], 8\n"
"buffer_load_dword v13, off, s[4:7], s9\n"
"v_mfma_f32_16x16x32_bf16 v[140:143], a[212:215], a[228:231], v[140:143]\n"
"s_add_u32 s9, s9, 4096\n"
"v_mfma_f32_16x16x32_bf16 v[108:111], a[212:215], a[220:223], v[108:111]\n"
"v_mfma_f32_16x16x32_bf16 v[108:111], a[208:211], a[216:219], v[108:111]\n"
"s_waitcnt lgkmcnt(0)\n"
"v_mfma_f32_16x16x32_bf16 v[80:83], a[0:3], a[64:67], v[80:83]\n"
"s_waitcnt vmcnt(8)\n"
"s_barrier\n"
"v_add_u32 v16, 0xd040, v16\n"
"v_add_u32 v17, 0xd040, v17\n"
"s_cmp_ge_u32 s25, 1020\n"
"s_cbranch_scc0 Lld1_%=\n"
"v_add_u32 v57, 16384, v56\n"
"buffer_load_dwordx4 v[44:47], v57, s[64:67], 0 offen sc1\n"
"Lld1_%=:\n"
"v_mfma_f32_16x16x32_bf16 v[80:83], a[4:7], a[68:71], v[80:83]\n"
"s_add_u32 m0, s84, 0\n"
"v_add_u32 v18, 0xd040, v18\n"
"v_add_u32 v19, 0xd040, v19\n"
"v_mfma_f32_16x16x32_bf16 v[112:115], a[4:7], a[76:79], v[112:115]\n"
"buffer_load_dwordx4 v8, s[48:51], s86 offen offset:0 nt lds\n"
"v_mfma_f32_16x16x32_bf16 v[112:115], a[0:3], a[72:75], v[112:115]\n"
"v_add_u32 v20, 0xd040, v20\n"
"v_add_u32 v21, 0xd040, v21\n"
"v_mfma_f32_16x16x32_bf16 v[144:147], a[0:3], a[80:83], v[144:147]\n"
"ds_read_b64_tr_b16 a[152:153], v16 offset:0\n"
"v_mfma_f32_16x16x32_bf16 v[144:147], a[4:7], a[84:87], v[144:147]\n"
"ds_read_b64_tr_b16 a[154:155], v16 offset:2048\n"
"v_mfma_f32_16x16x32_bf16 v[176:179], a[4:7], a[92:95], v[176:179]\n"
"ds_read_b64_tr_b16 a[156:157], v16 offset:16416\n"
"v_mfma_f32_16x16x32_bf16 v[176:179], a[0:3], a[88:91], v[176:179]\n"
"ds_read_b64_tr_b16 a[158:159], v16 offset:18464\n"
"v_mfma_f32_16x16x32_bf16 v[208:211], a[0:3], a[96:99], v[208:211]\n"
"ds_read_b128 a[216:219], v20 offset:0\n"
"v_mfma_f32_16x16x32_bf16 v[208:211], a[4:7], a[100:103], v[208:211]\n"
"ds_read_b128 a[220:223], v21 offset:0\n"
"v_mfma_f32_16x16x32_bf16 v[212:215], a[12:15], a[100:103], v[212:215]\n"
"ds_read_b128 a[224:227], v20 offset:2048\n"
"v_mfma_f32_16x16x32_bf16 v[212:215], a[8:11], a[96:99], v[212:215]\n"
"buffer_load_dwordx4 v8, s[48:51], s87 offen offset:1024 nt lds\n"
"v_mfma_f32_16x16x32_bf16 v[180:183], a[8:11], a[88:91], v[180:183]\n"
"ds_read_b128 a[228:231], v21 offset:2048\n"
"v_mfma_f32_16x16x32_bf16 v[180:183], a[12:15], a[92:95], v[180:183]\n"
"ds_read_b128 a[232:235], v20 offset:4096\n"
"v_mfma_f32_16x16x32_bf16 v[148:151], a[12:15], a[84:87], v[148:151]\n"
"ds_read_b128 a[236:239], v21 offset:4096\n"
"v_mfma_f32_16x16x32_bf16 v[148:151], a[8:11], a[80:83], v[148:151]\n"
"ds_read_b128 a[240:243], v20 offset:6144\n"
"v_mfma_f32_16x16x32_bf16 v[116:119], a[8:11], a[72:75], v[116:119]\n"
"ds_read_b128 a[244:247], v21 offset:6144\n"
"v_mfma_f32_16x16x32_bf16 v[116:119], a[12:15], a[76:79], v[116:119]\n"
"ds_read_b128 a[248:251], v20 offset:8192\n"
"v_mfma_f32_16x16x32_bf16 v[84:87], a[12:15], a[68:71], v[84:87]\n"
"ds_read_b128 a[252:255], v21 offset:8192\n"
"v_mfma_f32_16x16x32_bf16 v[84:87], a[8:11], a[64:67], v[84:87]\n"
"ds_read_b64_tr_b16 a[160:161], v16 offset:8\n"
"v_mfma_f32_16x16x32_bf16 v[88:91], a[16:19], a[64:67], v[88:91]\n"
"buffer_load_dwordx4 v8, s[48:51], s86 offen offset:2048 nt lds\n"
"v_mfma_f32_16x16x32_bf16 v[88:91], a[20:23], a[68:71], v[88:91]\n"
"ds_read_b64_tr_b16 a[162:163], v16 offset:2056\n"
"v_mfma_f32_16x16x32_bf16 v[120:123], a[20:23], a[76:79], v[120:123]\n"
"ds_read_b64_tr_b16 a[164:165], v16 offset:16424\n"
"v_mfma_f32_16x16x32_bf16 v[120:123], a[16:19], a[72:75], v[120:123]\n"
"ds_read_b64_tr_b16 a[166:167], v16 offset:18472\n"
"v_mfma_f32_16x16x32_bf16 v[152:155], a[16:19], a[80:83], v[152:155]\n"
"ds_read_b64_tr_b16 a[168:169], v17 offset:0\n"
"v_mfma_f32_16x16x32_bf16 v[152:155], a[20:23], a[84:87], v[152:155]\n"
"ds_read_b64_tr_b16 a[170:171], v17 offset:2048\n"
"v_mfma_f32_16x16x32_bf16 v[184:187], a[20:23], a[92:95], v[184:187]\n"
"ds_read_b64_tr_b16 a[172:173], v17 offset:16416\n"
"v_mfma_f32_16x16x32_bf16 v[184:187], a[16:19], a[88:91], v[184:187]\n"
"ds_read_b64_tr_b16 a[174:175], v17 offset:18464\n"
"v_mfma_f32_16x16x32_bf16 v[216:219], a[16:19], a[96:99], v[216:219]\n"
"ds_read_b64_tr_b16 a[176:177], v17 offset:8\n"
"v_mfma_f32_16x16x32_bf16 v[216:219], a[20:23], a[100:103], v[216:219]\n"
"buffer_load_dwordx4 v8, s[48:51], s87 offen offset:3072 nt lds\n"
"v_mfma_f32_16x16x32_bf16 v[220:223], a[28:31], a[100:103], v[220:223]\n"
"ds_read_b64_tr_b16 a[178:179], v17 offset:2056\n"
"s_add_u32 s86, s86, 16384\n"
"s_add_u32 s87, s87, 16384\n"
"v_mfma_f32_16x16x32_bf16 v[220:223], a[24:27], a[96:99], v[220:223]\n"
"ds_read_b64_tr_b16 a[180:181], v17 offset:16424\n"
"v_mfma_f32_16x16x32_bf16 v[188:191], a[24:27], a[88:91], v[188:191]\n"
"ds_read_b64_tr_b16 a[182:183], v17 offset:18472\n"
"v_mfma_f32_16x16x32_bf16 v[188:191], a[28:31], a[92:95], v[188:191]\n"
"ds_read_b64_tr_b16 a[184:185], v18 offset:0\n"
"v_mfma_f32_16x16x32_bf16 v[156:159], a[28:31], a[84:87], v[156:159]\n"
"ds_read_b64_tr_b16 a[186:187], v18 offset:2048\n"
"v_mfma_f32_16x16x32_bf16 v[156:159], a[24:27], a[80:83], v[156:159]\n"
"ds_read_b64_tr_b16 a[188:189], v18 offset:16416\n"
"v_mfma_f32_16x16x32_bf16 v[124:127], a[24:27], a[72:75], v[124:127]\n"
"ds_read_b64_tr_b16 a[190:191], v18 offset:18464\n"
"v_mfma_f32_16x16x32_bf16 v[124:127], a[28:31], a[76:79], v[124:127]\n"
"ds_read_b64_tr_b16 a[192:193], v18 offset:8\n"
"v_mfma_f32_16x16x32_bf16 v[92:95], a[28:31], a[68:71], v[92:95]\n"
"ds_read_b64_tr_b16 a[194:195], v18 offset:2056\n"
"v_mfma_f32_16x16x32_bf16 v[92:95], a[24:27], a[64:67], v[92:95]\n"
"ds_read_b64_tr_b16 a[196:197], v18 offset:16424\n"
"v_mfma_f32_16x16x32_bf16 v[96:99], a[32:35], a[64:67], v[96:99]\n"
"ds_read_b64_tr_b16 a[198:199], v18 offset:18472\n"
"v_mfma_f32_16x16x32_bf16 v[96:99], a[36:39], a[68:71], v[96:99]\n"
"ds_read_b64_tr_b16 a[200:201], v19 offset:0\n"
"v_mfma_f32_16x16x32_bf16 v[128:131], a[36:39], a[76:79], v[128:131]\n"
"ds_read_b64_tr_b16 a[202:203], v19 offset:2048\n"
"v_mfma_f32_16x16x32_bf16 v[128:131], a[32:35], a[72:75], v[128:131]\n"
"ds_read_b64_tr_b16 a[204:205], v19 offset:16416\n"
"v_mfma_f32_16x16x32_bf16 v[160:163], a[32:35], a[80:83], v[160:163]\n"
"ds_read_b64_tr_b16 a[206:207], v19 offset:18464\n"
"v_mfma_f32_16x16x32_bf16 v[160:163], a[36:39], a[84:87], v[160:163]\n"
"ds_read_b64_tr_b16 a[208:209], v19 offset:8\n"
"v_mfma_f32_16x16x32_bf16 v[192:195], a[36:39], a[92:95], v[192:195]\n"
"ds_read_b64_tr_b16 a[210:211], v19 offset:2056\n"
"v_mfma_f32_16x16x32_bf16 v[192:195], a[32:35], a[88:91], v[192:195]\n"
"ds_read_b64_tr_b16 a[212:213], v19 offset:16424\n"
"v_mfma_f32_16x16x32_bf16 v[224:227], a[32:35], a[96:99], v[224:227]\n"
"ds_read_b64_tr_b16 a[214:215], v19 offset:18472\n"
"v_mfma_f32_16x16x32_bf16 v[224:227], a[36:39], a[100:103], v[224:227]\n"
"v_mfma_f32_16x16x32_bf16 v[228:231], a[44:47], a[100:103], v[228:231]\n"
"v_mfma_f32_16x16x32_bf16 v[228:231], a[40:43], a[96:99], v[228:231]\n"
"v_mfma_f32_16x16x32_bf16 v[196:199], a[40:43], a[88:91], v[196:199]\n"
"v_mfma_f32_16x16x32_bf16 v[196:199], a[44:47], a[92:95], v[196:199]\n"
"v_mfma_f32_16x16x32_bf16 v[164:167], a[44:47], a[84:87], v[164:167]\n"
"v_mfma_f32_16x16x32_bf16 v[164:167], a[40:43], a[80:83], v[164:167]\n"
"v_mfma_f32_16x16x32_bf16 v[132:135], a[40:43], a[72:75], v[132:135]\n"
"v_mfma_f32_16x16x32_bf16 v[132:135], a[44:47], a[76:79], v[132:135]\n"
"v_mfma_f32_16x16x32_bf16 v[100:103], a[44:47], a[68:71], v[100:103]\n"
"v_mfma_f32_16x16x32_bf16 v[100:103], a[40:43], a[64:67], v[100:103]\n"
"v_mfma_f32_16x16x32_bf16 v[104:107], a[48:51], a[64:67], v[104:107]\n"
"v_mfma_f32_16x16x32_bf16 v[104:107], a[52:55], a[68:71], v[104:107]\n"
"v_mfma_f32_16x16x32_bf16 v[136:139], a[52:55], a[76:79], v[136:139]\n"
"v_mfma_f32_16x16x32_bf16 v[136:139], a[48:51], a[72:75], v[136:139]\n"
"v_mfma_f32_16x16x32_bf16 v[168:171], a[48:51], a[80:83], v[168:171]\n"
"v_mfma_f32_16x16x32_bf16 v[168:171], a[52:55], a[84:87], v[168:171]\n"
"v_mfma_f32_16x16x32_bf16 v[200:203], a[52:55], a[92:95], v[200:203]\n"
"v_mfma_f32_16x16x32_bf16 v[200:203], a[48:51], a[88:91], v[200:203]\n"
"v_mfma_f32_16x16x32_bf16 v[232:235], a[48:51], a[96:99], v[232:235]\n"
"v_mfma_f32_16x16x32_bf16 v[232:235], a[52:55], a[100:103], v[232:235]\n"
"v_mfma_f32_16x16x32_bf16 v[236:239], a[60:63], a[100:103], v[236:239]\n"
"v_mfma_f32_16x16x32_bf16 v[236:239], a[56:59], a[96:99], v[236:239]\n"
"v_mfma_f32_16x16x32_bf16 v[204:207], a[56:59], a[88:91], v[204:207]\n"
"v_mfma_f32_16x16x32_bf16 v[204:207], a[60:63], a[92:95], v[204:207]\n"
"v_mfma_f32_16x16x32_bf16 v[172:175], a[60:63], a[84:87], v[172:175]\n"
"v_mfma_f32_16x16x32_bf16 v[172:175], a[56:59], a[80:83], v[172:175]\n"
"v_mfma_f32_16x16x32_bf16 v[140:143], a[56:59], a[72:75], v[140:143]\n"
"buffer_load_dword v13, off, s[4:7], s9\n"
"v_mfma_f32_16x16x32_bf16 v[140:143], a[60:63], a[76:79], v[140:143]\n"
"s_add_u32 s9, s9, 4096\n"
"v_mfma_f32_16x16x32_bf16 v[108:111], a[60:63], a[68:71], v[108:111]\n"
"v_mfma_f32_16x16x32_bf16 v[108:111], a[56:59], a[64:67], v[108:111]\n"
"Ltrans_%=:\n"
"s_waitcnt lgkmcnt(0)\n"
"v_mfma_f32_16x16x32_bf16 v[80:83], a[152:155], a[216:219], v[80:83]\n"
"s_waitcnt vmcnt(8)\n"
"s_barrier\n"
"v_mfma_f32_16x16x32_bf16 v[80:83], a[156:159], a[220:223], v[80:83]\n"
"v_mfma_f32_16x16x32_bf16 v[84:87], a[164:167], a[220:223], v[84:87]\n"
"v_mfma_f32_16x16x32_bf16 v[84:87], a[160:163], a[216:219], v[84:87]\n"
"ds_read_b128 a[80:83], v1 offset:0\n"
"v_mfma_f32_16x16x32_bf16 v[88:91], a[168:171], a[216:219], v[88:91]\n"
"ds_read_b128 a[84:87], v2 offset:0\n"
"v_mfma_f32_16x16x32_bf16 v[88:91], a[172:175], a[220:223], v[88:91]\n"
"ds_read_b128 a[88:91], v3 offset:0\n"
"v_mfma_f32_16x16x32_bf16 v[92:95], a[180:183], a[220:223], v[92:95]\n"
"ds_read_b128 a[92:95], v4 offset:0\n"
"v_mul_f32 v80, v80, v10\n"
"v_mfma_f32_16x16x32_bf16 v[92:95], a[176:179], a[216:219], v[92:95]\n"
"ds_read_b128 a[96:99], v1 offset:4096\n"
"v_mul_f32 v81, v81, v10\n"
"v_mfma_f32_16x16x32_bf16 v[96:99], a[184:187], a[216:219], v[96:99]\n"
"ds_read_b128 a[100:103], v2 offset:4096\n"
"v_mul_f32 v82, v82, v10\n"
"v_mfma_f32_16x16x32_bf16 v[96:99], a[188:191], a[220:223], v[96:99]\n"
"ds_read_b128 a[104:107], v3 offset:4096\n"
"v_mul_f32 v83, v83, v10\n"
"v_mfma_f32_16x16x32_bf16 v[100:103], a[196:199], a[220:223], v[100:103]\n"
"ds_read_b128 a[108:111], v4 offset:4096\n"
"v_mul_f32 v84, v84, v10\n"
"v_mfma_f32_16x16x32_bf16 v[100:103], a[192:195], a[216:219], v[100:103]\n"
"v_mul_f32 v85, v85, v10\n"
"v_mul_f32 v86, v86, v10\n"
"v_mfma_f32_16x16x32_bf16 v[104:107], a[200:203], a[216:219], v[104:107]\n"
"v_mul_f32 v87, v87, v10\n"
"v_mfma_f32_16x16x32_bf16 v[104:107], a[204:207], a[220:223], v[104:107]\n"
"v_cvt_pk_bf16_f32 v80, v80, v81\n"
"v_cvt_pk_bf16_f32 v81, v82, v83\n"
"v_mfma_f32_16x16x32_bf16 v[108:111], a[212:215], a[220:223], v[108:111]\n"
"v_cvt_pk_bf16_f32 v82, v84, v85\n"
"v_mfma_f32_16x16x32_bf16 v[108:111], a[208:211], a[216:219], v[108:111]\n"
"v_cvt_pk_bf16_f32 v83, v86, v87\n"
"v_accvgpr_write_b32 a0, v80\n"
"v_mfma_f32_16x16x32_bf16 v[140:143], a[208:211], a[224:227], v[140:143]\n"
"v_accvgpr_write_b32 a1, v81\n"
"v_mfma_f32_16x16x32_bf16 v[140:143], a[212:215], a[228:231], v[140:143]\n"
"v_accvgpr_write_b32 a2, v82\n"
"v_accvgpr_write_b32 a3, v83\n"
"v_mfma_f32_16x16x32_bf16 v[136:139], a[204:207], a[228:231], v[136:139]\n"
"v_mul_f32 v88, v88, v10\n"
"v_mfma_f32_16x16x32_bf16 v[136:139], a[200:203], a[224:227], v[136:139]\n"
"s_add_u32 s96, s97, 4096\n"
"s_add_u32 m0, s94, 0\n"
"s_nop 0\n"
"buffer_load_dwordx4 v252, s[36:39], s96 offen nt lds\n"
"v_mul_f32 v89, v89, v10\n"
"v_mfma_f32_16x16x32_bf16 v[132:135], a[192:195], a[224:227], v[132:135]\n"
"v_mul_f32 v90, v90, v10\n"
"v_mfma_f32_16x16x32_bf16 v[132:135], a[196:199], a[228:231], v[132:135]\n"
"ds_read_b128 a[112:115], v1 offset:8192\n"
"v_mul_f32 v91, v91, v10\n"
"v_mfma_f32_16x16x32_bf16 v[128:131], a[188:191], a[228:231], v[128:131]\n"
"v_mul_f32 v92, v92, v10\n"
"v_mfma_f32_16x16x32_bf16 v[128:131], a[184:187], a[224:227], v[128:131]\n"
"v_mul_f32 v93, v93, v10\n"
"v_mul_f32 v94, v94, v10\n"
"v_mfma_f32_16x16x32_bf16 v[124:127], a[176:179], a[224:227], v[124:127]\n"
"v_mul_f32 v95, v95, v10\n"
"v_mfma_f32_16x16x32_bf16 v[124:127], a[180:183], a[228:231], v[124:127]\n"
"v_cvt_pk_bf16_f32 v88, v88, v89\n"
"v_cvt_pk_bf16_f32 v89, v90, v91\n"
"v_mfma_f32_16x16x32_bf16 v[120:123], a[172:175], a[228:231], v[120:123]\n"
"s_add_u32 s96, s97, 4224\n"
"s_add_u32 m0, s94, 1024\n"
"s_nop 0\n"
"buffer_load_dwordx4 v252, s[36:39], s96 offen nt lds\n"
"v_cvt_pk_bf16_f32 v90, v92, v93\n"
"v_mfma_f32_16x16x32_bf16 v[120:123], a[168:171], a[224:227], v[120:123]\n"
"v_cvt_pk_bf16_f32 v91, v94, v95\n"
"v_accvgpr_write_b32 a4, v88\n"
"v_mfma_f32_16x16x32_bf16 v[116:119], a[160:163], a[224:227], v[116:119]\n"
"ds_read_b128 a[116:119], v2 offset:8192\n"
"v_accvgpr_write_b32 a5, v89\n"
"v_mfma_f32_16x16x32_bf16 v[116:119], a[164:167], a[228:231], v[116:119]\n"
"v_accvgpr_write_b32 a6, v90\n"
"v_accvgpr_write_b32 a7, v91\n"
"v_mfma_f32_16x16x32_bf16 v[112:115], a[156:159], a[228:231], v[112:115]\n"
"v_mul_f32 v96, v96, v10\n"
"v_mfma_f32_16x16x32_bf16 v[112:115], a[152:155], a[224:227], v[112:115]\n"
"v_mul_f32 v97, v97, v10\n"
"v_mul_f32 v98, v98, v10\n"
"v_mfma_f32_16x16x32_bf16 v[144:147], a[152:155], a[232:235], v[144:147]\n"
"v_mul_f32 v99, v99, v10\n"
"v_mfma_f32_16x16x32_bf16 v[144:147], a[156:159], a[236:239], v[144:147]\n"
"s_add_u32 s96, s97, 13312\n"
"s_add_u32 m0, s94, 2048\n"
"s_nop 0\n"
"buffer_load_dwordx4 v252, s[36:39], s96 offen nt lds\n"
"v_mul_f32 v100, v100, v10\n"
"v_mfma_f32_16x16x32_bf16 v[148:151], a[164:167], a[236:239], v[148:151]\n"
"v_mul_f32 v101, v101, v10\n"
"v_mfma_f32_16x16x32_bf16 v[148:151], a[160:163], a[232:235], v[148:151]\n"
"ds_read_b128 a[120:123], v3 offset:8192\n"
"v_mul_f32 v102, v102, v10\n"
"v_mfma_f32_16x16x32_bf16 v[152:155], a[168:171], a[232:235], v[152:155]\n"
"v_mul_f32 v103, v103, v10\n"
"v_mfma_f32_16x16x32_bf16 v[152:155], a[172:175], a[236:239], v[152:155]\n"
"v_cvt_pk_bf16_f32 v96, v96, v97\n"
"v_cvt_pk_bf16_f32 v97, v98, v99\n"
"v_mfma_f32_16x16x32_bf16 v[156:159], a[180:183], a[236:239], v[156:159]\n"
"v_cvt_pk_bf16_f32 v98, v100, v101\n"
"v_mfma_f32_16x16x32_bf16 v[156:159], a[176:179], a[232:235], v[156:159]\n"
"v_cvt_pk_bf16_f32 v99, v102, v103\n"
"v_accvgpr_write_b32 a8, v96\n"
"v_mfma_f32_16x16x32_bf16 v[160:163], a[184:187], a[232:235], v[160:163]\n"
"s_add_u32 s96, s97, 13440\n"
"s_add_u32 m0, s94, 3072\n"
"s_nop 0\n"
"buffer_load_dwordx4 v252, s[36:39], s96 offen nt lds\n"
"v_accvgpr_write_b32 a9, v97\n"
"v_mfma_f32_16x16x32_bf16 v[160:163], a[188:191], a[236:239], v[160:163]\n"
"v_accvgpr_write_b32 a10, v98\n"
"v_accvgpr_write_b32 a11, v99\n"
"v_mfma_f32_16x16x32_bf16 v[164:167], a[196:199], a[236:239], v[164:167]\n"
"ds_read_b128 a[124:127], v4 offset:8192\n"
"v_mul_f32 v104, v104, v10\n"
"v_mfma_f32_16x16x32_bf16 v[164:167], a[192:195], a[232:235], v[164:167]\n"
"v_mul_f32 v105, v105, v10\n"
"v_mul_f32 v106, v106, v10\n"
"v_mfma_f32_16x16x32_bf16 v[168:171], a[200:203], a[232:235], v[168:171]\n"
"v_mul_f32 v107, v107, v10\n"
"v_mfma_f32_16x16x32_bf16 v[168:171], a[204:207], a[236:239], v[168:171]\n"
"v_mul_f32 v108, v108, v10\n"
"v_mul_f32 v109, v109, v10\n"
"v_mfma_f32_16x16x32_bf16 v[172:175], a[212:215], a[236:239], v[172:175]\n"
"v_mul_f32 v110, v110, v10\n"
"v_mfma_f32_16x16x32_bf16 v[172:175], a[208:211], a[232:235], v[172:175]\n"
"s_add_u32 s96, s97, 22528\n"
"s_add_u32 m0, s94, 4096\n"
"s_nop 0\n"
"buffer_load_dwordx4 v252, s[36:39], s96 offen nt lds\n"
"v_mul_f32 v111, v111, v10\n"
"v_mfma_f32_16x16x32_bf16 v[204:207], a[208:211], a[240:243], v[204:207]\n"
"v_cvt_pk_bf16_f32 v104, v104, v105\n"
"v_mfma_f32_16x16x32_bf16 v[204:207], a[212:215], a[244:247], v[204:207]\n"
"ds_read_b128 a[128:131], v1 offset:12288\n"
"v_cvt_pk_bf16_f32 v105, v106, v107\n"
"v_mfma_f32_16x16x32_bf16 v[200:203], a[204:207], a[244:247], v[200:203]\n"
"v_cvt_pk_bf16_f32 v106, v108, v109\n"
"v_mfma_f32_16x16x32_bf16 v[200:203], a[200:203], a[240:243], v[200:203]\n"
"v_cvt_pk_bf16_f32 v107, v110, v111\n"
"v_accvgpr_write_b32 a12, v104\n"
"v_mfma_f32_16x16x32_bf16 v[196:199], a[192:195], a[240:243], v[196:199]\n"
"v_accvgpr_write_b32 a13, v105\n"
"v_mfma_f32_16x16x32_bf16 v[196:199], a[196:199], a[244:247], v[196:199]\n"
"v_accvgpr_write_b32 a14, v106\n"
"v_accvgpr_write_b32 a15, v107\n"
"v_mfma_f32_16x16x32_bf16 v[192:195], a[188:191], a[244:247], v[192:195]\n"
"s_add_u32 s96, s97, 22656\n"
"s_add_u32 m0, s94, 5120\n"
"s_nop 0\n"
"buffer_load_dwordx4 v252, s[36:39], s96 offen nt lds\n"
"v_mul_f32 v136, v136, v10\n"
"v_mfma_f32_16x16x32_bf16 v[192:195], a[184:187], a[240:243], v[192:195]\n"
"v_mul_f32 v137, v137, v10\n"
"v_mul_f32 v138, v138, v10\n"
"v_mfma_f32_16x16x32_bf16 v[188:191], a[176:179], a[240:243], v[188:191]\n"
"ds_read_b128 a[132:135], v2 offset:12288\n"
"v_mul_f32 v139, v139, v10\n"
"v_mfma_f32_16x16x32_bf16 v[188:191], a[180:183], a[244:247], v[188:191]\n"
"v_mul_f32 v140, v140, v10\n"
"v_mul_f32 v141, v141, v10\n"
"v_mfma_f32_16x16x32_bf16 v[184:187], a[172:175], a[244:247], v[184:187]\n"
"v_mul_f32 v142, v142, v10\n"
"v_mfma_f32_16x16x32_bf16 v[184:187], a[168:171], a[240:243], v[184:187]\n"
"v_mul_f32 v143, v143, v10\n"
"v_cvt_pk_bf16_f32 v136, v136, v137\n"
"v_mfma_f32_16x16x32_bf16 v[180:183], a[160:163], a[240:243], v[180:183]\n"
"v_cvt_pk_bf16_f32 v137, v138, v139\n"
"v_mfma_f32_16x16x32_bf16 v[180:183], a[164:167], a[244:247], v[180:183]\n"
"s_add_u32 s96, s97, 31744\n"
"s_add_u32 m0, s94, 6144\n"
"s_nop 0\n"
"buffer_load_dwordx4 v252, s[36:39], s96 offen nt lds\n"
"v_cvt_pk_bf16_f32 v138, v140, v141\n"
"v_mfma_f32_16x16x32_bf16 v[176:179], a[156:159], a[244:247], v[176:179]\n"
"v_cvt_pk_bf16_f32 v139, v142, v143\n"
"v_mfma_f32_16x16x32_bf16 v[176:179], a[152:155], a[240:243], v[176:179]\n"
"ds_read_b128 a[136:139], v3 offset:12288\n"
"v_accvgpr_write_b32 a28, v136\n"
"v_mfma_f32_16x16x32_bf16 v[208:211], a[152:155], a[248:251], v[208:211]\n"
"v_accvgpr_write_b32 a29, v137\n"
"v_mfma_f32_16x16x32_bf16 v[208:211], a[156:159], a[252:255], v[208:211]\n"
"v_accvgpr_write_b32 a30, v138\n"
"v_accvgpr_write_b32 a31, v139\n"
"v_mfma_f32_16x16x32_bf16 v[212:215], a[164:167], a[252:255], v[212:215]\n"
"v_mul_f32 v128, v128, v10\n"
"v_mfma_f32_16x16x32_bf16 v[212:215], a[160:163], a[248:251], v[212:215]\n"
"v_mul_f32 v129, v129, v10\n"
"v_mul_f32 v130, v130, v10\n"
"v_mfma_f32_16x16x32_bf16 v[216:219], a[168:171], a[248:251], v[216:219]\n"
"s_add_u32 s96, s97, 31872\n"
"s_add_u32 m0, s94, 7168\n"
"s_nop 0\n"
"buffer_load_dwordx4 v252, s[36:39], s96 offen nt lds\n"
"v_mul_f32 v131, v131, v10\n"
"v_mfma_f32_16x16x32_bf16 v[216:219], a[172:175], a[252:255], v[216:219]\n"
"v_mul_f32 v132, v132, v10\n"
"v_mul_f32 v133, v133, v10\n"
"v_mfma_f32_16x16x32_bf16 v[220:223], a[180:183], a[252:255], v[220:223]\n"
"ds_read_b128 a[140:143], v4 offset:12288\n"
"v_mul_f32 v134, v134, v10\n"
"v_mfma_f32_16x16x32_bf16 v[220:223], a[176:179], a[248:251], v[220:223]\n"
"v_mul_f32 v135, v135, v10\n"
"v_cvt_pk_bf16_f32 v128, v128, v129\n"
"v_mfma_f32_16x16x32_bf16 v[224:227], a[184:187], a[248:251], v[224:227]\n"
"v_cvt_pk_bf16_f32 v129, v130, v131\n"
"v_mfma_f32_16x16x32_bf16 v[224:227], a[188:191], a[252:255], v[224:227]\n"
"v_cvt_pk_bf16_f32 v130, v132, v133\n"
"v_cvt_pk_bf16_f32 v131, v134, v135\n"
"v_mfma_f32_16x16x32_bf16 v[228:231], a[196:199], a[252:255], v[228:231]\n"
"v_accvgpr_write_b32 a24, v128\n"
"v_mfma_f32_16x16x32_bf16 v[228:231], a[192:195], a[248:251], v[228:231]\n"
"v_accvgpr_write_b32 a25, v129\n"
"v_accvgpr_write_b32 a26, v130\n"
"v_mfma_f32_16x16x32_bf16 v[232:235], a[200:203], a[248:251], v[232:235]\n"
"v_accvgpr_write_b32 a27, v131\n"
"v_mfma_f32_16x16x32_bf16 v[232:235], a[204:207], a[252:255], v[232:235]\n"
"v_mul_f32 v120, v120, v10\n"
"v_mul_f32 v121, v121, v10\n"
"v_mfma_f32_16x16x32_bf16 v[236:239], a[212:215], a[252:255], v[236:239]\n"
"v_mul_f32 v122, v122, v10\n"
"v_mfma_f32_16x16x32_bf16 v[236:239], a[208:211], a[248:251], v[236:239]\n"
"s_waitcnt vmcnt(8)\n"
"s_barrier\n"
"s_cmp_ge_u32 s25, 1020\n"
"s_cbranch_scc0 Lld2_%=\n"
"v_mov_b32 v48, v44\n"
"v_add_u32 v49, 1, v40\n"
"v_mov_b32 v50, v45\n"
"v_add_u32 v51, 1, v41\n"
"v_mov_b32 v52, v46\n"
"v_add_u32 v53, 1, v42\n"
"v_mov_b32 v54, v47\n"
"v_add_u32 v55, 1, v43\n"
"v_lshlrev_b32 v57, 1, v56\n"
"v_add_u32 v57, 24576, v57\n"
"buffer_store_dwordx4 v[48:51], v57, s[64:67], 0 offen sc1\n"
"buffer_store_dwordx4 v[52:55], v57, s[64:67], 0 offen offset:16 sc1\n"
"Lld2_%=:\n"
"v_mul_f32 v123, v123, v10\n"
"s_waitcnt lgkmcnt(8)\n"
"v_mfma_f32_16x16x32_bf16 v[16:19], a[80:83], a[0:3], 0\n"
"s_lshl_b32 s96, %[wid], 5\n"
"s_add_u32 s96, s96, 5120\n"
"buffer_load_dwordx4 a[248:251], v253, s[36:39], s96 offen nt\n"
"v_mul_f32 v124, v124, v10\n"
"v_mfma_f32_16x16x32_bf16 v[16:19], a[84:87], a[4:7], v[16:19]\n"
"ds_read_b64_tr_b16 a[144:145], v5 offset:0\n"
"v_mul_f32 v125, v125, v10\n"
"v_mfma_f32_16x16x32_bf16 v[16:19], a[88:91], a[8:11], v[16:19]\n"
"ds_read_b64_tr_b16 a[146:147], v5 offset:4096\n"
"v_mul_f32 v126, v126, v10\n"
"v_mfma_f32_16x16x32_bf16 v[16:19], a[92:95], a[12:15], v[16:19]\n"
"ds_read_b64_tr_b16 a[176:177], v5 offset:8192\n"
"v_mul_f32 v127, v127, v10\n"
"v_mfma_f32_16x16x32_bf16 v[20:23], a[108:111], a[12:15], 0\n"
"ds_read_b64_tr_b16 a[178:179], v5 offset:12288\n"
"v_cvt_pk_bf16_f32 v120, v120, v121\n"
"v_mfma_f32_16x16x32_bf16 v[20:23], a[104:107], a[8:11], v[20:23]\n"
"ds_read_b64_tr_b16 a[148:149], v6 offset:0\n"
"v_cvt_pk_bf16_f32 v121, v122, v123\n"
"v_mfma_f32_16x16x32_bf16 v[20:23], a[100:103], a[4:7], v[20:23]\n"
"ds_read_b64_tr_b16 a[150:151], v6 offset:4096\n"
"v_cvt_pk_bf16_f32 v122, v124, v125\n"
"v_mfma_f32_16x16x32_bf16 v[20:23], a[96:99], a[0:3], v[20:23]\n"
"s_waitcnt lgkmcnt(6)\n"
"ds_read_b64_tr_b16 a[180:181], v6 offset:8192\n"
"v_cvt_pk_bf16_f32 v123, v126, v127\n"
"v_accvgpr_write_b32 a20, v120\n"
"v_accvgpr_write_b32 a21, v121\n"
"v_accvgpr_write_b32 a22, v122\n"
"v_accvgpr_write_b32 a23, v123\n"
"v_mul_f32 v112, v112, v10\n"
"v_mul_f32 v113, v113, v10\n"
"v_mul_f32 v114, v114, v10\n"
"v_mul_f32 v115, v115, v10\n"
"v_mul_f32 v116, v116, v10\n"
"v_mul_f32 v117, v117, v10\n"
"v_mul_f32 v118, v118, v10\n"
"v_mul_f32 v119, v119, v10\n"
"v_cvt_pk_bf16_f32 v112, v112, v113\n"
"v_cvt_pk_bf16_f32 v113, v114, v115\n"
"v_cvt_pk_bf16_f32 v114, v116, v117\n"
"v_cvt_pk_bf16_f32 v115, v118, v119\n"
"v_accvgpr_write_b32 a16, v112\n"
"v_accvgpr_write_b32 a17, v113\n"
"v_accvgpr_write_b32 a18, v114\n"
"v_accvgpr_write_b32 a19, v115\n"
"v_max3_f32 v40, v16, v17, v18\n"
"v_max3_f32 v41, v19, v20, v21\n"
"v_max3_f32 v40, v40, v22, v23\n"
"v_max_f32 v40, v40, v41\n"
"v_mov_b32 v41, v40\n"
"v_accvgpr_write_b32 a208, v35\n"
"v_accvgpr_write_b32 a209, v35\n"
"v_accvgpr_write_b32 a210, v35\n"
"v_accvgpr_write_b32 a211, v35\n"
"v_pk_mul_f32 v[144:145], v[144:145], v[10:11]\n"
"v_pk_mul_f32 v[146:147], v[146:147], v[10:11]\n"
"v_pk_mul_f32 v[148:149], v[148:149], v[10:11]\n"
"v_pk_mul_f32 v[150:151], v[150:151], v[10:11]\n"
"v_cvt_pk_bf16_f32 v144, v144, v145\n"
"v_cvt_pk_bf16_f32 v145, v146, v147\n"
"v_cvt_pk_bf16_f32 v146, v148, v149\n"
"v_cvt_pk_bf16_f32 v147, v150, v151\n"
"v_accvgpr_write_b32 a32, v144\n"
"v_accvgpr_write_b32 a33, v145\n"
"v_accvgpr_write_b32 a34, v146\n"
"v_accvgpr_write_b32 a35, v147\n"
"v_pk_mul_f32 v[152:153], v[152:153], v[10:11]\n"
"v_pk_mul_f32 v[154:155], v[154:155], v[10:11]\n"
"v_pk_mul_f32 v[156:157], v[156:157], v[10:11]\n"
"v_pk_mul_f32 v[158:159], v[158:159], v[10:11]\n"
"v_cvt_pk_bf16_f32 v152, v152, v153\n"
"v_cvt_pk_bf16_f32 v153, v154, v155\n"
"v_cvt_pk_bf16_f32 v154, v156, v157\n"
"v_cvt_pk_bf16_f32 v155, v158, v159\n"
"v_accvgpr_write_b32 a36, v152\n"
"v_accvgpr_write_b32 a37, v153\n"
"v_accvgpr_write_b32 a38, v154\n"
"v_accvgpr_write_b32 a39, v155\n"
"v_pk_mul_f32 v[160:161], v[160:161], v[10:11]\n"
"v_pk_mul_f32 v[162:163], v[162:163], v[10:11]\n"
"v_pk_mul_f32 v[164:165], v[164:165], v[10:11]\n"
"v_pk_mul_f32 v[166:167], v[166:167], v[10:11]\n"
"v_cvt_pk_bf16_f32 v160, v160, v161\n"
"v_cvt_pk_bf16_f32 v161, v162, v163\n"
"v_cvt_pk_bf16_f32 v162, v164, v165\n"
"v_cvt_pk_bf16_f32 v163, v166, v167\n"
"v_accvgpr_write_b32 a40, v160\n"
"v_accvgpr_write_b32 a41, v161\n"
"v_accvgpr_write_b32 a42, v162\n"
"v_accvgpr_write_b32 a43, v163\n"
"v_pk_mul_f32 v[168:169], v[168:169], v[10:11]\n"
"v_pk_mul_f32 v[170:171], v[170:171], v[10:11]\n"
"v_pk_mul_f32 v[172:173], v[172:173], v[10:11]\n"
"v_pk_mul_f32 v[174:175], v[174:175], v[10:11]\n"
"v_cvt_pk_bf16_f32 v168, v168, v169\n"
"v_cvt_pk_bf16_f32 v169, v170, v171\n"
"v_cvt_pk_bf16_f32 v170, v172, v173\n"
"v_cvt_pk_bf16_f32 v171, v174, v175\n"
"v_accvgpr_write_b32 a44, v168\n"
"v_accvgpr_write_b32 a45, v169\n"
"v_accvgpr_write_b32 a46, v170\n"
"v_accvgpr_write_b32 a47, v171\n"
"v_permlane16_swap_b32 v40, v41\n"
"v_max_f32 v40, v40, v41\n"
"v_mov_b32 v41, v40\n"
"ds_read_b64_tr_b16 a[182:183], v6 offset:12288\n"
"ds_read_b64_tr_b16 a[152:153], v5 offset:512\n"
"v_permlane32_swap_b32 v40, v41\n"
"v_min_f32_e64 v40, -v40, -v41\n"
"v_mov_b32 v41, v40\n"
"v_mov_b32 v42, v40\n"
"v_mov_b32 v43, v40\n"
"v_add_f32 v16, v16, v40\n"
"v_add_f32 v17, v17, v40\n"
"v_mfma_f32_16x16x32_bf16 v[24:27], a[112:115], a[0:3], v[40:43]\n"
"ds_read_b64_tr_b16 a[154:155], v5 offset:4608\n"
"v_add_f32 v18, v18, v40\n"
"v_mfma_f32_16x16x32_bf16 v[24:27], a[116:119], a[4:7], v[24:27]\n"
"ds_read_b64_tr_b16 a[184:185], v5 offset:8704\n"
"v_add_f32 v19, v19, v40\n"
"v_mfma_f32_16x16x32_bf16 v[24:27], a[120:123], a[8:11], v[24:27]\n"
"ds_read_b64_tr_b16 a[186:187], v5 offset:12800\n"
"v_add_f32 v20, v20, v40\n"
"v_mfma_f32_16x16x32_bf16 v[24:27], a[124:127], a[12:15], v[24:27]\n"
"ds_read_b64_tr_b16 a[156:157], v6 offset:512\n"
"v_add_f32 v21, v21, v40\n"
"v_mfma_f32_16x16x32_bf16 v[28:31], a[140:143], a[12:15], v[40:43]\n"
"ds_read_b64_tr_b16 a[158:159], v6 offset:4608\n"
"v_add_f32 v22, v22, v40\n"
"v_mfma_f32_16x16x32_bf16 v[28:31], a[136:139], a[8:11], v[28:31]\n"
"ds_read_b64_tr_b16 a[188:189], v6 offset:8704\n"
"v_add_f32 v23, v23, v40\n"
"v_mfma_f32_16x16x32_bf16 v[28:31], a[132:135], a[4:7], v[28:31]\n"
"ds_read_b64_tr_b16 a[190:191], v6 offset:12800\n"
"v_mfma_f32_16x16x32_bf16 v[28:31], a[128:131], a[0:3], v[28:31]\n"
"s_add_u32 m0, s84, 16384\n"
"s_nop 0\n"
"buffer_load_dwordx4 v8, s[48:51], s86 offen offset:0 nt lds\n"
"v_mfma_f32_16x16x32_bf16 v[112:115], a[80:83], a[16:19], 0\n"
"ds_read_b64_tr_b16 a[160:161], v5 offset:1024\n"
"v_mfma_f32_16x16x32_bf16 v[112:115], a[84:87], a[20:23], v[112:115]\n"
"ds_read_b64_tr_b16 a[162:163], v5 offset:5120\n"
"v_mfma_f32_16x16x32_bf16 v[112:115], a[88:91], a[24:27], v[112:115]\n"
"ds_read_b64_tr_b16 a[192:193], v5 offset:9216\n"
"v_mfma_f32_16x16x32_bf16 v[112:115], a[92:95], a[28:31], v[112:115]\n"
"ds_read_b64_tr_b16 a[194:195], v5 offset:13312\n"
"v_mfma_f32_16x16x32_bf16 v[116:119], a[108:111], a[28:31], 0\n"
"ds_read_b64_tr_b16 a[164:165], v6 offset:1024\n"
"v_mfma_f32_16x16x32_bf16 v[116:119], a[104:107], a[24:27], v[116:119]\n"
"ds_read_b64_tr_b16 a[166:167], v6 offset:5120\n"
"v_mfma_f32_16x16x32_bf16 v[116:119], a[100:103], a[20:23], v[116:119]\n"
"ds_read_b64_tr_b16 a[196:197], v6 offset:9216\n"
"v_mfma_f32_16x16x32_bf16 v[116:119], a[96:99], a[16:19], v[116:119]\n"
"ds_read_b64_tr_b16 a[198:199], v6 offset:13312\n"
"v_max3_f32 v44, v112, v113, v114\n"
"buffer_load_dwordx4 v8, s[48:51], s87 offen offset:1024 nt lds\n"
"ds_read_b64_tr_b16 a[168:169], v5 offset:1536\n"
"ds_read_b64_tr_b16 a[170:171], v5 offset:5632\n"
"ds_read_b64_tr_b16 a[200:201], v5 offset:9728\n"
"ds_read_b64_tr_b16 a[202:203], v5 offset:13824\n"
"ds_read_b64_tr_b16 a[172:173], v6 offset:1536\n"
"v_max3_f32 v45, v115, v116, v117\n"
"v_max3_f32 v44, v44, v118, v119\n"
"v_max_f32 v44, v44, v45\n"
"v_mov_b32 v45, v44\n"
"v_exp_f32 v16, v16\n"
"v_exp_f32 v17, v17\n"
"v_exp_f32 v18, v18\n"
"v_exp_f32 v19, v19\n"
"v_cvt_pk_bf16_f32 v32, v16, v17\n"
"v_exp_f32 v20, v20\n"
"v_exp_f32 v21, v21\n"
"v_cvt_pk_bf16_f32 v33, v18, v19\n"
"v_exp_f32 v22, v22\n"
"v_exp_f32 v23, v23\n"
"v_cvt_pk_bf16_f32 v34, v20, v21\n"
"v_cvt_pk_bf16_f32 v35, v22, v23\n"
"v_permlane16_swap_b32 v44, v45\n"
"v_max_f32 v44, v44, v45\n"
"v_mfma_f32_16x16x32_bf16 v[60:63], a[208:211], v[32:35], 0\n"
"s_waitcnt lgkmcnt(15)\n"
"ds_read_b64_tr_b16 a[174:175], v6 offset:5632\n"
"v_mov_b32 v45, v44\n"
"v_mfma_f32_16x16x32_bf16 v[80:83], a[144:147], v[32:35], 0\n"
"ds_read_b64_tr_b16 a[204:205], v6 offset:9728\n"
"v_permlane32_swap_b32 v44, v45\n"
"v_min_f32_e64 v44, -v44, -v45\n"
"v_mov_b32 v45, v44\n"
"v_mov_b32 v46, v44\n"
"v_mov_b32 v47, v44\n"
"v_add_f32 v112, v112, v44\n"
"v_add_f32 v113, v113, v44\n"
"v_add_f32 v114, v114, v44\n"
"v_add_f32 v115, v115, v44\n"
"v_add_f32 v116, v116, v44\n"
"v_add_f32 v117, v117, v44\n"
"v_add_f32 v118, v118, v44\n"
"v_add_f32 v119, v119, v44\n"
"v_exp_f32 v24, v24\n"
"v_exp_f32 v25, v25\n"
"v_exp_f32 v26, v26\n"
"v_exp_f32 v27, v27\n"
"v_cvt_pk_bf16_f32 v36, v24, v25\n"
"v_exp_f32 v28, v28\n"
"v_exp_f32 v29, v29\n"
"v_cvt_pk_bf16_f32 v37, v26, v27\n"
"v_exp_f32 v30, v30\n"
"v_exp_f32 v31, v31\n"
"v_cvt_pk_bf16_f32 v38, v28, v29\n"
"v_cvt_pk_bf16_f32 v39, v30, v31\n"
"v_pk_mul_f32 v[200:201], v[200:201], v[10:11]\n"
"v_pk_mul_f32 v[202:203], v[202:203], v[10:11]\n"
"v_mfma_f32_16x16x32_bf16 v[80:83], a[176:179], v[36:39], v[80:83]\n"
"ds_read_b64_tr_b16 a[206:207], v6 offset:13824\n"
"v_mfma_f32_16x16x32_bf16 v[84:87], a[180:183], v[36:39], 0\n"
"buffer_load_dwordx4 v8, s[48:51], s86 offen offset:2048 nt lds\n"
"v_mfma_f32_16x16x32_bf16 v[84:87], a[148:151], v[32:35], v[84:87]\n"
"v_mul_f32 v204, v204, v10\n"
"v_mul_f32 v205, v205, v10\n"
"v_mfma_f32_16x16x32_bf16 v[88:91], a[152:155], v[32:35], 0\n"
"v_mul_f32 v206, v206, v10\n"
"v_mul_f32 v207, v207, v10\n"
"v_mfma_f32_16x16x32_bf16 v[88:91], a[184:187], v[36:39], v[88:91]\n"
"s_waitcnt lgkmcnt(15)\n"
"v_cvt_pk_bf16_f32 v200, v200, v201\n"
"v_cvt_pk_bf16_f32 v201, v202, v203\n"
"v_mfma_f32_16x16x32_bf16 v[92:95], a[188:191], v[36:39], 0\n"
"v_cvt_pk_bf16_f32 v202, v204, v205\n"
"v_cvt_pk_bf16_f32 v203, v206, v207\n"
"v_mfma_f32_16x16x32_bf16 v[92:95], a[156:159], v[32:35], v[92:95]\n"
"v_accvgpr_write_b32 a60, v200\n"
"v_accvgpr_write_b32 a61, v201\n"
"v_mfma_f32_16x16x32_bf16 v[24:27], a[112:115], a[16:19], v[44:47]\n"
"v_accvgpr_write_b32 a62, v202\n"
"v_accvgpr_write_b32 a63, v203\n"
"v_mfma_f32_16x16x32_bf16 v[24:27], a[116:119], a[20:23], v[24:27]\n"
"v_mul_f32 v192, v192, v10\n"
"v_mul_f32 v193, v193, v10\n"
"v_mfma_f32_16x16x32_bf16 v[24:27], a[120:123], a[24:27], v[24:27]\n"
"v_mul_f32 v194, v194, v10\n"
"v_mul_f32 v195, v195, v10\n"
"v_mfma_f32_16x16x32_bf16 v[24:27], a[124:127], a[28:31], v[24:27]\n"
"buffer_load_dwordx4 v8, s[48:51], s87 offen offset:3072 nt lds\n"
"s_add_u32 s86, s86, 16384\n"
"s_add_u32 s87, s87, 16384\n"
"v_mfma_f32_16x16x32_bf16 v[28:31], a[140:143], a[28:31], v[44:47]\n"
"v_mul_f32 v196, v196, v10\n"
"v_mul_f32 v197, v197, v10\n"
"v_mfma_f32_16x16x32_bf16 v[28:31], a[136:139], a[24:27], v[28:31]\n"
"v_mul_f32 v198, v198, v10\n"
"v_mul_f32 v199, v199, v10\n"
"v_mfma_f32_16x16x32_bf16 v[28:31], a[132:135], a[20:23], v[28:31]\n"
"v_cvt_pk_bf16_f32 v192, v192, v193\n"
"v_cvt_pk_bf16_f32 v193, v194, v195\n"
"v_mfma_f32_16x16x32_bf16 v[28:31], a[128:131], a[16:19], v[28:31]\n"
"v_cvt_pk_bf16_f32 v194, v196, v197\n"
"v_cvt_pk_bf16_f32 v195, v198, v199\n"
"v_mfma_f32_16x16x32_bf16 v[144:147], a[80:83], a[32:35], 0\n"
"v_accvgpr_write_b32 a56, v192\n"
"v_accvgpr_write_b32 a57, v193\n"
"v_mfma_f32_16x16x32_bf16 v[144:147], a[84:87], a[36:39], v[144:147]\n"
"v_accvgpr_write_b32 a58, v194\n"
"v_accvgpr_write_b32 a59, v195\n"
"v_mfma_f32_16x16x32_bf16 v[144:147], a[88:91], a[40:43], v[144:147]\n"
"v_mul_f32 v184, v184, v10\n"
"v_mul_f32 v185, v185, v10\n"
"v_mfma_f32_16x16x32_bf16 v[144:147], a[92:95], a[44:47], v[144:147]\n"
"v_mul_f32 v186, v186, v10\n"
"v_mul_f32 v187, v187, v10\n"
"v_mfma_f32_16x16x32_bf16 v[148:151], a[108:111], a[44:47], 0\n"
"s_add_u32 m0, s83, 32768\n"
"s_nop 0\n"
"buffer_load_dwordx4 v240, s[44:47], s85 offen offset:0 nt lds\n"
"v_mfma_f32_16x16x32_bf16 v[148:151], a[104:107], a[40:43], v[148:151]\n"
"v_mul_f32 v188, v188, v10\n"
"v_mul_f32 v189, v189, v10\n"
"v_mfma_f32_16x16x32_bf16 v[148:151], a[100:103], a[36:39], v[148:151]\n"
"v_mul_f32 v190, v190, v10\n"
"v_mul_f32 v191, v191, v10\n"
"v_mfma_f32_16x16x32_bf16 v[148:151], a[96:99], a[32:35], v[148:151]\n"
"v_cvt_pk_bf16_f32 v184, v184, v185\n"
"v_cvt_pk_bf16_f32 v185, v186, v187\n"
"v_mfma_f32_16x16x32_bf16 v[60:63], a[208:211], v[36:39], v[60:63]\n"
"s_waitcnt lgkmcnt(12)\n"
"v_cvt_pk_bf16_f32 v186, v188, v189\n"
"v_cvt_pk_bf16_f32 v187, v190, v191\n"
"v_mfma_f32_16x16x32_bf16 v[96:99], a[192:195], v[36:39], 0\n"
"v_accvgpr_write_b32 a52, v184\n"
"v_accvgpr_write_b32 a53, v185\n"
"v_mfma_f32_16x16x32_bf16 v[96:99], a[160:163], v[32:35], v[96:99]\n"
"s_waitcnt lgkmcnt(8)\n"
"v_accvgpr_write_b32 a54, v186\n"
"v_accvgpr_write_b32 a55, v187\n"
"v_mfma_f32_16x16x32_bf16 v[100:103], a[164:167], v[32:35], 0\n"
"v_mul_f32 v176, v176, v10\n"
"v_mul_f32 v177, v177, v10\n"
"v_mfma_f32_16x16x32_bf16 v[100:103], a[196:199], v[36:39], v[100:103]\n"
"s_waitcnt lgkmcnt(4)\n"
"v_mul_f32 v178, v178, v10\n"
"v_mul_f32 v179, v179, v10\n"
"v_mfma_f32_16x16x32_bf16 v[104:107], a[200:203], v[36:39], 0\n"
"buffer_load_dwordx4 v241, s[44:47], s85 offen offset:1024 nt lds\n"
"v_mfma_f32_16x16x32_bf16 v[104:107], a[168:171], v[32:35], v[104:107]\n"
"s_waitcnt lgkmcnt(0)\n"
"v_mul_f32 v180, v180, v10\n"
"v_mul_f32 v181, v181, v10\n"
"v_mfma_f32_16x16x32_bf16 v[108:111], a[172:175], v[32:35], 0\n"
"v_mul_f32 v182, v182, v10\n"
"v_mul_f32 v183, v183, v10\n"
"v_mfma_f32_16x16x32_bf16 v[108:111], a[204:207], v[36:39], v[108:111]\n"
"v_cvt_pk_bf16_f32 v176, v176, v177\n"
"v_cvt_pk_bf16_f32 v177, v178, v179\n"
"v_cvt_pk_bf16_f32 v178, v180, v181\n"
"v_cvt_pk_bf16_f32 v179, v182, v183\n"
"v_accvgpr_write_b32 a48, v176\n"
"v_accvgpr_write_b32 a49, v177\n"
"v_accvgpr_write_b32 a50, v178\n"
"v_accvgpr_write_b32 a51, v179\n"
"v_max3_f32 v48, v144, v145, v146\n"
"v_max3_f32 v49, v147, v148, v149\n"
"v_max3_f32 v48, v48, v150, v151\n"
"v_max_f32 v48, v48, v49\n"
"v_mov_b32 v49, v48\n"
"v_exp_f32 v16, v112\n"
"v_exp_f32 v17, v113\n"
"v_exp_f32 v18, v114\n"
"v_exp_f32 v19, v115\n"
"v_cvt_pk_bf16_f32 v244, v16, v17\n"
"v_exp_f32 v20, v116\n"
"v_exp_f32 v21, v117\n"
"v_cvt_pk_bf16_f32 v245, v18, v19\n"
"v_exp_f32 v22, v118\n"
"v_exp_f32 v23, v119\n"
"v_cvt_pk_bf16_f32 v246, v20, v21\n"
"v_cvt_pk_bf16_f32 v247, v22, v23\n"
"v_permlane16_swap_b32 v48, v49\n"
"v_max_f32 v48, v48, v49\n"
"v_mfma_f32_16x16x32_bf16 v[64:67], a[208:211], v[244:247], 0\n"
"v_mov_b32 v49, v48\n"
"v_mfma_f32_16x16x32_bf16 v[112:115], a[144:147], v[244:247], 0\n"
"s_nop 0\n"
"v_permlane32_swap_b32 v48, v49\n"
"v_min_f32_e64 v48, -v48, -v49\n"
"v_mov_b32 v49, v48\n"
"v_mov_b32 v50, v48\n"
"v_mov_b32 v51, v48\n"
"v_add_f32 v144, v144, v48\n"
"v_add_f32 v145, v145, v48\n"
"v_add_f32 v146, v146, v48\n"
"v_add_f32 v147, v147, v48\n"
"v_add_f32 v148, v148, v48\n"
"v_add_f32 v149, v149, v48\n"
"v_add_f32 v150, v150, v48\n"
"v_add_f32 v151, v151, v48\n"
"v_exp_f32 v24, v24\n"
"v_exp_f32 v25, v25\n"
"v_exp_f32 v26, v26\n"
"v_exp_f32 v27, v27\n"
"v_cvt_pk_bf16_f32 v248, v24, v25\n"
"v_exp_f32 v28, v28\n"
"v_exp_f32 v29, v29\n"
"v_cvt_pk_bf16_f32 v249, v26, v27\n"
"v_exp_f32 v30, v30\n"
"v_exp_f32 v31, v31\n"
"v_cvt_pk_bf16_f32 v250, v28, v29\n"
"v_cvt_pk_bf16_f32 v251, v30, v31\n"
"v_pk_mul_f32 v[208:209], v[208:209], v[10:11]\n"
"v_pk_mul_f32 v[210:211], v[210:211], v[10:11]\n"
"v_mfma_f32_16x16x32_bf16 v[112:115], a[176:179], v[248:251], v[112:115]\n"
"v_mul_f32 v212, v212, v10\n"
"v_mul_f32 v213, v213, v10\n"
"v_mfma_f32_16x16x32_bf16 v[116:119], a[180:183], v[248:251], 0\n"
"v_mul_f32 v214, v214, v10\n"
"v_mul_f32 v215, v215, v10\n"
"v_mfma_f32_16x16x32_bf16 v[116:119], a[148:151], v[244:247], v[116:119]\n"
"v_cvt_pk_bf16_f32 v208, v208, v209\n"
"v_cvt_pk_bf16_f32 v209, v210, v211\n"
"v_mfma_f32_16x16x32_bf16 v[120:123], a[152:155], v[244:247], 0\n"
"buffer_load_dwordx4 v242, s[44:47], s85 offen offset:2048 nt lds\n"
"v_cvt_pk_bf16_f32 v210, v212, v213\n"
"v_mfma_f32_16x16x32_bf16 v[120:123], a[184:187], v[248:251], v[120:123]\n"
"v_cvt_pk_bf16_f32 v211, v214, v215\n"
"v_accvgpr_write_b32 a64, v208\n"
"v_mfma_f32_16x16x32_bf16 v[124:127], a[188:191], v[248:251], 0\n"
"v_accvgpr_write_b32 a65, v209\n"
"v_accvgpr_write_b32 a66, v210\n"
"v_mfma_f32_16x16x32_bf16 v[124:127], a[156:159], v[244:247], v[124:127]\n"
"v_accvgpr_write_b32 a67, v211\n"
"v_mfma_f32_16x16x32_bf16 v[24:27], a[112:115], a[32:35], v[48:51]\n"
"v_mul_f32 v216, v216, v10\n"
"v_mul_f32 v217, v217, v10\n"
"v_mfma_f32_16x16x32_bf16 v[24:27], a[116:119], a[36:39], v[24:27]\n"
"v_mul_f32 v218, v218, v10\n"
"v_mul_f32 v219, v219, v10\n"
"v_mfma_f32_16x16x32_bf16 v[24:27], a[120:123], a[40:43], v[24:27]\n"
"v_mul_f32 v220, v220, v10\n"
"v_mul_f32 v221, v221, v10\n"
"v_mfma_f32_16x16x32_bf16 v[24:27], a[124:127], a[44:47], v[24:27]\n"
"v_mul_f32 v222, v222, v10\n"
"v_mul_f32 v223, v223, v10\n"
"v_mfma_f32_16x16x32_bf16 v[28:31], a[140:143], a[44:47], v[48:51]\n"
"v_cvt_pk_bf16_f32 v216, v216, v217\n"
"v_cvt_pk_bf16_f32 v217, v218, v219\n"
"v_mfma_f32_16x16x32_bf16 v[28:31], a[136:139], a[40:43], v[28:31]\n"
"buffer_load_dwordx4 v243, s[44:47], s85 offen offset:3072 nt lds\n"
"s_add_u32 s85, s85, 16384\n"
"v_cvt_pk_bf16_f32 v218, v220, v221\n"
"v_mfma_f32_16x16x32_bf16 v[28:31], a[132:135], a[36:39], v[28:31]\n"
"v_cvt_pk_bf16_f32 v219, v222, v223\n"
"v_accvgpr_write_b32 a68, v216\n"
"v_mfma_f32_16x16x32_bf16 v[28:31], a[128:131], a[32:35], v[28:31]\n"
"v_accvgpr_write_b32 a69, v217\n"
"v_accvgpr_write_b32 a70, v218\n"
"v_mfma_f32_16x16x32_bf16 v[176:179], a[80:83], a[48:51], 0\n"
"v_accvgpr_write_b32 a71, v219\n"
"v_mfma_f32_16x16x32_bf16 v[176:179], a[84:87], a[52:55], v[176:179]\n"
"v_mul_f32 v224, v224, v10\n"
"v_mul_f32 v225, v225, v10\n"
"v_mfma_f32_16x16x32_bf16 v[176:179], a[88:91], a[56:59], v[176:179]\n"
"v_mul_f32 v226, v226, v10\n"
"v_mul_f32 v227, v227, v10\n"
"v_mfma_f32_16x16x32_bf16 v[176:179], a[92:95], a[60:63], v[176:179]\n"
"v_mul_f32 v228, v228, v10\n"
"v_mul_f32 v229, v229, v10\n"
"v_mfma_f32_16x16x32_bf16 v[180:183], a[108:111], a[60:63], 0\n"
"v_mul_f32 v230, v230, v10\n"
"v_mul_f32 v231, v231, v10\n"
"v_mfma_f32_16x16x32_bf16 v[180:183], a[104:107], a[56:59], v[180:183]\n"
"v_cvt_pk_bf16_f32 v224, v224, v225\n"
"v_cvt_pk_bf16_f32 v225, v226, v227\n"
"v_mfma_f32_16x16x32_bf16 v[180:183], a[100:103], a[52:55], v[180:183]\n"
"s_add_u32 m0, s84, 32768\n"
"s_nop 0\n"
"buffer_load_dwordx4 v8, s[48:51], s86 offen offset:0 nt lds\n"
"v_cvt_pk_bf16_f32 v226, v228, v229\n"
"v_mfma_f32_16x16x32_bf16 v[180:183], a[96:99], a[48:51], v[180:183]\n"
"v_cvt_pk_bf16_f32 v227, v230, v231\n"
"v_accvgpr_write_b32 a72, v224\n"
"v_mfma_f32_16x16x32_bf16 v[64:67], a[208:211], v[248:251], v[64:67]\n"
"v_accvgpr_write_b32 a73, v225\n"
"v_accvgpr_write_b32 a74, v226\n"
"v_mfma_f32_16x16x32_bf16 v[128:131], a[192:195], v[248:251], 0\n"
"v_accvgpr_write_b32 a75, v227\n"
"v_mfma_f32_16x16x32_bf16 v[128:131], a[160:163], v[244:247], v[128:131]\n"
"v_mul_f32 v232, v232, v10\n"
"v_mul_f32 v233, v233, v10\n"
"v_mfma_f32_16x16x32_bf16 v[132:135], a[164:167], v[244:247], 0\n"
"v_mul_f32 v234, v234, v10\n"
"v_mul_f32 v235, v235, v10\n"
"v_mfma_f32_16x16x32_bf16 v[132:135], a[196:199], v[248:251], v[132:135]\n"
"v_mul_f32 v236, v236, v10\n"
"v_mul_f32 v237, v237, v10\n"
"v_mfma_f32_16x16x32_bf16 v[136:139], a[200:203], v[248:251], 0\n"
"v_mul_f32 v238, v238, v10\n"
"v_mul_f32 v239, v239, v10\n"
"v_mfma_f32_16x16x32_bf16 v[136:139], a[168:171], v[244:247], v[136:139]\n"
"v_cvt_pk_bf16_f32 v232, v232, v233\n"
"v_cvt_pk_bf16_f32 v233, v234, v235\n"
"v_mfma_f32_16x16x32_bf16 v[140:143], a[172:175], v[244:247], 0\n"
"buffer_load_dwordx4 v8, s[48:51], s87 offen offset:1024 nt lds\n"
"v_cvt_pk_bf16_f32 v234, v236, v237\n"
"v_mfma_f32_16x16x32_bf16 v[140:143], a[204:207], v[248:251], v[140:143]\n"
"v_cvt_pk_bf16_f32 v235, v238, v239\n"
"v_accvgpr_write_b32 a76, v232\n"
"v_mfma_f32_16x16x32_bf16 v[208:211], a[80:83], a[64:67], 0\n"
"v_accvgpr_write_b32 a77, v233\n"
"v_accvgpr_write_b32 a78, v234\n"
"v_mfma_f32_16x16x32_bf16 v[208:211], a[84:87], a[68:71], v[208:211]\n"
"v_accvgpr_write_b32 a79, v235\n"
"v_max3_f32 v52, v176, v177, v178\n"
"v_mfma_f32_16x16x32_bf16 v[208:211], a[88:91], a[72:75], v[208:211]\n"
"v_max3_f32 v53, v179, v180, v181\n"
"v_max3_f32 v52, v52, v182, v183\n"
"v_mfma_f32_16x16x32_bf16 v[208:211], a[92:95], a[76:79], v[208:211]\n"
"v_max_f32 v52, v52, v53\n"
"v_mov_b32 v53, v52\n"
"v_mfma_f32_16x16x32_bf16 v[212:215], a[108:111], a[76:79], 0\n"
"v_exp_f32 v16, v144\n"
"v_mfma_f32_16x16x32_bf16 v[212:215], a[104:107], a[72:75], v[212:215]\n"
"v_exp_f32 v17, v145\n"
"v_mfma_f32_16x16x32_bf16 v[212:215], a[100:103], a[68:71], v[212:215]\n"
"v_exp_f32 v18, v146\n"
"v_mfma_f32_16x16x32_bf16 v[212:215], a[96:99], a[64:67], v[212:215]\n"
"buffer_load_dwordx4 v8, s[48:51], s86 offen offset:2048 nt lds\n"
"v_exp_f32 v19, v147\n"
"v_cvt_pk_bf16_f32 v32, v16, v17\n"
"v_exp_f32 v20, v148\n"
"v_exp_f32 v21, v149\n"
"v_cvt_pk_bf16_f32 v33, v18, v19\n"
"v_exp_f32 v22, v150\n"
"v_exp_f32 v23, v151\n"
"v_cvt_pk_bf16_f32 v34, v20, v21\n"
"v_cvt_pk_bf16_f32 v35, v22, v23\n"
"v_permlane16_swap_b32 v52, v53\n"
"v_max_f32 v52, v52, v53\n"
"v_mov_b32 v53, v52\n"
"s_nop 0\n"
"s_nop 0\n"
"v_permlane32_swap_b32 v52, v53\n"
"v_min_f32_e64 v52, -v52, -v53\n"
"v_mov_b32 v53, v52\n"
"v_mov_b32 v54, v52\n"
"v_mov_b32 v55, v52\n"
"v_add_f32 v176, v176, v52\n"
"v_add_f32 v177, v177, v52\n"
"v_add_f32 v178, v178, v52\n"
"v_add_f32 v179, v179, v52\n"
"v_add_f32 v180, v180, v52\n"
"v_add_f32 v181, v181, v52\n"
"v_add_f32 v182, v182, v52\n"
"v_add_f32 v183, v183, v52\n"
"v_exp_f32 v24, v24\n"
"v_exp_f32 v25, v25\n"
"v_exp_f32 v26, v26\n"
"v_exp_f32 v27, v27\n"
"v_cvt_pk_bf16_f32 v36, v24, v25\n"
"v_exp_f32 v28, v28\n"
"v_exp_f32 v29, v29\n"
"v_cvt_pk_bf16_f32 v37, v26, v27\n"
"v_mfma_f32_16x16x32_bf16 v[24:27], a[112:115], a[48:51], v[52:55]\n"
"v_exp_f32 v30, v30\n"
"v_mfma_f32_16x16x32_bf16 v[24:27], a[116:119], a[52:55], v[24:27]\n"
"v_exp_f32 v31, v31\n"
"v_mfma_f32_16x16x32_bf16 v[24:27], a[120:123], a[56:59], v[24:27]\n"
"v_cvt_pk_bf16_f32 v38, v28, v29\n"
"v_cvt_pk_bf16_f32 v39, v30, v31\n"
"v_mfma_f32_16x16x32_bf16 v[24:27], a[124:127], a[60:63], v[24:27]\n"
"v_max3_f32 v56, v208, v209, v210\n"
"v_max3_f32 v57, v211, v212, v213\n"
"v_mfma_f32_16x16x32_bf16 v[28:31], a[140:143], a[60:63], v[52:55]\n"
"v_max3_f32 v56, v56, v214, v215\n"
"v_max_f32 v56, v56, v57\n"
"v_mfma_f32_16x16x32_bf16 v[28:31], a[136:139], a[56:59], v[28:31]\n"
"v_mov_b32 v57, v56\n"
"v_mfma_f32_16x16x32_bf16 v[28:31], a[132:135], a[52:55], v[28:31]\n"
"v_mfma_f32_16x16x32_bf16 v[28:31], a[128:131], a[48:51], v[28:31]\n"
"v_permlane16_swap_b32 v56, v57\n"
"v_max_f32 v56, v56, v57\n"
"v_mfma_f32_16x16x32_bf16 v[68:71], a[208:211], v[32:35], 0\n"
"buffer_load_dwordx4 v8, s[48:51], s87 offen offset:3072 nt lds\n"
"s_add_u32 s86, s86, 16384\n"
"s_add_u32 s87, s87, 16384\n"
"v_mov_b32 v57, v56\n"
"v_mfma_f32_16x16x32_bf16 v[144:147], a[144:147], v[32:35], 0\n"
"v_mfma_f32_16x16x32_bf16 v[144:147], a[176:179], v[36:39], v[144:147]\n"
"v_permlane32_swap_b32 v56, v57\n"
"v_min_f32_e64 v56, -v56, -v57\n"
"v_mfma_f32_16x16x32_bf16 v[148:151], a[180:183], v[36:39], 0\n"
"v_mov_b32 v57, v56\n"
"v_mov_b32 v58, v56\n"
"v_mfma_f32_16x16x32_bf16 v[148:151], a[148:151], v[32:35], v[148:151]\n"
"v_mov_b32 v59, v56\n"
"v_mfma_f32_16x16x32_bf16 v[152:155], a[152:155], v[32:35], 0\n"
"v_exp_f32 v16, v176\n"
"v_mfma_f32_16x16x32_bf16 v[152:155], a[184:187], v[36:39], v[152:155]\n"
"v_exp_f32 v17, v177\n"
"v_mfma_f32_16x16x32_bf16 v[156:159], a[188:191], v[36:39], 0\n"
"v_exp_f32 v18, v178\n"
"v_mfma_f32_16x16x32_bf16 v[156:159], a[156:159], v[32:35], v[156:159]\n"
"v_exp_f32 v19, v179\n"
"v_cvt_pk_bf16_f32 v244, v16, v17\n"
"v_exp_f32 v20, v180\n"
"v_exp_f32 v21, v181\n"
"v_cvt_pk_bf16_f32 v245, v18, v19\n"
"v_exp_f32 v22, v182\n"
"v_exp_f32 v23, v183\n"
"v_cvt_pk_bf16_f32 v246, v20, v21\n"
"v_cvt_pk_bf16_f32 v247, v22, v23\n"
"v_mov_b32 v11, 0x53800000\n"
"s_mov_b64 s[98:99], 0\n"
"s_sub_u32 s96, s94, 65536\n"
"v_add_u32 v252, s96, v5\n"
"v_add_u32 v253, s96, v6\n"
"v_add_u32 v254, s94, v9\n"
"v_lshrrev_b32 v255, 2, v0\n"
"v_lshlrev_b32 v255, 5, v255\n"
"v_and_b32 v12, 3, v0\n"
"v_lshl_add_u32 v255, v12, 3, v255\n"
"v_add_u32 v255, s94, v255\n"
"v_mfma_f32_16x16x32_bf16 v[16:19], a[80:83], a[64:67], v[56:59]\n"
"s_waitcnt vmcnt(8) lgkmcnt(0)\n"
"s_barrier\n"
"s_cmp_ge_u32 s25, 1020\n"
"s_cbranch_scc0 Lld3_%=\n"
"s_lshl_b32 s96, %[wid], 10\n"
"v_add_u32 v13, s96, v9\n"
"v_add_u32 v12, 16384, v13\n"
"buffer_load_dwordx4 a[252:255], v12, s[64:67], 0 offen sc1\n"
"v_lshlrev_b32 v13, 1, v13\n"
"v_add_u32 v13, 24576, v13\n"
"Lld3_%=:\n"
"v_exp_f32 v24, v24\n"
"v_mfma_f32_16x16x32_bf16 v[16:19], a[84:87], a[68:71], v[16:19]\n"
"ds_read_b128 a[80:83], v1 offset:16384\n"
"v_mfma_f32_16x16x32_bf16 v[16:19], a[88:91], a[72:75], v[16:19]\n"
"v_exp_f32 v25, v25\n"
"v_mfma_f32_16x16x32_bf16 v[16:19], a[92:95], a[76:79], v[16:19]\n"
"ds_read_b128 a[84:87], v2 offset:16384\n"
"v_cvt_pk_bf16_f32 v248, v24, v25\n"
"v_mfma_f32_16x16x32_bf16 v[20:23], a[108:111], a[76:79], v[56:59]\n"
"v_exp_f32 v26, v26\n"
"v_mfma_f32_16x16x32_bf16 v[20:23], a[104:107], a[72:75], v[20:23]\n"
"ds_read_b128 a[88:91], v3 offset:16384\n"
"v_mfma_f32_16x16x32_bf16 v[20:23], a[100:103], a[68:71], v[20:23]\n"
"v_exp_f32 v27, v27\n"
"v_mfma_f32_16x16x32_bf16 v[20:23], a[96:99], a[64:67], v[20:23]\n"
"ds_read_b128 a[92:95], v4 offset:16384\n"
"v_cvt_pk_bf16_f32 v249, v26, v27\n"
"v_mfma_f32_16x16x32_bf16 v[68:71], a[208:211], v[36:39], v[68:71]\n"
"v_exp_f32 v28, v28\n"
"v_mfma_f32_16x16x32_bf16 v[160:163], a[192:195], v[36:39], 0\n"
"ds_read_b128 a[108:111], v4 offset:20480\n"
"v_mfma_f32_16x16x32_bf16 v[160:163], a[160:163], v[32:35], v[160:163]\n"
"v_exp_f32 v29, v29\n"
"v_mfma_f32_16x16x32_bf16 v[164:167], a[164:167], v[32:35], 0\n"
"ds_read_b128 a[104:107], v3 offset:20480\n"
"v_cvt_pk_bf16_f32 v250, v28, v29\n"
"v_mfma_f32_16x16x32_bf16 v[164:167], a[196:199], v[36:39], v[164:167]\n"
"v_exp_f32 v30, v30\n"
"v_mfma_f32_16x16x32_bf16 v[168:171], a[200:203], v[36:39], 0\n"
"ds_read_b128 a[100:103], v2 offset:20480\n"
"v_mfma_f32_16x16x32_bf16 v[168:171], a[168:171], v[32:35], v[168:171]\n"
"v_exp_f32 v31, v31\n"
"v_mfma_f32_16x16x32_bf16 v[172:175], a[172:175], v[32:35], 0\n"
"s_add_u32 m0, s83, 49152\n"
"ds_read_b128 a[96:99], v1 offset:20480\n"
"v_cvt_pk_bf16_f32 v251, v30, v31\n"
"v_mfma_f32_16x16x32_bf16 v[172:175], a[204:207], v[36:39], v[172:175]\n"
"buffer_load_dwordx4 v240, s[44:47], s85 offen offset:0 nt lds\n"
"v_mfma_f32_16x16x32_bf16 v[24:27], a[112:115], a[64:67], v[56:59]\n"
"v_exp_f32 v16, v16\n"
"v_mfma_f32_16x16x32_bf16 v[24:27], a[116:119], a[68:71], v[24:27]\n"
"ds_read_b128 a[112:115], v1 offset:24576\n"
"v_mfma_f32_16x16x32_bf16 v[24:27], a[120:123], a[72:75], v[24:27]\n"
"v_exp_f32 v17, v17\n"
"v_mfma_f32_16x16x32_bf16 v[24:27], a[124:127], a[76:79], v[24:27]\n"
"ds_read_b128 a[116:119], v2 offset:24576\n"
"v_cvt_pk_bf16_f32 v32, v16, v17\n"
"v_mfma_f32_16x16x32_bf16 v[28:31], a[140:143], a[76:79], v[56:59]\n"
"v_exp_f32 v18, v18\n"
"v_mfma_f32_16x16x32_bf16 v[28:31], a[136:139], a[72:75], v[28:31]\n"
"ds_read_b128 a[120:123], v3 offset:24576\n"
"v_mfma_f32_16x16x32_bf16 v[28:31], a[132:135], a[68:71], v[28:31]\n"
"v_exp_f32 v19, v19\n"
"v_mfma_f32_16x16x32_bf16 v[28:31], a[128:131], a[64:67], v[28:31]\n"
"ds_read_b128 a[124:127], v4 offset:24576\n"
"v_cvt_pk_bf16_f32 v33, v18, v19\n"
"v_mfma_f32_16x16x32_bf16 v[72:75], a[208:211], v[244:247], 0\n"
"v_exp_f32 v20, v20\n"
"v_mfma_f32_16x16x32_bf16 v[176:179], a[144:147], v[244:247], 0\n"
"ds_read_b128 a[140:143], v4 offset:28672\n"
"v_mfma_f32_16x16x32_bf16 v[176:179], a[176:179], v[248:251], v[176:179]\n"
"v_exp_f32 v21, v21\n"
"v_mfma_f32_16x16x32_bf16 v[180:183], a[180:183], v[248:251], 0\n"
"ds_read_b128 a[136:139], v3 offset:28672\n"
"v_cvt_pk_bf16_f32 v34, v20, v21\n"
"v_mfma_f32_16x16x32_bf16 v[180:183], a[148:151], v[244:247], v[180:183]\n"
"v_exp_f32 v22, v22\n"
"v_mfma_f32_16x16x32_bf16 v[184:187], a[152:155], v[244:247], 0\n"
"ds_read_b128 a[132:135], v2 offset:28672\n"
"v_mfma_f32_16x16x32_bf16 v[184:187], a[184:187], v[248:251], v[184:187]\n"
"v_exp_f32 v23, v23\n"
"v_mfma_f32_16x16x32_bf16 v[188:191], a[188:191], v[248:251], 0\n"
"ds_read_b128 a[128:131], v1 offset:28672\n"
"v_cvt_pk_bf16_f32 v35, v22, v23\n"
"v_mfma_f32_16x16x32_bf16 v[188:191], a[156:159], v[244:247], v[188:191]\n"
"s_waitcnt lgkmcnt(8)\n"
"buffer_load_dwordx4 v241, s[44:47], s85 offen offset:1024 nt lds\n"
"v_mfma_f32_16x16x32_bf16 v[16:19], a[80:83], a[0:3], v[40:43]\n"
"v_exp_f32 v24, v24\n"
"v_mfma_f32_16x16x32_bf16 v[16:19], a[84:87], a[4:7], v[16:19]\n"
"ds_read_b64_tr_b16 a[212:213], v252 offset:0\n"
"v_mfma_f32_16x16x32_bf16 v[16:19], a[88:91], a[8:11], v[16:19]\n"
"v_exp_f32 v25, v25\n"
"v_mfma_f32_16x16x32_bf16 v[16:19], a[92:95], a[12:15], v[16:19]\n"
"ds_read_b64_tr_b16 a[214:215], v252 offset:4096\n"
"v_cvt_pk_bf16_f32 v36, v24, v25\n"
"v_mfma_f32_16x16x32_bf16 v[20:23], a[108:111], a[12:15], v[40:43]\n"
"v_exp_f32 v26, v26\n"
"v_mfma_f32_16x16x32_bf16 v[20:23], a[104:107], a[8:11], v[20:23]\n"
"ds_read_b64_tr_b16 a[216:217], v253 offset:0\n"
"v_mfma_f32_16x16x32_bf16 v[20:23], a[100:103], a[4:7], v[20:23]\n"
"v_exp_f32 v27, v27\n"
"v_mfma_f32_16x16x32_bf16 v[20:23], a[96:99], a[0:3], v[20:23]\n"
"ds_read_b64_tr_b16 a[218:219], v253 offset:4096\n"
"v_cvt_pk_bf16_f32 v37, v26, v27\n"
"v_mfma_f32_16x16x32_bf16 v[72:75], a[208:211], v[248:251], v[72:75]\n"
"v_exp_f32 v28, v28\n"
"v_mfma_f32_16x16x32_bf16 v[192:195], a[192:195], v[248:251], 0\n"
"ds_read_b64_tr_b16 a[220:221], v252 offset:512\n"
"v_mfma_f32_16x16x32_bf16 v[192:195], a[160:163], v[244:247], v[192:195]\n"
"v_exp_f32 v29, v29\n"
"v_mfma_f32_16x16x32_bf16 v[196:199], a[164:167], v[244:247], 0\n"
"ds_read_b64_tr_b16 a[222:223], v252 offset:4608\n"
"v_cvt_pk_bf16_f32 v38, v28, v29\n"
"v_mfma_f32_16x16x32_bf16 v[196:199], a[196:199], v[248:251], v[196:199]\n"
"v_exp_f32 v30, v30\n"
"v_mfma_f32_16x16x32_bf16 v[200:203], a[200:203], v[248:251], 0\n"
"ds_read_b64_tr_b16 a[224:225], v253 offset:512\n"
"v_mfma_f32_16x16x32_bf16 v[200:203], a[168:171], v[244:247], v[200:203]\n"
"v_exp_f32 v31, v31\n"
"v_mfma_f32_16x16x32_bf16 v[204:207], a[172:175], v[244:247], 0\n"
"ds_read_b64_tr_b16 a[226:227], v253 offset:4608\n"
"v_cvt_pk_bf16_f32 v39, v30, v31\n"
"v_mfma_f32_16x16x32_bf16 v[204:207], a[204:207], v[248:251], v[204:207]\n"
"s_waitcnt lgkmcnt(8)\n"
"buffer_load_dwordx4 v242, s[44:47], s85 offen offset:2048 nt lds\n"
"v_mfma_f32_16x16x32_bf16 v[24:27], a[112:115], a[0:3], v[40:43]\n"
"v_exp_f32 v16, v16\n"
"v_mfma_f32_16x16x32_bf16 v[24:27], a[116:119], a[4:7], v[24:27]\n"
"ds_read_b64_tr_b16 a[228:229], v252 offset:1024\n"
"v_mfma_f32_16x16x32_bf16 v[24:27], a[120:123], a[8:11], v[24:27]\n"
"v_exp_f32 v17, v17\n"
"v_mfma_f32_16x16x32_bf16 v[24:27], a[124:127], a[12:15], v[24:27]\n"
"ds_read_b64_tr_b16 a[230:231], v252 offset:5120\n"
"v_cvt_pk_bf16_f32 v244, v16, v17\n"
"v_mfma_f32_16x16x32_bf16 v[28:31], a[140:143], a[12:15], v[40:43]\n"
"v_exp_f32 v18, v18\n"
"v_mfma_f32_16x16x32_bf16 v[28:31], a[136:139], a[8:11], v[28:31]\n"
"ds_read_b64_tr_b16 a[232:233], v253 offset:1024\n"
"v_mfma_f32_16x16x32_bf16 v[28:31], a[132:135], a[4:7], v[28:31]\n"
"v_exp_f32 v19, v19\n"
"v_mfma_f32_16x16x32_bf16 v[28:31], a[128:131], a[0:3], v[28:31]\n"
"ds_read_b64_tr_b16 a[234:235], v253 offset:5120\n"
"v_cvt_pk_bf16_f32 v245, v18, v19\n"
"v_mfma_f32_16x16x32_bf16 v[76:79], a[208:211], v[32:35], 0\n"
"v_exp_f32 v20, v20\n"
"v_mfma_f32_16x16x32_bf16 v[208:211], a[144:147], v[32:35], 0\n"
"ds_read_b64_tr_b16 a[236:237], v252 offset:1536\n"
"v_mfma_f32_16x16x32_bf16 v[208:211], a[176:179], v[36:39], v[208:211]\n"
"v_exp_f32 v21, v21\n"
"v_mfma_f32_16x16x32_bf16 v[212:215], a[180:183], v[36:39], 0\n"
"ds_read_b64_tr_b16 a[144:145], v5 offset:16384\n"
"v_cvt_pk_bf16_f32 v246, v20, v21\n"
"v_mfma_f32_16x16x32_bf16 v[212:215], a[148:151], v[32:35], v[212:215]\n"
"v_exp_f32 v22, v22\n"
"v_mfma_f32_16x16x32_bf16 v[216:219], a[152:155], v[32:35], 0\n"
"ds_read_b64_tr_b16 a[146:147], v5 offset:20480\n"
"v_mfma_f32_16x16x32_bf16 v[216:219], a[184:187], v[36:39], v[216:219]\n"
"v_exp_f32 v23, v23\n"
"v_mfma_f32_16x16x32_bf16 v[220:223], a[188:191], v[36:39], 0\n"
"ds_read_b64_tr_b16 a[176:177], v5 offset:24576\n"
"v_cvt_pk_bf16_f32 v247, v22, v23\n"
"v_mfma_f32_16x16x32_bf16 v[220:223], a[156:159], v[32:35], v[220:223]\n"
"ds_read_b64_tr_b16 a[178:179], v5 offset:28672\n"
"v_mfma_f32_16x16x32_bf16 v[16:19], a[80:83], a[16:19], v[44:47]\n"
"v_exp_f32 v24, v24\n"
"v_mfma_f32_16x16x32_bf16 v[16:19], a[84:87], a[20:23], v[16:19]\n"
"ds_read_b64_tr_b16 a[148:149], v6 offset:16384\n"
"v_mfma_f32_16x16x32_bf16 v[16:19], a[88:91], a[24:27], v[16:19]\n"
"ds_read_b64_tr_b16 a[150:151], v6 offset:20480\n"
"v_exp_f32 v25, v25\n"
"v_mfma_f32_16x16x32_bf16 v[16:19], a[92:95], a[28:31], v[16:19]\n"
"ds_read_b64_tr_b16 a[180:181], v6 offset:24576\n"
"v_cvt_pk_bf16_f32 v248, v24, v25\n"
"v_mfma_f32_16x16x32_bf16 v[20:23], a[108:111], a[28:31], v[44:47]\n"
"v_exp_f32 v26, v26\n"
"v_mfma_f32_16x16x32_bf16 v[20:23], a[104:107], a[24:27], v[20:23]\n"
"ds_read_b64_tr_b16 a[182:183], v6 offset:28672\n"
"v_mfma_f32_16x16x32_bf16 v[20:23], a[100:103], a[20:23], v[20:23]\n"
"v_exp_f32 v27, v27\n"
"v_mfma_f32_16x16x32_bf16 v[20:23], a[96:99], a[16:19], v[20:23]\n"
"ds_read_b64_tr_b16 a[152:153], v5 offset:16896\n"
"v_cvt_pk_bf16_f32 v249, v26, v27\n"
"v_mfma_f32_16x16x32_bf16 v[76:79], a[208:211], v[36:39], v[76:79]\n"
"ds_read_b64_tr_b16 a[154:155], v5 offset:20992\n"
"v_exp_f32 v28, v28\n"
"v_mfma_f32_16x16x32_bf16 v[224:227], a[192:195], v[36:39], 0\n"
"ds_read_b64_tr_b16 a[184:185], v5 offset:25088\n"
"v_mfma_f32_16x16x32_bf16 v[224:227], a[160:163], v[32:35], v[224:227]\n"
"v_exp_f32 v29, v29\n"
"v_mfma_f32_16x16x32_bf16 v[228:231], a[164:167], v[32:35], 0\n"
"ds_read_b64_tr_b16 a[186:187], v5 offset:29184\n"
"v_cvt_pk_bf16_f32 v250, v28, v29\n"
"v_mfma_f32_16x16x32_bf16 v[228:231], a[196:199], v[36:39], v[228:231]\n"
"v_exp_f32 v30, v30\n"
"v_mfma_f32_16x16x32_bf16 v[232:235], a[200:203], v[36:39], 0\n"
"ds_read_b64_tr_b16 a[156:157], v6 offset:16896\n"
"v_mfma_f32_16x16x32_bf16 v[232:235], a[168:171], v[32:35], v[232:235]\n"
"ds_read_b64_tr_b16 a[158:159], v6 offset:20992\n"
"v_exp_f32 v31, v31\n"
"v_mfma_f32_16x16x32_bf16 v[236:239], a[172:175], v[32:35], 0\n"
"ds_read_b64_tr_b16 a[188:189], v6 offset:25088\n"
"v_cvt_pk_bf16_f32 v251, v30, v31\n"
"v_mfma_f32_16x16x32_bf16 v[236:239], a[204:207], v[36:39], v[236:239]\n"
"ds_read_b64_tr_b16 a[190:191], v6 offset:29184\n"
"v_mfma_f32_16x16x32_bf16 v[24:27], a[112:115], a[16:19], v[44:47]\n"
"v_exp_f32 v16, v16\n"
"v_mfma_f32_16x16x32_bf16 v[24:27], a[116:119], a[20:23], v[24:27]\n"
"ds_read_b64_tr_b16 a[160:161], v5 offset:17408\n"
"v_mfma_f32_16x16x32_bf16 v[24:27], a[120:123], a[24:27], v[24:27]\n"
"ds_read_b64_tr_b16 a[162:163], v5 offset:21504\n"
"v_exp_f32 v17, v17\n"
"v_mfma_f32_16x16x32_bf16 v[24:27], a[124:127], a[28:31], v[24:27]\n"
"ds_read_b64_tr_b16 a[192:193], v5 offset:25600\n"
"v_cvt_pk_bf16_f32 v32, v16, v17\n"
"v_mfma_f32_16x16x32_bf16 v[28:31], a[140:143], a[28:31], v[44:47]\n"
"v_exp_f32 v18, v18\n"
"v_mfma_f32_16x16x32_bf16 v[28:31], a[136:139], a[24:27], v[28:31]\n"
"ds_read_b64_tr_b16 a[194:195], v5 offset:29696\n"
"v_mfma_f32_16x16x32_bf16 v[28:31], a[132:135], a[20:23], v[28:31]\n"
"v_exp_f32 v19, v19\n"
"v_mfma_f32_16x16x32_bf16 v[28:31], a[128:131], a[16:19], v[28:31]\n"
"ds_read_b64_tr_b16 a[164:165], v6 offset:17408\n"
"v_cvt_pk_bf16_f32 v33, v18, v19\n"
"v_mfma_f32_16x16x32_bf16 v[60:63], a[208:211], v[244:247], v[60:63]\n"
"s_waitcnt lgkmcnt(15)\n"
"ds_read_b64_tr_b16 a[166:167], v6 offset:21504\n"
"v_exp_f32 v20, v20\n"
"v_mfma_f32_16x16x32_bf16 v[80:83], a[144:147], v[244:247], v[80:83]\n"
"ds_read_b64_tr_b16 a[196:197], v6 offset:25600\n"
"v_mfma_f32_16x16x32_bf16 v[80:83], a[176:179], v[248:251], v[80:83]\n"
"s_waitcnt lgkmcnt(15)\n"
"v_exp_f32 v21, v21\n"
"v_mfma_f32_16x16x32_bf16 v[84:87], a[180:183], v[248:251], v[84:87]\n"
"ds_read_b64_tr_b16 a[198:199], v6 offset:29696\n"
"v_cvt_pk_bf16_f32 v34, v20, v21\n"
"v_mfma_f32_16x16x32_bf16 v[84:87], a[148:151], v[244:247], v[84:87]\n"
"s_waitcnt lgkmcnt(12)\n"
"v_exp_f32 v22, v22\n"
"v_mfma_f32_16x16x32_bf16 v[88:91], a[152:155], v[244:247], v[88:91]\n"
"ds_read_b64_tr_b16 a[168:169], v5 offset:17920\n"
"v_mfma_f32_16x16x32_bf16 v[88:91], a[184:187], v[248:251], v[88:91]\n"
"s_waitcnt lgkmcnt(9)\n"
"v_exp_f32 v23, v23\n"
"v_mfma_f32_16x16x32_bf16 v[92:95], a[188:191], v[248:251], v[92:95]\n"
"ds_read_b64_tr_b16 a[170:171], v5 offset:22016\n"
"v_cvt_pk_bf16_f32 v35, v22, v23\n"
"v_mfma_f32_16x16x32_bf16 v[92:95], a[156:159], v[244:247], v[92:95]\n"
"ds_read_b64_tr_b16 a[200:201], v5 offset:26112\n"
"v_mfma_f32_16x16x32_bf16 v[16:19], a[80:83], a[32:35], v[48:51]\n"
"v_exp_f32 v24, v24\n"
"v_mfma_f32_16x16x32_bf16 v[16:19], a[84:87], a[36:39], v[16:19]\n"
"ds_read_b64_tr_b16 a[202:203], v5 offset:30208\n"
"v_mfma_f32_16x16x32_bf16 v[16:19], a[88:91], a[40:43], v[16:19]\n"
"v_exp_f32 v25, v25\n"
"v_mfma_f32_16x16x32_bf16 v[16:19], a[92:95], a[44:47], v[16:19]\n"
"ds_read_b64_tr_b16 a[172:173], v6 offset:17920\n"
"v_cvt_pk_bf16_f32 v36, v24, v25\n"
"v_mfma_f32_16x16x32_bf16 v[20:23], a[108:111], a[44:47], v[48:51]\n"
"v_exp_f32 v26, v26\n"
"v_mfma_f32_16x16x32_bf16 v[20:23], a[104:107], a[40:43], v[20:23]\n"
"ds_read_b64_tr_b16 a[174:175], v6 offset:22016\n"
"v_mfma_f32_16x16x32_bf16 v[20:23], a[100:103], a[36:39], v[20:23]\n"
"v_exp_f32 v27, v27\n"
"v_mfma_f32_16x16x32_bf16 v[20:23], a[96:99], a[32:35], v[20:23]\n"
"ds_read_b64_tr_b16 a[204:205], v6 offset:26112\n"
"v_cvt_pk_bf16_f32 v37, v26, v27\n"
"v_mfma_f32_16x16x32_bf16 v[60:63], a[208:211], v[248:251], v[60:63]\n"
"s_waitcnt lgkmcnt(11)\n"
"v_exp_f32 v28, v28\n"
"v_mfma_f32_16x16x32_bf16 v[96:99], a[192:195], v[248:251], v[96:99]\n"
"ds_read_b64_tr_b16 a[206:207], v6 offset:30208\n"
"v_mfma_f32_16x16x32_bf16 v[96:99], a[160:163], v[244:247], v[96:99]\n"
"s_waitcnt lgkmcnt(8)\n"
"v_exp_f32 v29, v29\n"
"v_mfma_f32_16x16x32_bf16 v[100:103], a[164:167], v[244:247], v[100:103]\n"
"v_cvt_pk_bf16_f32 v38, v28, v29\n"
"v_mfma_f32_16x16x32_bf16 v[100:103], a[196:199], v[248:251], v[100:103]\n"
"s_waitcnt lgkmcnt(4)\n"
"v_exp_f32 v30, v30\n"
"v_mfma_f32_16x16x32_bf16 v[104:107], a[200:203], v[248:251], v[104:107]\n"
"buffer_load_dwordx4 v243, s[44:47], s85 offen offset:3072 nt lds\n"
"v_mfma_f32_16x16x32_bf16 v[104:107], a[168:171], v[244:247], v[104:107]\n"
"s_waitcnt lgkmcnt(0)\n"
"s_add_u32 s85, s85, 16384\n"
"v_exp_f32 v31, v31\n"
"v_mfma_f32_16x16x32_bf16 v[108:111], a[172:175], v[244:247], v[108:111]\n"
"v_cvt_pk_bf16_f32 v39, v30, v31\n"
"v_mfma_f32_16x16x32_bf16 v[108:111], a[204:207], v[248:251], v[108:111]\n"
"v_mfma_f32_16x16x32_bf16 v[24:27], a[112:115], a[32:35], v[48:51]\n"
"v_exp_f32 v16, v16\n"
"v_mfma_f32_16x16x32_bf16 v[24:27], a[116:119], a[36:39], v[24:27]\n"
"ds_read_b64_tr_b16 a[238:239], v252 offset:5632\n"
"v_mfma_f32_16x16x32_bf16 v[24:27], a[120:123], a[40:43], v[24:27]\n"
"v_exp_f32 v17, v17\n"
"v_mfma_f32_16x16x32_bf16 v[24:27], a[124:127], a[44:47], v[24:27]\n"
"ds_read_b64_tr_b16 a[240:241], v253 offset:1536\n"
"v_cvt_pk_bf16_f32 v244, v16, v17\n"
"v_mfma_f32_16x16x32_bf16 v[28:31], a[140:143], a[44:47], v[48:51]\n"
"v_exp_f32 v18, v18\n"
"v_mfma_f32_16x16x32_bf16 v[28:31], a[136:139], a[40:43], v[28:31]\n"
"ds_read_b64_tr_b16 a[242:243], v253 offset:5632\n"
"v_mfma_f32_16x16x32_bf16 v[28:31], a[132:135], a[36:39], v[28:31]\n"
"v_exp_f32 v19, v19\n"
"v_mfma_f32_16x16x32_bf16 v[28:31], a[128:131], a[32:35], v[28:31]\n"
"ds_write_b128 v254, a[248:251]\n"
"v_cvt_pk_bf16_f32 v245, v18, v19\n"
"v_mfma_f32_16x16x32_bf16 v[64:67], a[208:211], v[32:35], v[64:67]\n"
"v_exp_f32 v20, v20\n"
"v_mfma_f32_16x16x32_bf16 v[112:115], a[144:147], v[32:35], v[112:115]\n"
"ds_read_b64_tr_b16 a[244:245], v255 offset:0\n"
"v_mfma_f32_16x16x32_bf16 v[112:115], a[176:179], v[36:39], v[112:115]\n"
"v_exp_f32 v21, v21\n"
"v_mfma_f32_16x16x32_bf16 v[116:119], a[180:183], v[36:39], v[116:119]\n"
"ds_read_b64_tr_b16 a[246:247], v255 offset:512\n"
"v_cvt_pk_bf16_f32 v246, v20, v21\n"
"v_mfma_f32_16x16x32_bf16 v[116:119], a[148:151], v[32:35], v[116:119]\n"
"v_exp_f32 v22, v22\n"
"v_mfma_f32_16x16x32_bf16 v[120:123], a[152:155], v[32:35], v[120:123]\n"
"v_mfma_f32_16x16x32_bf16 v[120:123], a[184:187], v[36:39], v[120:123]\n"
"v_exp_f32 v23, v23\n"
"v_mfma_f32_16x16x32_bf16 v[124:127], a[188:191], v[36:39], v[124:127]\n"
"s_add_u32 m0, s84, 49152\n"
"v_cvt_pk_bf16_f32 v247, v22, v23\n"
"v_mfma_f32_16x16x32_bf16 v[124:127], a[156:159], v[32:35], v[124:127]\n"
"buffer_load_dwordx4 v8, s[48:51], s86 offen offset:0 nt lds\n"
"v_mfma_f32_16x16x32_bf16 v[16:19], a[80:83], a[48:51], v[52:55]\n"
"v_exp_f32 v24, v24\n"
"v_mfma_f32_16x16x32_bf16 v[16:19], a[84:87], a[52:55], v[16:19]\n"
"v_mfma_f32_16x16x32_bf16 v[16:19], a[88:91], a[56:59], v[16:19]\n"
"v_exp_f32 v25, v25\n"
"v_mfma_f32_16x16x32_bf16 v[16:19], a[92:95], a[60:63], v[16:19]\n"
"v_cvt_pk_bf16_f32 v248, v24, v25\n"
"v_mfma_f32_16x16x32_bf16 v[20:23], a[108:111], a[60:63], v[52:55]\n"
"v_exp_f32 v26, v26\n"
"v_mfma_f32_16x16x32_bf16 v[20:23], a[104:107], a[56:59], v[20:23]\n"
"buffer_load_dwordx4 v8, s[48:51], s87 offen offset:1024 nt lds\n"
"v_mfma_f32_16x16x32_bf16 v[20:23], a[100:103], a[52:55], v[20:23]\n"
"v_exp_f32 v27, v27\n"
"v_mfma_f32_16x16x32_bf16 v[20:23], a[96:99], a[48:51], v[20:23]\n"
"v_cvt_pk_bf16_f32 v249, v26, v27\n"
"v_mfma_f32_16x16x32_bf16 v[64:67], a[208:211], v[36:39], v[64:67]\n"
"v_exp_f32 v28, v28\n"
"v_mfma_f32_16x16x32_bf16 v[128:131], a[192:195], v[36:39], v[128:131]\n"
"v_mfma_f32_16x16x32_bf16 v[128:131], a[160:163], v[32:35], v[128:131]\n"
"v_exp_f32 v29, v29\n"
"v_mfma_f32_16x16x32_bf16 v[132:135], a[164:167], v[32:35], v[132:135]\n"
"v_cvt_pk_bf16_f32 v250, v28, v29\n"
"v_mfma_f32_16x16x32_bf16 v[132:135], a[196:199], v[36:39], v[132:135]\n"
"v_exp_f32 v30, v30\n"
"v_mfma_f32_16x16x32_bf16 v[136:139], a[200:203], v[36:39], v[136:139]\n"
"v_mfma_f32_16x16x32_bf16 v[136:139], a[168:171], v[32:35], v[136:139]\n"
"v_exp_f32 v31, v31\n"
"v_mfma_f32_16x16x32_bf16 v[140:143], a[172:175], v[32:35], v[140:143]\n"
"v_cvt_pk_bf16_f32 v251, v30, v31\n"
"v_mfma_f32_16x16x32_bf16 v[140:143], a[204:207], v[36:39], v[140:143]\n"
"buffer_load_dwordx4 v8, s[48:51], s86 offen offset:2048 nt lds\n"
"v_mfma_f32_16x16x32_bf16 v[24:27], a[112:115], a[48:51], v[52:55]\n"
"v_exp_f32 v16, v16\n"
"v_mfma_f32_16x16x32_bf16 v[24:27], a[116:119], a[52:55], v[24:27]\n"
"v_mfma_f32_16x16x32_bf16 v[24:27], a[120:123], a[56:59], v[24:27]\n"
"v_exp_f32 v17, v17\n"
"v_mfma_f32_16x16x32_bf16 v[24:27], a[124:127], a[60:63], v[24:27]\n"
"v_cvt_pk_bf16_f32 v32, v16, v17\n"
"v_mfma_f32_16x16x32_bf16 v[28:31], a[140:143], a[60:63], v[52:55]\n"
"v_exp_f32 v18, v18\n"
"v_mfma_f32_16x16x32_bf16 v[28:31], a[136:139], a[56:59], v[28:31]\n"
"v_mfma_f32_16x16x32_bf16 v[28:31], a[132:135], a[52:55], v[28:31]\n"
"v_exp_f32 v19, v19\n"
"v_mfma_f32_16x16x32_bf16 v[28:31], a[128:131], a[48:51], v[28:31]\n"
"v_cvt_pk_bf16_f32 v33, v18, v19\n"
"v_mfma_f32_16x16x32_bf16 v[68:71], a[208:211], v[244:247], v[68:71]\n"
"v_exp_f32 v20, v20\n"
"v_mfma_f32_16x16x32_bf16 v[144:147], a[144:147], v[244:247], v[144:147]\n"
"buffer_load_dwordx4 v8, s[48:51], s87 offen offset:3072 nt lds\n"
"v_mfma_f32_16x16x32_bf16 v[144:147], a[176:179], v[248:251], v[144:147]\n"
"s_add_u32 s86, s86, 16384\n"
"s_add_u32 s87, s87, 16384\n"
"v_exp_f32 v21, v21\n"
"v_mfma_f32_16x16x32_bf16 v[148:151], a[180:183], v[248:251], v[148:151]\n"
"v_cvt_pk_bf16_f32 v34, v20, v21\n"
"v_mfma_f32_16x16x32_bf16 v[148:151], a[148:151], v[244:247], v[148:151]\n"
"v_exp_f32 v22, v22\n"
"v_mfma_f32_16x16x32_bf16 v[152:155], a[152:155], v[244:247], v[152:155]\n"
"v_mfma_f32_16x16x32_bf16 v[152:155], a[184:187], v[248:251], v[152:155]\n"
"v_exp_f32 v23, v23\n"
"v_mfma_f32_16x16x32_bf16 v[156:159], a[188:191], v[248:251], v[156:159]\n"
"v_cvt_pk_bf16_f32 v35, v22, v23\n"
"v_mfma_f32_16x16x32_bf16 v[156:159], a[156:159], v[244:247], v[156:159]\n"
"buffer_load_dword v10, off, s[4:7], s9\n"
"s_add_u32 s9, s9, 4096\n"
"v_mfma_f32_16x16x32_bf16 v[16:19], a[80:83], a[64:67], v[56:59]\n"
"s_waitcnt vmcnt(8) lgkmcnt(0)\n"
"s_barrier\n"
"v_exp_f32 v24, v24\n"
"v_mfma_f32_16x16x32_bf16 v[16:19], a[84:87], a[68:71], v[16:19]\n"
"ds_read_b128 a[80:83], v1 offset:32768\n"
"v_mfma_f32_16x16x32_bf16 v[16:19], a[88:91], a[72:75], v[16:19]\n"
"v_exp_f32 v25, v25\n"
"v_mfma_f32_16x16x32_bf16 v[16:19], a[92:95], a[76:79], v[16:19]\n"
"ds_read_b128 a[84:87], v2 offset:32768\n"
"v_cvt_pk_bf16_f32 v36, v24, v25\n"
"v_mfma_f32_16x16x32_bf16 v[20:23], a[108:111], a[76:79], v[56:59]\n"
"v_exp_f32 v26, v26\n"
"v_mfma_f32_16x16x32_bf16 v[20:23], a[104:107], a[72:75], v[20:23]\n"
"ds_read_b128 a[88:91], v3 offset:32768\n"
"v_mfma_f32_16x16x32_bf16 v[20:23], a[100:103], a[68:71], v[20:23]\n"
"v_exp_f32 v27, v27\n"
"v_mfma_f32_16x16x32_bf16 v[20:23], a[96:99], a[64:67], v[20:23]\n"
"ds_read_b128 a[92:95], v4 offset:32768\n"
"v_cvt_pk_bf16_f32 v37, v26, v27\n"
"v_mfma_f32_16x16x32_bf16 v[68:71], a[208:211], v[248:251], v[68:71]\n"
"v_exp_f32 v28, v28\n"
"v_mfma_f32_16x16x32_bf16 v[160:163], a[192:195], v[248:251], v[160:163]\n"
"ds_read_b128 a[108:111], v4 offset:36864\n"
"v_mfma_f32_16x16x32_bf16 v[160:163], a[160:163], v[244:247], v[160:163]\n"
"v_exp_f32 v29, v29\n"
"v_mfma_f32_16x16x32_bf16 v[164:167], a[164:167], v[244:247], v[164:167]\n"
"ds_read_b128 a[104:107], v3 offset:36864\n"
"v_cvt_pk_bf16_f32 v38, v28, v29\n"
"v_mfma_f32_16x16x32_bf16 v[164:167], a[196:199], v[248:251], v[164:167]\n"
"v_exp_f32 v30, v30\n"
"v_mfma_f32_16x16x32_bf16 v[168:171], a[200:203], v[248:251], v[168:171]\n"
"ds_read_b128 a[100:103], v2 offset:36864\n"
"v_mfma_f32_16x16x32_bf16 v[168:171], a[168:171], v[244:247], v[168:171]\n"
"v_exp_f32 v31, v31\n"
"v_mfma_f32_16x16x32_bf16 v[172:175], a[172:175], v[244:247], v[172:175]\n"
"s_add_u32 m0, s83, 0\n"
"ds_read_b128 a[96:99], v1 offset:36864\n"
"v_cvt_pk_bf16_f32 v39, v30, v31\n"
"v_mfma_f32_16x16x32_bf16 v[172:175], a[204:207], v[248:251], v[172:175]\n"
"buffer_load_dwordx4 v240, s[44:47], s85 offen offset:0 nt lds\n"
"v_mfma_f32_16x16x32_bf16 v[24:27], a[112:115], a[64:67], v[56:59]\n"
"v_exp_f32 v16, v16\n"
"v_mfma_f32_16x16x32_bf16 v[24:27], a[116:119], a[68:71], v[24:27]\n"
"ds_read_b128 a[112:115], v1 offset:40960\n"
"v_mfma_f32_16x16x32_bf16 v[24:27], a[120:123], a[72:75], v[24:27]\n"
"v_exp_f32 v17, v17\n"
"v_mfma_f32_16x16x32_bf16 v[24:27], a[124:127], a[76:79], v[24:27]\n"
"ds_read_b128 a[116:119], v2 offset:40960\n"
"v_cvt_pk_bf16_f32 v244, v16, v17\n"
"v_mfma_f32_16x16x32_bf16 v[28:31], a[140:143], a[76:79], v[56:59]\n"
"v_exp_f32 v18, v18\n"
"v_mfma_f32_16x16x32_bf16 v[28:31], a[136:139], a[72:75], v[28:31]\n"
"ds_read_b128 a[120:123], v3 offset:40960\n"
"v_mfma_f32_16x16x32_bf16 v[28:31], a[132:135], a[68:71], v[28:31]\n"
"v_exp_f32 v19, v19\n"
"v_mfma_f32_16x16x32_bf16 v[28:31], a[128:131], a[64:67], v[28:31]\n"
"ds_read_b128 a[124:127], v4 offset:40960\n"
"v_cvt_pk_bf16_f32 v245, v18, v19\n"
"v_mfma_f32_16x16x32_bf16 v[72:75], a[208:211], v[32:35], v[72:75]\n"
"v_exp_f32 v20, v20\n"
"v_mfma_f32_16x16x32_bf16 v[176:179], a[144:147], v[32:35], v[176:179]\n"
"ds_read_b128 a[140:143], v4 offset:45056\n"
"v_mfma_f32_16x16x32_bf16 v[176:179], a[176:179], v[36:39], v[176:179]\n"
"v_exp_f32 v21, v21\n"
"v_mfma_f32_16x16x32_bf16 v[180:183], a[180:183], v[36:39], v[180:183]\n"
"ds_read_b128 a[136:139], v3 offset:45056\n"
"v_cvt_pk_bf16_f32 v246, v20, v21\n"
"v_mfma_f32_16x16x32_bf16 v[180:183], a[148:151], v[32:35], v[180:183]\n"
"v_exp_f32 v22, v22\n"
"v_mfma_f32_16x16x32_bf16 v[184:187], a[152:155], v[32:35], v[184:187]\n"
"ds_read_b128 a[132:135], v2 offset:45056\n"
"v_mfma_f32_16x16x32_bf16 v[184:187], a[184:187], v[36:39], v[184:187]\n"
"v_exp_f32 v23, v23\n"
"v_mfma_f32_16x16x32_bf16 v[188:191], a[188:191], v[36:39], v[188:191]\n"
"ds_read_b128 a[128:131], v1 offset:45056\n"
"v_cvt_pk_bf16_f32 v247, v22, v23\n"
"v_mfma_f32_16x16x32_bf16 v[188:191], a[156:159], v[32:35], v[188:191]\n"
"s_waitcnt lgkmcnt(8)\n"
"buffer_load_dwordx4 v241, s[44:47], s85 offen offset:1024 nt lds\n"
"v_mfma_f32_16x16x32_bf16 v[16:19], a[80:83], a[0:3], v[40:43]\n"
"v_exp_f32 v24, v24\n"
"v_mfma_f32_16x16x32_bf16 v[16:19], a[84:87], a[4:7], v[16:19]\n"
"v_mfma_f32_16x16x32_bf16 v[16:19], a[88:91], a[8:11], v[16:19]\n"
"v_exp_f32 v25, v25\n"
"v_mfma_f32_16x16x32_bf16 v[16:19], a[92:95], a[12:15], v[16:19]\n"
"v_cvt_pk_bf16_f32 v248, v24, v25\n"
"v_mfma_f32_16x16x32_bf16 v[20:23], a[108:111], a[12:15], v[40:43]\n"
"v_exp_f32 v26, v26\n"
"v_mfma_f32_16x16x32_bf16 v[20:23], a[104:107], a[8:11], v[20:23]\n"
"buffer_store_dwordx4 a[212:215], v9, s[56:59], s68 offen sc1\n"
"v_mfma_f32_16x16x32_bf16 v[20:23], a[100:103], a[4:7], v[20:23]\n"
"v_exp_f32 v27, v27\n"
"v_mfma_f32_16x16x32_bf16 v[20:23], a[96:99], a[0:3], v[20:23]\n"
"v_cvt_pk_bf16_f32 v249, v26, v27\n"
"v_mfma_f32_16x16x32_bf16 v[72:75], a[208:211], v[36:39], v[72:75]\n"
"v_exp_f32 v28, v28\n"
"v_mfma_f32_16x16x32_bf16 v[192:195], a[192:195], v[36:39], v[192:195]\n"
"v_mfma_f32_16x16x32_bf16 v[192:195], a[160:163], v[32:35], v[192:195]\n"
"v_exp_f32 v29, v29\n"
"v_mfma_f32_16x16x32_bf16 v[196:199], a[164:167], v[32:35], v[196:199]\n"
"v_cvt_pk_bf16_f32 v250, v28, v29\n"
"v_mfma_f32_16x16x32_bf16 v[196:199], a[196:199], v[36:39], v[196:199]\n"
"v_exp_f32 v30, v30\n"
"v_mfma_f32_16x16x32_bf16 v[200:203], a[200:203], v[36:39], v[200:203]\n"
"buffer_load_dwordx4 v242, s[44:47], s85 offen offset:2048 nt lds\n"
"v_mfma_f32_16x16x32_bf16 v[200:203], a[168:171], v[32:35], v[200:203]\n"
"v_exp_f32 v31, v31\n"
"v_mfma_f32_16x16x32_bf16 v[204:207], a[172:175], v[32:35], v[204:207]\n"
"v_cvt_pk_bf16_f32 v251, v30, v31\n"
"v_mfma_f32_16x16x32_bf16 v[204:207], a[204:207], v[36:39], v[204:207]\n"
"s_waitcnt lgkmcnt(0)\n"
"v_mfma_f32_16x16x32_bf16 v[24:27], a[112:115], a[0:3], v[40:43]\n"
"v_exp_f32 v16, v16\n"
"v_mfma_f32_16x16x32_bf16 v[24:27], a[116:119], a[4:7], v[24:27]\n"
"v_mfma_f32_16x16x32_bf16 v[24:27], a[120:123], a[8:11], v[24:27]\n"
"v_exp_f32 v17, v17\n"
"v_mfma_f32_16x16x32_bf16 v[24:27], a[124:127], a[12:15], v[24:27]\n"
"v_cvt_pk_bf16_f32 v32, v16, v17\n"
"v_mfma_f32_16x16x32_bf16 v[28:31], a[140:143], a[12:15], v[40:43]\n"
"v_exp_f32 v18, v18\n"
"v_mfma_f32_16x16x32_bf16 v[28:31], a[136:139], a[8:11], v[28:31]\n"
"buffer_load_dwordx4 v243, s[44:47], s85 offen offset:3072 nt lds\n"
"v_mfma_f32_16x16x32_bf16 v[28:31], a[132:135], a[4:7], v[28:31]\n"
"s_add_u32 s85, s85, 16384\n"
"v_exp_f32 v19, v19\n"
"v_mfma_f32_16x16x32_bf16 v[28:31], a[128:131], a[0:3], v[28:31]\n"
"v_cvt_pk_bf16_f32 v33, v18, v19\n"
"v_mfma_f32_16x16x32_bf16 v[76:79], a[208:211], v[244:247], v[76:79]\n"
"v_exp_f32 v20, v20\n"
"v_mfma_f32_16x16x32_bf16 v[208:211], a[144:147], v[244:247], v[208:211]\n"
"v_mfma_f32_16x16x32_bf16 v[208:211], a[176:179], v[248:251], v[208:211]\n"
"v_exp_f32 v21, v21\n"
"v_mfma_f32_16x16x32_bf16 v[212:215], a[180:183], v[248:251], v[212:215]\n"
"ds_read_b64_tr_b16 a[144:145], v5 offset:32768\n"
"v_cvt_pk_bf16_f32 v34, v20, v21\n"
"v_mfma_f32_16x16x32_bf16 v[212:215], a[148:151], v[244:247], v[212:215]\n"
"v_exp_f32 v22, v22\n"
"v_mfma_f32_16x16x32_bf16 v[216:219], a[152:155], v[244:247], v[216:219]\n"
"ds_read_b64_tr_b16 a[146:147], v5 offset:36864\n"
"v_mfma_f32_16x16x32_bf16 v[216:219], a[184:187], v[248:251], v[216:219]\n"
"v_exp_f32 v23, v23\n"
"v_mfma_f32_16x16x32_bf16 v[220:223], a[188:191], v[248:251], v[220:223]\n"
"ds_read_b64_tr_b16 a[176:177], v5 offset:40960\n"
"v_cvt_pk_bf16_f32 v35, v22, v23\n"
"v_mfma_f32_16x16x32_bf16 v[220:223], a[156:159], v[244:247], v[220:223]\n"
"ds_read_b64_tr_b16 a[178:179], v5 offset:45056\n"
"v_mfma_f32_16x16x32_bf16 v[16:19], a[80:83], a[16:19], v[44:47]\n"
"v_exp_f32 v24, v24\n"
"v_mfma_f32_16x16x32_bf16 v[16:19], a[84:87], a[20:23], v[16:19]\n"
"ds_read_b64_tr_b16 a[148:149], v6 offset:32768\n"
"v_mfma_f32_16x16x32_bf16 v[16:19], a[88:91], a[24:27], v[16:19]\n"
"ds_read_b64_tr_b16 a[150:151], v6 offset:36864\n"
"v_exp_f32 v25, v25\n"
"v_mfma_f32_16x16x32_bf16 v[16:19], a[92:95], a[28:31], v[16:19]\n"
"ds_read_b64_tr_b16 a[180:181], v6 offset:40960\n"
"v_cvt_pk_bf16_f32 v36, v24, v25\n"
"v_mfma_f32_16x16x32_bf16 v[20:23], a[108:111], a[28:31], v[44:47]\n"
"v_exp_f32 v26, v26\n"
"v_mfma_f32_16x16x32_bf16 v[20:23], a[104:107], a[24:27], v[20:23]\n"
"ds_read_b64_tr_b16 a[182:183], v6 offset:45056\n"
"v_mfma_f32_16x16x32_bf16 v[20:23], a[100:103], a[20:23], v[20:23]\n"
"v_exp_f32 v27, v27\n"
"v_mfma_f32_16x16x32_bf16 v[20:23], a[96:99], a[16:19], v[20:23]\n"
"ds_read_b64_tr_b16 a[152:153], v5 offset:33280\n"
"v_cvt_pk_bf16_f32 v37, v26, v27\n"
"v_mfma_f32_16x16x32_bf16 v[76:79], a[208:211], v[248:251], v[76:79]\n"
"ds_read_b64_tr_b16 a[154:155], v5 offset:37376\n"
"v_exp_f32 v28, v28\n"
"v_mfma_f32_16x16x32_bf16 v[224:227], a[192:195], v[248:251], v[224:227]\n"
"ds_read_b64_tr_b16 a[184:185], v5 offset:41472\n"
"v_mfma_f32_16x16x32_bf16 v[224:227], a[160:163], v[244:247], v[224:227]\n"
"v_exp_f32 v29, v29\n"
"v_mfma_f32_16x16x32_bf16 v[228:231], a[164:167], v[244:247], v[228:231]\n"
"ds_read_b64_tr_b16 a[186:187], v5 offset:45568\n"
"v_cvt_pk_bf16_f32 v38, v28, v29\n"
"v_mfma_f32_16x16x32_bf16 v[228:231], a[196:199], v[248:251], v[228:231]\n"
"v_exp_f32 v30, v30\n"
"v_mfma_f32_16x16x32_bf16 v[232:235], a[200:203], v[248:251], v[232:235]\n"
"ds_read_b64_tr_b16 a[156:157], v6 offset:33280\n"
"v_mfma_f32_16x16x32_bf16 v[232:235], a[168:171], v[244:247], v[232:235]\n"
"ds_read_b64_tr_b16 a[158:159], v6 offset:37376\n"
"v_exp_f32 v31, v31\n"
"v_mfma_f32_16x16x32_bf16 v[236:239], a[172:175], v[244:247], v[236:239]\n"
"ds_read_b64_tr_b16 a[188:189], v6 offset:41472\n"
"v_cvt_pk_bf16_f32 v39, v30, v31\n"
"v_mfma_f32_16x16x32_bf16 v[236:239], a[204:207], v[248:251], v[236:239]\n"
"ds_read_b64_tr_b16 a[190:191], v6 offset:45568\n"
"v_mfma_f32_16x16x32_bf16 v[24:27], a[112:115], a[16:19], v[44:47]\n"
"v_exp_f32 v16, v16\n"
"v_mfma_f32_16x16x32_bf16 v[24:27], a[116:119], a[20:23], v[24:27]\n"
"ds_read_b64_tr_b16 a[160:161], v5 offset:33792\n"
"v_mfma_f32_16x16x32_bf16 v[24:27], a[120:123], a[24:27], v[24:27]\n"
"ds_read_b64_tr_b16 a[162:163], v5 offset:37888\n"
"v_exp_f32 v17, v17\n"
"v_mfma_f32_16x16x32_bf16 v[24:27], a[124:127], a[28:31], v[24:27]\n"
"ds_read_b64_tr_b16 a[192:193], v5 offset:41984\n"
"v_cvt_pk_bf16_f32 v244, v16, v17\n"
"v_mfma_f32_16x16x32_bf16 v[28:31], a[140:143], a[28:31], v[44:47]\n"
"v_exp_f32 v18, v18\n"
"v_mfma_f32_16x16x32_bf16 v[28:31], a[136:139], a[24:27], v[28:31]\n"
"ds_read_b64_tr_b16 a[194:195], v5 offset:46080\n"
"v_mfma_f32_16x16x32_bf16 v[28:31], a[132:135], a[20:23], v[28:31]\n"
"v_exp_f32 v19, v19\n"
"v_mfma_f32_16x16x32_bf16 v[28:31], a[128:131], a[16:19], v[28:31]\n"
"ds_read_b64_tr_b16 a[164:165], v6 offset:33792\n"
"v_cvt_pk_bf16_f32 v245, v18, v19\n"
"v_mfma_f32_16x16x32_bf16 v[60:63], a[208:211], v[32:35], v[60:63]\n"
"s_waitcnt lgkmcnt(15)\n"
"ds_read_b64_tr_b16 a[166:167], v6 offset:37888\n"
"v_exp_f32 v20, v20\n"
"v_mfma_f32_16x16x32_bf16 v[80:83], a[144:147], v[32:35], v[80:83]\n"
"ds_read_b64_tr_b16 a[196:197], v6 offset:41984\n"
"v_mfma_f32_16x16x32_bf16 v[80:83], a[176:179], v[36:39], v[80:83]\n"
"s_waitcnt lgkmcnt(15)\n"
"v_exp_f32 v21, v21\n"
"v_mfma_f32_16x16x32_bf16 v[84:87], a[180:183], v[36:39], v[84:87]\n"
"ds_read_b64_tr_b16 a[198:199], v6 offset:46080\n"
"v_cvt_pk_bf16_f32 v246, v20, v21\n"
"v_mfma_f32_16x16x32_bf16 v[84:87], a[148:151], v[32:35], v[84:87]\n"
"s_waitcnt lgkmcnt(12)\n"
"v_exp_f32 v22, v22\n"
"v_mfma_f32_16x16x32_bf16 v[88:91], a[152:155], v[32:35], v[88:91]\n"
"ds_read_b64_tr_b16 a[168:169], v5 offset:34304\n"
"v_mfma_f32_16x16x32_bf16 v[88:91], a[184:187], v[36:39], v[88:91]\n"
"s_waitcnt lgkmcnt(9)\n"
"v_exp_f32 v23, v23\n"
"v_mfma_f32_16x16x32_bf16 v[92:95], a[188:191], v[36:39], v[92:95]\n"
"ds_read_b64_tr_b16 a[170:171], v5 offset:38400\n"
"v_cvt_pk_bf16_f32 v247, v22, v23\n"
"v_mfma_f32_16x16x32_bf16 v[92:95], a[156:159], v[32:35], v[92:95]\n"
"ds_read_b64_tr_b16 a[200:201], v5 offset:42496\n"
"v_mfma_f32_16x16x32_bf16 v[16:19], a[80:83], a[32:35], v[48:51]\n"
"v_exp_f32 v24, v24\n"
"v_mfma_f32_16x16x32_bf16 v[16:19], a[84:87], a[36:39], v[16:19]\n"
"ds_read_b64_tr_b16 a[202:203], v5 offset:46592\n"
"v_mfma_f32_16x16x32_bf16 v[16:19], a[88:91], a[40:43], v[16:19]\n"
"v_exp_f32 v25, v25\n"
"v_mfma_f32_16x16x32_bf16 v[16:19], a[92:95], a[44:47], v[16:19]\n"
"ds_read_b64_tr_b16 a[172:173], v6 offset:34304\n"
"v_cvt_pk_bf16_f32 v248, v24, v25\n"
"v_mfma_f32_16x16x32_bf16 v[20:23], a[108:111], a[44:47], v[48:51]\n"
"v_exp_f32 v26, v26\n"
"v_mfma_f32_16x16x32_bf16 v[20:23], a[104:107], a[40:43], v[20:23]\n"
"ds_read_b64_tr_b16 a[174:175], v6 offset:38400\n"
"v_mfma_f32_16x16x32_bf16 v[20:23], a[100:103], a[36:39], v[20:23]\n"
"v_exp_f32 v27, v27\n"
"v_mfma_f32_16x16x32_bf16 v[20:23], a[96:99], a[32:35], v[20:23]\n"
"ds_read_b64_tr_b16 a[204:205], v6 offset:42496\n"
"v_cvt_pk_bf16_f32 v249, v26, v27\n"
"v_mfma_f32_16x16x32_bf16 v[60:63], a[208:211], v[36:39], v[60:63]\n"
"s_waitcnt lgkmcnt(11)\n"
"v_exp_f32 v28, v28\n"
"v_mfma_f32_16x16x32_bf16 v[96:99], a[192:195], v[36:39], v[96:99]\n"
"ds_read_b64_tr_b16 a[206:207], v6 offset:46592\n"
"v_mfma_f32_16x16x32_bf16 v[96:99], a[160:163], v[32:35], v[96:99]\n"
"s_waitcnt lgkmcnt(8)\n"
"v_exp_f32 v29, v29\n"
"v_mfma_f32_16x16x32_bf16 v[100:103], a[164:167], v[32:35], v[100:103]\n"
"v_cvt_pk_bf16_f32 v250, v28, v29\n"
"v_mfma_f32_16x16x32_bf16 v[100:103], a[196:199], v[36:39], v[100:103]\n"
"s_waitcnt lgkmcnt(4)\n"
"v_exp_f32 v30, v30\n"
"v_mfma_f32_16x16x32_bf16 v[104:107], a[200:203], v[36:39], v[104:107]\n"
"buffer_store_dwordx4 a[216:219], v9, s[56:59], s69 offen sc1\n"
"v_mfma_f32_16x16x32_bf16 v[104:107], a[168:171], v[32:35], v[104:107]\n"
"s_waitcnt lgkmcnt(0)\n"
"v_exp_f32 v31, v31\n"
"v_mfma_f32_16x16x32_bf16 v[108:111], a[172:175], v[32:35], v[108:111]\n"
"v_cvt_pk_bf16_f32 v251, v30, v31\n"
"v_mfma_f32_16x16x32_bf16 v[108:111], a[204:207], v[36:39], v[108:111]\n"
"v_mfma_f32_16x16x32_bf16 v[24:27], a[112:115], a[32:35], v[48:51]\n"
"v_exp_f32 v16, v16\n"
"v_mfma_f32_16x16x32_bf16 v[24:27], a[116:119], a[36:39], v[24:27]\n"
"v_mfma_f32_16x16x32_bf16 v[24:27], a[120:123], a[40:43], v[24:27]\n"
"v_exp_f32 v17, v17\n"
"v_mfma_f32_16x16x32_bf16 v[24:27], a[124:127], a[44:47], v[24:27]\n"
"v_cvt_pk_bf16_f32 v32, v16, v17\n"
"v_mfma_f32_16x16x32_bf16 v[28:31], a[140:143], a[44:47], v[48:51]\n"
"v_exp_f32 v18, v18\n"
"v_mfma_f32_16x16x32_bf16 v[28:31], a[136:139], a[40:43], v[28:31]\n"
"v_mfma_f32_16x16x32_bf16 v[28:31], a[132:135], a[36:39], v[28:31]\n"
"v_exp_f32 v19, v19\n"
"v_mfma_f32_16x16x32_bf16 v[28:31], a[128:131], a[32:35], v[28:31]\n"
"v_cvt_pk_bf16_f32 v33, v18, v19\n"
"v_mfma_f32_16x16x32_bf16 v[64:67], a[208:211], v[244:247], v[64:67]\n"
"s_add_u32 m0, s84, 0\n"
"v_exp_f32 v20, v20\n"
"v_mfma_f32_16x16x32_bf16 v[112:115], a[144:147], v[244:247], v[112:115]\n"
"buffer_load_dwordx4 v8, s[48:51], s86 offen offset:0 nt lds\n"
"v_mfma_f32_16x16x32_bf16 v[112:115], a[176:179], v[248:251], v[112:115]\n"
"v_exp_f32 v21, v21\n"
"v_mfma_f32_16x16x32_bf16 v[116:119], a[180:183], v[248:251], v[116:119]\n"
"v_cvt_pk_bf16_f32 v34, v20, v21\n"
"v_mfma_f32_16x16x32_bf16 v[116:119], a[148:151], v[244:247], v[116:119]\n"
"v_exp_f32 v22, v22\n"
"v_mfma_f32_16x16x32_bf16 v[120:123], a[152:155], v[244:247], v[120:123]\n"
"v_mfma_f32_16x16x32_bf16 v[120:123], a[184:187], v[248:251], v[120:123]\n"
"v_exp_f32 v23, v23\n"
"v_mfma_f32_16x16x32_bf16 v[124:127], a[188:191], v[248:251], v[124:127]\n"
"v_cvt_pk_bf16_f32 v35, v22, v23\n"
"v_mfma_f32_16x16x32_bf16 v[124:127], a[156:159], v[244:247], v[124:127]\n"
"buffer_load_dwordx4 v8, s[48:51], s87 offen offset:1024 nt lds\n"
"v_mfma_f32_16x16x32_bf16 v[16:19], a[80:83], a[48:51], v[52:55]\n"
"v_exp_f32 v24, v24\n"
"v_mfma_f32_16x16x32_bf16 v[16:19], a[84:87], a[52:55], v[16:19]\n"
"v_mfma_f32_16x16x32_bf16 v[16:19], a[88:91], a[56:59], v[16:19]\n"
"v_exp_f32 v25, v25\n"
"v_mfma_f32_16x16x32_bf16 v[16:19], a[92:95], a[60:63], v[16:19]\n"
"v_cvt_pk_bf16_f32 v36, v24, v25\n"
"v_mfma_f32_16x16x32_bf16 v[20:23], a[108:111], a[60:63], v[52:55]\n"
"v_exp_f32 v26, v26\n"
"v_mfma_f32_16x16x32_bf16 v[20:23], a[104:107], a[56:59], v[20:23]\n"
"buffer_store_dwordx4 a[220:223], v9, s[56:59], s70 offen sc1\n"
"v_mfma_f32_16x16x32_bf16 v[20:23], a[100:103], a[52:55], v[20:23]\n"
"v_exp_f32 v27, v27\n"
"v_mfma_f32_16x16x32_bf16 v[20:23], a[96:99], a[48:51], v[20:23]\n"
"v_cvt_pk_bf16_f32 v37, v26, v27\n"
"v_mfma_f32_16x16x32_bf16 v[64:67], a[208:211], v[248:251], v[64:67]\n"
"v_exp_f32 v28, v28\n"
"v_mfma_f32_16x16x32_bf16 v[128:131], a[192:195], v[248:251], v[128:131]\n"
"v_mfma_f32_16x16x32_bf16 v[128:131], a[160:163], v[244:247], v[128:131]\n"
"v_exp_f32 v29, v29\n"
"v_mfma_f32_16x16x32_bf16 v[132:135], a[164:167], v[244:247], v[132:135]\n"
"v_cvt_pk_bf16_f32 v38, v28, v29\n"
"v_mfma_f32_16x16x32_bf16 v[132:135], a[196:199], v[248:251], v[132:135]\n"
"v_exp_f32 v30, v30\n"
"v_mfma_f32_16x16x32_bf16 v[136:139], a[200:203], v[248:251], v[136:139]\n"
"v_mfma_f32_16x16x32_bf16 v[136:139], a[168:171], v[244:247], v[136:139]\n"
"v_exp_f32 v31, v31\n"
"v_mfma_f32_16x16x32_bf16 v[140:143], a[172:175], v[244:247], v[140:143]\n"
"v_cvt_pk_bf16_f32 v39, v30, v31\n"
"v_mfma_f32_16x16x32_bf16 v[140:143], a[204:207], v[248:251], v[140:143]\n"
"buffer_load_dwordx4 v8, s[48:51], s86 offen offset:2048 nt lds\n"
"v_mfma_f32_16x16x32_bf16 v[24:27], a[112:115], a[48:51], v[52:55]\n"
"v_exp_f32 v16, v16\n"
"v_mfma_f32_16x16x32_bf16 v[24:27], a[116:119], a[52:55], v[24:27]\n"
"v_mfma_f32_16x16x32_bf16 v[24:27], a[120:123], a[56:59], v[24:27]\n"
"v_exp_f32 v17, v17\n"
"v_mfma_f32_16x16x32_bf16 v[24:27], a[124:127], a[60:63], v[24:27]\n"
"v_cvt_pk_bf16_f32 v244, v16, v17\n"
"v_mfma_f32_16x16x32_bf16 v[28:31], a[140:143], a[60:63], v[52:55]\n"
"v_exp_f32 v18, v18\n"
"v_mfma_f32_16x16x32_bf16 v[28:31], a[136:139], a[56:59], v[28:31]\n"
"v_mfma_f32_16x16x32_bf16 v[28:31], a[132:135], a[52:55], v[28:31]\n"
"v_exp_f32 v19, v19\n"
"v_mfma_f32_16x16x32_bf16 v[28:31], a[128:131], a[48:51], v[28:31]\n"
"v_cvt_pk_bf16_f32 v245, v18, v19\n"
"v_mfma_f32_16x16x32_bf16 v[68:71], a[208:211], v[32:35], v[68:71]\n"
"v_exp_f32 v20, v20\n"
"v_mfma_f32_16x16x32_bf16 v[144:147], a[144:147], v[32:35], v[144:147]\n"
"buffer_load_dwordx4 v8, s[48:51], s87 offen offset:3072 nt lds\n"
"v_mfma_f32_16x16x32_bf16 v[144:147], a[176:179], v[36:39], v[144:147]\n"
"s_add_u32 s86, s86, 16384\n"
"s_add_u32 s87, s87, 16384\n"
"v_exp_f32 v21, v21\n"
"v_mfma_f32_16x16x32_bf16 v[148:151], a[180:183], v[36:39], v[148:151]\n"
"v_cvt_pk_bf16_f32 v246, v20, v21\n"
"v_mfma_f32_16x16x32_bf16 v[148:151], a[148:151], v[32:35], v[148:151]\n"
"v_exp_f32 v22, v22\n"
"v_mfma_f32_16x16x32_bf16 v[152:155], a[152:155], v[32:35], v[152:155]\n"
"v_mfma_f32_16x16x32_bf16 v[152:155], a[184:187], v[36:39], v[152:155]\n"
"v_exp_f32 v23, v23\n"
"v_mfma_f32_16x16x32_bf16 v[156:159], a[188:191], v[36:39], v[156:159]\n"
"v_cvt_pk_bf16_f32 v247, v22, v23\n"
"v_mfma_f32_16x16x32_bf16 v[156:159], a[156:159], v[32:35], v[156:159]\n"
"buffer_load_dword v10, off, s[4:7], s9\n"
"s_add_u32 s9, s9, 4096\n"
"v_mfma_f32_16x16x32_bf16 v[16:19], a[80:83], a[64:67], v[56:59]\n"
"s_waitcnt vmcnt(11) lgkmcnt(0)\n"
"s_barrier\n"
"s_cmp_ge_u32 s25, 1020\n"
"s_cbranch_scc0 Lld4_%=\n"
"buffer_store_dword a252, v13, s[64:67], 0 offen offset:0 sc1\n"
"buffer_store_dword a253, v13, s[64:67], 0 offen offset:8 sc1\n"
"buffer_store_dword a254, v13, s[64:67], 0 offen offset:16 sc1\n"
"buffer_store_dword a255, v13, s[64:67], 0 offen offset:24 sc1\n"
"s_nop 1\n"
"buffer_load_dwordx4 a[252:255], v12, s[64:67], 0 offen sc1\n"
"Lld4_%=:\n"
"v_exp_f32 v24, v24\n"
"v_mfma_f32_16x16x32_bf16 v[16:19], a[84:87], a[68:71], v[16:19]\n"
"ds_read_b128 a[80:83], v1 offset:49152\n"
"v_mfma_f32_16x16x32_bf16 v[16:19], a[88:91], a[72:75], v[16:19]\n"
"v_exp_f32 v25, v25\n"
"v_mfma_f32_16x16x32_bf16 v[16:19], a[92:95], a[76:79], v[16:19]\n"
"ds_read_b128 a[84:87], v2 offset:49152\n"
"v_cvt_pk_bf16_f32 v248, v24, v25\n"
"v_mfma_f32_16x16x32_bf16 v[20:23], a[108:111], a[76:79], v[56:59]\n"
"v_exp_f32 v26, v26\n"
"v_mfma_f32_16x16x32_bf16 v[20:23], a[104:107], a[72:75], v[20:23]\n"
"ds_read_b128 a[88:91], v3 offset:49152\n"
"v_mfma_f32_16x16x32_bf16 v[20:23], a[100:103], a[68:71], v[20:23]\n"
"v_exp_f32 v27, v27\n"
"v_mfma_f32_16x16x32_bf16 v[20:23], a[96:99], a[64:67], v[20:23]\n"
"ds_read_b128 a[92:95], v4 offset:49152\n"
"v_cvt_pk_bf16_f32 v249, v26, v27\n"
"v_mfma_f32_16x16x32_bf16 v[68:71], a[208:211], v[36:39], v[68:71]\n"
"v_exp_f32 v28, v28\n"
"v_mfma_f32_16x16x32_bf16 v[160:163], a[192:195], v[36:39], v[160:163]\n"
"ds_read_b128 a[108:111], v4 offset:53248\n"
"v_mfma_f32_16x16x32_bf16 v[160:163], a[160:163], v[32:35], v[160:163]\n"
"v_exp_f32 v29, v29\n"
"v_mfma_f32_16x16x32_bf16 v[164:167], a[164:167], v[32:35], v[164:167]\n"
"ds_read_b128 a[104:107], v3 offset:53248\n"
"v_cvt_pk_bf16_f32 v250, v28, v29\n"
"v_mfma_f32_16x16x32_bf16 v[164:167], a[196:199], v[36:39], v[164:167]\n"
"v_exp_f32 v30, v30\n"
"v_mfma_f32_16x16x32_bf16 v[168:171], a[200:203], v[36:39], v[168:171]\n"
"ds_read_b128 a[100:103], v2 offset:53248\n"
"v_mfma_f32_16x16x32_bf16 v[168:171], a[168:171], v[32:35], v[168:171]\n"
"v_exp_f32 v31, v31\n"
"v_mfma_f32_16x16x32_bf16 v[172:175], a[172:175], v[32:35], v[172:175]\n"
"s_add_u32 m0, s83, 16384\n"
"ds_read_b128 a[96:99], v1 offset:53248\n"
"v_cvt_pk_bf16_f32 v251, v30, v31\n"
"v_mfma_f32_16x16x32_bf16 v[172:175], a[204:207], v[36:39], v[172:175]\n"
"buffer_load_dwordx4 v240, s[44:47], s85 offen offset:0 nt lds\n"
"v_mfma_f32_16x16x32_bf16 v[24:27], a[112:115], a[64:67], v[56:59]\n"
"v_exp_f32 v16, v16\n"
"v_mfma_f32_16x16x32_bf16 v[24:27], a[116:119], a[68:71], v[24:27]\n"
"ds_read_b128 a[112:115], v1 offset:57344\n"
"v_mfma_f32_16x16x32_bf16 v[24:27], a[120:123], a[72:75], v[24:27]\n"
"v_exp_f32 v17, v17\n"
"v_mfma_f32_16x16x32_bf16 v[24:27], a[124:127], a[76:79], v[24:27]\n"
"ds_read_b128 a[116:119], v2 offset:57344\n"
"v_cvt_pk_bf16_f32 v32, v16, v17\n"
"v_mfma_f32_16x16x32_bf16 v[28:31], a[140:143], a[76:79], v[56:59]\n"
"v_exp_f32 v18, v18\n"
"v_mfma_f32_16x16x32_bf16 v[28:31], a[136:139], a[72:75], v[28:31]\n"
"ds_read_b128 a[120:123], v3 offset:57344\n"
"v_mfma_f32_16x16x32_bf16 v[28:31], a[132:135], a[68:71], v[28:31]\n"
"v_exp_f32 v19, v19\n"
"v_mfma_f32_16x16x32_bf16 v[28:31], a[128:131], a[64:67], v[28:31]\n"
"ds_read_b128 a[124:127], v4 offset:57344\n"
"v_cvt_pk_bf16_f32 v33, v18, v19\n"
"v_mfma_f32_16x16x32_bf16 v[72:75], a[208:211], v[244:247], v[72:75]\n"
"v_exp_f32 v20, v20\n"
"v_mfma_f32_16x16x32_bf16 v[176:179], a[144:147], v[244:247], v[176:179]\n"
"ds_read_b128 a[140:143], v4 offset:61440\n"
"v_mfma_f32_16x16x32_bf16 v[176:179], a[176:179], v[248:251], v[176:179]\n"
"v_exp_f32 v21, v21\n"
"v_mfma_f32_16x16x32_bf16 v[180:183], a[180:183], v[248:251], v[180:183]\n"
"ds_read_b128 a[136:139], v3 offset:61440\n"
"v_cvt_pk_bf16_f32 v34, v20, v21\n"
"v_mfma_f32_16x16x32_bf16 v[180:183], a[148:151], v[244:247], v[180:183]\n"
"v_exp_f32 v22, v22\n"
"v_mfma_f32_16x16x32_bf16 v[184:187], a[152:155], v[244:247], v[184:187]\n"
"ds_read_b128 a[132:135], v2 offset:61440\n"
"v_mfma_f32_16x16x32_bf16 v[184:187], a[184:187], v[248:251], v[184:187]\n"
"v_exp_f32 v23, v23\n"
"v_mfma_f32_16x16x32_bf16 v[188:191], a[188:191], v[248:251], v[188:191]\n"
"ds_read_b128 a[128:131], v1 offset:61440\n"
"v_cvt_pk_bf16_f32 v35, v22, v23\n"
"v_mfma_f32_16x16x32_bf16 v[188:191], a[156:159], v[244:247], v[188:191]\n"
"s_waitcnt lgkmcnt(8)\n"
"buffer_load_dwordx4 v241, s[44:47], s85 offen offset:1024 nt lds\n"
"v_mfma_f32_16x16x32_bf16 v[16:19], a[80:83], a[0:3], v[40:43]\n"
"v_exp_f32 v24, v24\n"
"v_mfma_f32_16x16x32_bf16 v[16:19], a[84:87], a[4:7], v[16:19]\n"
"v_mfma_f32_16x16x32_bf16 v[16:19], a[88:91], a[8:11], v[16:19]\n"
"v_exp_f32 v25, v25\n"
"v_mfma_f32_16x16x32_bf16 v[16:19], a[92:95], a[12:15], v[16:19]\n"
"v_cvt_pk_bf16_f32 v36, v24, v25\n"
"v_mfma_f32_16x16x32_bf16 v[20:23], a[108:111], a[12:15], v[40:43]\n"
"v_exp_f32 v26, v26\n"
"v_mfma_f32_16x16x32_bf16 v[20:23], a[104:107], a[8:11], v[20:23]\n"
"buffer_store_dwordx4 a[224:227], v9, s[56:59], s71 offen sc1\n"
"v_mfma_f32_16x16x32_bf16 v[20:23], a[100:103], a[4:7], v[20:23]\n"
"v_exp_f32 v27, v27\n"
"v_mfma_f32_16x16x32_bf16 v[20:23], a[96:99], a[0:3], v[20:23]\n"
"v_cvt_pk_bf16_f32 v37, v26, v27\n"
"v_mfma_f32_16x16x32_bf16 v[72:75], a[208:211], v[248:251], v[72:75]\n"
"v_exp_f32 v28, v28\n"
"v_mfma_f32_16x16x32_bf16 v[192:195], a[192:195], v[248:251], v[192:195]\n"
"v_mfma_f32_16x16x32_bf16 v[192:195], a[160:163], v[244:247], v[192:195]\n"
"v_exp_f32 v29, v29\n"
"v_mfma_f32_16x16x32_bf16 v[196:199], a[164:167], v[244:247], v[196:199]\n"
"v_cvt_pk_bf16_f32 v38, v28, v29\n"
"v_mfma_f32_16x16x32_bf16 v[196:199], a[196:199], v[248:251], v[196:199]\n"
"v_exp_f32 v30, v30\n"
"v_mfma_f32_16x16x32_bf16 v[200:203], a[200:203], v[248:251], v[200:203]\n"
"buffer_load_dwordx4 v242, s[44:47], s85 offen offset:2048 nt lds\n"
"v_mfma_f32_16x16x32_bf16 v[200:203], a[168:171], v[244:247], v[200:203]\n"
"v_exp_f32 v31, v31\n"
"v_mfma_f32_16x16x32_bf16 v[204:207], a[172:175], v[244:247], v[204:207]\n"
"v_cvt_pk_bf16_f32 v39, v30, v31\n"
"v_mfma_f32_16x16x32_bf16 v[204:207], a[204:207], v[248:251], v[204:207]\n"
"s_waitcnt lgkmcnt(0)\n"
"v_mfma_f32_16x16x32_bf16 v[24:27], a[112:115], a[0:3], v[40:43]\n"
"v_exp_f32 v16, v16\n"
"v_mfma_f32_16x16x32_bf16 v[24:27], a[116:119], a[4:7], v[24:27]\n"
"v_mfma_f32_16x16x32_bf16 v[24:27], a[120:123], a[8:11], v[24:27]\n"
"v_exp_f32 v17, v17\n"
"v_mfma_f32_16x16x32_bf16 v[24:27], a[124:127], a[12:15], v[24:27]\n"
"v_cvt_pk_bf16_f32 v244, v16, v17\n"
"v_mfma_f32_16x16x32_bf16 v[28:31], a[140:143], a[12:15], v[40:43]\n"
"v_exp_f32 v18, v18\n"
"v_mfma_f32_16x16x32_bf16 v[28:31], a[136:139], a[8:11], v[28:31]\n"
"buffer_load_dwordx4 v243, s[44:47], s85 offen offset:3072 nt lds\n"
"v_mfma_f32_16x16x32_bf16 v[28:31], a[132:135], a[4:7], v[28:31]\n"
"s_add_u32 s85, s85, 16384\n"
"v_exp_f32 v19, v19\n"
"v_mfma_f32_16x16x32_bf16 v[28:31], a[128:131], a[0:3], v[28:31]\n"
"v_cvt_pk_bf16_f32 v245, v18, v19\n"
"v_mfma_f32_16x16x32_bf16 v[76:79], a[208:211], v[32:35], v[76:79]\n"
"v_exp_f32 v20, v20\n"
"v_mfma_f32_16x16x32_bf16 v[208:211], a[144:147], v[32:35], v[208:211]\n"
"v_mfma_f32_16x16x32_bf16 v[208:211], a[176:179], v[36:39], v[208:211]\n"
"v_exp_f32 v21, v21\n"
"v_mfma_f32_16x16x32_bf16 v[212:215], a[180:183], v[36:39], v[212:215]\n"
"ds_read_b64_tr_b16 a[144:145], v5 offset:49152\n"
"v_cvt_pk_bf16_f32 v246, v20, v21\n"
"v_mfma_f32_16x16x32_bf16 v[212:215], a[148:151], v[32:35], v[212:215]\n"
"v_exp_f32 v22, v22\n"
"v_mfma_f32_16x16x32_bf16 v[216:219], a[152:155], v[32:35], v[216:219]\n"
"ds_read_b64_tr_b16 a[146:147], v5 offset:53248\n"
"v_mfma_f32_16x16x32_bf16 v[216:219], a[184:187], v[36:39], v[216:219]\n"
"v_exp_f32 v23, v23\n"
"v_mfma_f32_16x16x32_bf16 v[220:223], a[188:191], v[36:39], v[220:223]\n"
"ds_read_b64_tr_b16 a[176:177], v5 offset:57344\n"
"v_cvt_pk_bf16_f32 v247, v22, v23\n"
"v_mfma_f32_16x16x32_bf16 v[220:223], a[156:159], v[32:35], v[220:223]\n"
"ds_read_b64_tr_b16 a[178:179], v5 offset:61440\n"
"v_mfma_f32_16x16x32_bf16 v[16:19], a[80:83], a[16:19], v[44:47]\n"
"v_exp_f32 v24, v24\n"
"v_mfma_f32_16x16x32_bf16 v[16:19], a[84:87], a[20:23], v[16:19]\n"
"ds_read_b64_tr_b16 a[148:149], v6 offset:49152\n"
"v_mfma_f32_16x16x32_bf16 v[16:19], a[88:91], a[24:27], v[16:19]\n"
"ds_read_b64_tr_b16 a[150:151], v6 offset:53248\n"
"v_exp_f32 v25, v25\n"
"v_mfma_f32_16x16x32_bf16 v[16:19], a[92:95], a[28:31], v[16:19]\n"
"ds_read_b64_tr_b16 a[180:181], v6 offset:57344\n"
"v_cvt_pk_bf16_f32 v248, v24, v25\n"
"v_mfma_f32_16x16x32_bf16 v[20:23], a[108:111], a[28:31], v[44:47]\n"
"v_exp_f32 v26, v26\n"
"v_mfma_f32_16x16x32_bf16 v[20:23], a[104:107], a[24:27], v[20:23]\n"
"ds_read_b64_tr_b16 a[182:183], v6 offset:61440\n"
"v_mfma_f32_16x16x32_bf16 v[20:23], a[100:103], a[20:23], v[20:23]\n"
"v_exp_f32 v27, v27\n"
"v_mfma_f32_16x16x32_bf16 v[20:23], a[96:99], a[16:19], v[20:23]\n"
"ds_read_b64_tr_b16 a[152:153], v5 offset:49664\n"
"v_cvt_pk_bf16_f32 v249, v26, v27\n"
"v_mfma_f32_16x16x32_bf16 v[76:79], a[208:211], v[36:39], v[76:79]\n"
"ds_read_b64_tr_b16 a[154:155], v5 offset:53760\n"
"v_exp_f32 v28, v28\n"
"v_mfma_f32_16x16x32_bf16 v[224:227], a[192:195], v[36:39], v[224:227]\n"
"ds_read_b64_tr_b16 a[184:185], v5 offset:57856\n"
"v_mfma_f32_16x16x32_bf16 v[224:227], a[160:163], v[32:35], v[224:227]\n"
"v_exp_f32 v29, v29\n"
"v_mfma_f32_16x16x32_bf16 v[228:231], a[164:167], v[32:35], v[228:231]\n"
"ds_read_b64_tr_b16 a[186:187], v5 offset:61952\n"
"v_cvt_pk_bf16_f32 v250, v28, v29\n"
"v_mfma_f32_16x16x32_bf16 v[228:231], a[196:199], v[36:39], v[228:231]\n"
"v_exp_f32 v30, v30\n"
"v_mfma_f32_16x16x32_bf16 v[232:235], a[200:203], v[36:39], v[232:235]\n"
"ds_read_b64_tr_b16 a[156:157], v6 offset:49664\n"
"v_mfma_f32_16x16x32_bf16 v[232:235], a[168:171], v[32:35], v[232:235]\n"
"ds_read_b64_tr_b16 a[158:159], v6 offset:53760\n"
"v_exp_f32 v31, v31\n"
"v_mfma_f32_16x16x32_bf16 v[236:239], a[172:175], v[32:35], v[236:239]\n"
"ds_read_b64_tr_b16 a[188:189], v6 offset:57856\n"
"v_cvt_pk_bf16_f32 v251, v30, v31\n"
"v_mfma_f32_16x16x32_bf16 v[236:239], a[204:207], v[36:39], v[236:239]\n"
"ds_read_b64_tr_b16 a[190:191], v6 offset:61952\n"
"v_mfma_f32_16x16x32_bf16 v[24:27], a[112:115], a[16:19], v[44:47]\n"
"v_exp_f32 v16, v16\n"
"v_mfma_f32_16x16x32_bf16 v[24:27], a[116:119], a[20:23], v[24:27]\n"
"ds_read_b64_tr_b16 a[160:161], v5 offset:50176\n"
"v_mfma_f32_16x16x32_bf16 v[24:27], a[120:123], a[24:27], v[24:27]\n"
"ds_read_b64_tr_b16 a[162:163], v5 offset:54272\n"
"v_exp_f32 v17, v17\n"
"v_mfma_f32_16x16x32_bf16 v[24:27], a[124:127], a[28:31], v[24:27]\n"
"ds_read_b64_tr_b16 a[192:193], v5 offset:58368\n"
"v_cvt_pk_bf16_f32 v32, v16, v17\n"
"v_mfma_f32_16x16x32_bf16 v[28:31], a[140:143], a[28:31], v[44:47]\n"
"v_exp_f32 v18, v18\n"
"v_mfma_f32_16x16x32_bf16 v[28:31], a[136:139], a[24:27], v[28:31]\n"
"ds_read_b64_tr_b16 a[194:195], v5 offset:62464\n"
"v_mfma_f32_16x16x32_bf16 v[28:31], a[132:135], a[20:23], v[28:31]\n"
"v_exp_f32 v19, v19\n"
"v_mfma_f32_16x16x32_bf16 v[28:31], a[128:131], a[16:19], v[28:31]\n"
"ds_read_b64_tr_b16 a[164:165], v6 offset:50176\n"
"v_cvt_pk_bf16_f32 v33, v18, v19\n"
"v_mfma_f32_16x16x32_bf16 v[60:63], a[208:211], v[244:247], v[60:63]\n"
"s_waitcnt lgkmcnt(15)\n"
"ds_read_b64_tr_b16 a[166:167], v6 offset:54272\n"
"v_exp_f32 v20, v20\n"
"v_mfma_f32_16x16x32_bf16 v[80:83], a[144:147], v[244:247], v[80:83]\n"
"ds_read_b64_tr_b16 a[196:197], v6 offset:58368\n"
"v_mfma_f32_16x16x32_bf16 v[80:83], a[176:179], v[248:251], v[80:83]\n"
"s_waitcnt lgkmcnt(15)\n"
"v_exp_f32 v21, v21\n"
"v_mfma_f32_16x16x32_bf16 v[84:87], a[180:183], v[248:251], v[84:87]\n"
"ds_read_b64_tr_b16 a[198:199], v6 offset:62464\n"
"v_cvt_pk_bf16_f32 v34, v20, v21\n"
"v_mfma_f32_16x16x32_bf16 v[84:87], a[148:151], v[244:247], v[84:87]\n"
"s_waitcnt lgkmcnt(12)\n"
"v_exp_f32 v22, v22\n"
"v_mfma_f32_16x16x32_bf16 v[88:91], a[152:155], v[244:247], v[88:91]\n"
"ds_read_b64_tr_b16 a[168:169], v5 offset:50688\n"
"v_mfma_f32_16x16x32_bf16 v[88:91], a[184:187], v[248:251], v[88:91]\n"
"s_waitcnt lgkmcnt(9)\n"
"v_exp_f32 v23, v23\n"
"v_mfma_f32_16x16x32_bf16 v[92:95], a[188:191], v[248:251], v[92:95]\n"
"ds_read_b64_tr_b16 a[170:171], v5 offset:54784\n"
"v_cvt_pk_bf16_f32 v35, v22, v23\n"
"v_mfma_f32_16x16x32_bf16 v[92:95], a[156:159], v[244:247], v[92:95]\n"
"ds_read_b64_tr_b16 a[200:201], v5 offset:58880\n"
"v_mfma_f32_16x16x32_bf16 v[16:19], a[80:83], a[32:35], v[48:51]\n"
"v_exp_f32 v24, v24\n"
"v_mfma_f32_16x16x32_bf16 v[16:19], a[84:87], a[36:39], v[16:19]\n"
"ds_read_b64_tr_b16 a[202:203], v5 offset:62976\n"
"v_mfma_f32_16x16x32_bf16 v[16:19], a[88:91], a[40:43], v[16:19]\n"
"v_exp_f32 v25, v25\n"
"v_mfma_f32_16x16x32_bf16 v[16:19], a[92:95], a[44:47], v[16:19]\n"
"ds_read_b64_tr_b16 a[172:173], v6 offset:50688\n"
"v_cvt_pk_bf16_f32 v36, v24, v25\n"
"v_mfma_f32_16x16x32_bf16 v[20:23], a[108:111], a[44:47], v[48:51]\n"
"v_exp_f32 v26, v26\n"
"v_mfma_f32_16x16x32_bf16 v[20:23], a[104:107], a[40:43], v[20:23]\n"
"ds_read_b64_tr_b16 a[174:175], v6 offset:54784\n"
"v_mfma_f32_16x16x32_bf16 v[20:23], a[100:103], a[36:39], v[20:23]\n"
"v_exp_f32 v27, v27\n"
"v_mfma_f32_16x16x32_bf16 v[20:23], a[96:99], a[32:35], v[20:23]\n"
"ds_read_b64_tr_b16 a[204:205], v6 offset:58880\n"
"v_cvt_pk_bf16_f32 v37, v26, v27\n"
"v_mfma_f32_16x16x32_bf16 v[60:63], a[208:211], v[248:251], v[60:63]\n"
"s_waitcnt lgkmcnt(11)\n"
"v_exp_f32 v28, v28\n"
"v_mfma_f32_16x16x32_bf16 v[96:99], a[192:195], v[248:251], v[96:99]\n"
"ds_read_b64_tr_b16 a[206:207], v6 offset:62976\n"
"v_mfma_f32_16x16x32_bf16 v[96:99], a[160:163], v[244:247], v[96:99]\n"
"s_waitcnt lgkmcnt(8)\n"
"v_exp_f32 v29, v29\n"
"v_mfma_f32_16x16x32_bf16 v[100:103], a[164:167], v[244:247], v[100:103]\n"
"v_cvt_pk_bf16_f32 v38, v28, v29\n"
"v_mfma_f32_16x16x32_bf16 v[100:103], a[196:199], v[248:251], v[100:103]\n"
"s_waitcnt lgkmcnt(4)\n"
"v_exp_f32 v30, v30\n"
"v_mfma_f32_16x16x32_bf16 v[104:107], a[200:203], v[248:251], v[104:107]\n"
"buffer_store_dwordx4 a[228:231], v9, s[56:59], s72 offen sc1\n"
"v_mfma_f32_16x16x32_bf16 v[104:107], a[168:171], v[244:247], v[104:107]\n"
"s_waitcnt lgkmcnt(0)\n"
"v_exp_f32 v31, v31\n"
"v_mfma_f32_16x16x32_bf16 v[108:111], a[172:175], v[244:247], v[108:111]\n"
"v_cvt_pk_bf16_f32 v39, v30, v31\n"
"v_mfma_f32_16x16x32_bf16 v[108:111], a[204:207], v[248:251], v[108:111]\n"
"v_mfma_f32_16x16x32_bf16 v[24:27], a[112:115], a[32:35], v[48:51]\n"
"v_exp_f32 v16, v16\n"
"v_mfma_f32_16x16x32_bf16 v[24:27], a[116:119], a[36:39], v[24:27]\n"
"v_mfma_f32_16x16x32_bf16 v[24:27], a[120:123], a[40:43], v[24:27]\n"
"v_exp_f32 v17, v17\n"
"v_mfma_f32_16x16x32_bf16 v[24:27], a[124:127], a[44:47], v[24:27]\n"
"v_cvt_pk_bf16_f32 v244, v16, v17\n"
"v_mfma_f32_16x16x32_bf16 v[28:31], a[140:143], a[44:47], v[48:51]\n"
"v_exp_f32 v18, v18\n"
"v_mfma_f32_16x16x32_bf16 v[28:31], a[136:139], a[40:43], v[28:31]\n"
"v_mfma_f32_16x16x32_bf16 v[28:31], a[132:135], a[36:39], v[28:31]\n"
"v_exp_f32 v19, v19\n"
"v_mfma_f32_16x16x32_bf16 v[28:31], a[128:131], a[32:35], v[28:31]\n"
"v_cvt_pk_bf16_f32 v245, v18, v19\n"
"v_mfma_f32_16x16x32_bf16 v[64:67], a[208:211], v[32:35], v[64:67]\n"
"s_add_u32 m0, s84, 16384\n"
"v_exp_f32 v20, v20\n"
"v_mfma_f32_16x16x32_bf16 v[112:115], a[144:147], v[32:35], v[112:115]\n"
"buffer_load_dwordx4 v8, s[48:51], s86 offen offset:0 nt lds\n"
"v_mfma_f32_16x16x32_bf16 v[112:115], a[176:179], v[36:39], v[112:115]\n"
"v_exp_f32 v21, v21\n"
"v_mfma_f32_16x16x32_bf16 v[116:119], a[180:183], v[36:39], v[116:119]\n"
"v_cvt_pk_bf16_f32 v246, v20, v21\n"
"v_mfma_f32_16x16x32_bf16 v[116:119], a[148:151], v[32:35], v[116:119]\n"
"v_exp_f32 v22, v22\n"
"v_mfma_f32_16x16x32_bf16 v[120:123], a[152:155], v[32:35], v[120:123]\n"
"v_mfma_f32_16x16x32_bf16 v[120:123], a[184:187], v[36:39], v[120:123]\n"
"v_exp_f32 v23, v23\n"
"v_mfma_f32_16x16x32_bf16 v[124:127], a[188:191], v[36:39], v[124:127]\n"
"v_cvt_pk_bf16_f32 v247, v22, v23\n"
"v_mfma_f32_16x16x32_bf16 v[124:127], a[156:159], v[32:35], v[124:127]\n"
"buffer_load_dwordx4 v8, s[48:51], s87 offen offset:1024 nt lds\n"
"v_mfma_f32_16x16x32_bf16 v[16:19], a[80:83], a[48:51], v[52:55]\n"
"v_exp_f32 v24, v24\n"
"v_mfma_f32_16x16x32_bf16 v[16:19], a[84:87], a[52:55], v[16:19]\n"
"v_mfma_f32_16x16x32_bf16 v[16:19], a[88:91], a[56:59], v[16:19]\n"
"v_exp_f32 v25, v25\n"
"v_mfma_f32_16x16x32_bf16 v[16:19], a[92:95], a[60:63], v[16:19]\n"
"v_cvt_pk_bf16_f32 v248, v24, v25\n"
"v_mfma_f32_16x16x32_bf16 v[20:23], a[108:111], a[60:63], v[52:55]\n"
"v_exp_f32 v26, v26\n"
"v_mfma_f32_16x16x32_bf16 v[20:23], a[104:107], a[56:59], v[20:23]\n"
"buffer_store_dwordx4 a[232:235], v9, s[56:59], s73 offen sc1\n"
"v_mfma_f32_16x16x32_bf16 v[20:23], a[100:103], a[52:55], v[20:23]\n"
"v_exp_f32 v27, v27\n"
"v_mfma_f32_16x16x32_bf16 v[20:23], a[96:99], a[48:51], v[20:23]\n"
"v_cvt_pk_bf16_f32 v249, v26, v27\n"
"v_mfma_f32_16x16x32_bf16 v[64:67], a[208:211], v[36:39], v[64:67]\n"
"v_exp_f32 v28, v28\n"
"v_mfma_f32_16x16x32_bf16 v[128:131], a[192:195], v[36:39], v[128:131]\n"
"v_mfma_f32_16x16x32_bf16 v[128:131], a[160:163], v[32:35], v[128:131]\n"
"v_exp_f32 v29, v29\n"
"v_mfma_f32_16x16x32_bf16 v[132:135], a[164:167], v[32:35], v[132:135]\n"
"v_cvt_pk_bf16_f32 v250, v28, v29\n"
"v_mfma_f32_16x16x32_bf16 v[132:135], a[196:199], v[36:39], v[132:135]\n"
"v_exp_f32 v30, v30\n"
"v_mfma_f32_16x16x32_bf16 v[136:139], a[200:203], v[36:39], v[136:139]\n"
"v_mfma_f32_16x16x32_bf16 v[136:139], a[168:171], v[32:35], v[136:139]\n"
"v_exp_f32 v31, v31\n"
"v_mfma_f32_16x16x32_bf16 v[140:143], a[172:175], v[32:35], v[140:143]\n"
"v_cvt_pk_bf16_f32 v251, v30, v31\n"
"v_mfma_f32_16x16x32_bf16 v[140:143], a[204:207], v[36:39], v[140:143]\n"
"buffer_load_dwordx4 v8, s[48:51], s86 offen offset:2048 nt lds\n"
"v_mfma_f32_16x16x32_bf16 v[24:27], a[112:115], a[48:51], v[52:55]\n"
"v_exp_f32 v16, v16\n"
"v_mfma_f32_16x16x32_bf16 v[24:27], a[116:119], a[52:55], v[24:27]\n"
"v_mfma_f32_16x16x32_bf16 v[24:27], a[120:123], a[56:59], v[24:27]\n"
"v_exp_f32 v17, v17\n"
"v_mfma_f32_16x16x32_bf16 v[24:27], a[124:127], a[60:63], v[24:27]\n"
"v_cvt_pk_bf16_f32 v32, v16, v17\n"
"v_mfma_f32_16x16x32_bf16 v[28:31], a[140:143], a[60:63], v[52:55]\n"
"v_exp_f32 v18, v18\n"
"v_mfma_f32_16x16x32_bf16 v[28:31], a[136:139], a[56:59], v[28:31]\n"
"v_mfma_f32_16x16x32_bf16 v[28:31], a[132:135], a[52:55], v[28:31]\n"
"v_exp_f32 v19, v19\n"
"v_mfma_f32_16x16x32_bf16 v[28:31], a[128:131], a[48:51], v[28:31]\n"
"v_cvt_pk_bf16_f32 v33, v18, v19\n"
"v_mfma_f32_16x16x32_bf16 v[68:71], a[208:211], v[244:247], v[68:71]\n"
"v_exp_f32 v20, v20\n"
"v_mfma_f32_16x16x32_bf16 v[144:147], a[144:147], v[244:247], v[144:147]\n"
"buffer_load_dwordx4 v8, s[48:51], s87 offen offset:3072 nt lds\n"
"v_mfma_f32_16x16x32_bf16 v[144:147], a[176:179], v[248:251], v[144:147]\n"
"s_add_u32 s86, s86, 16384\n"
"s_add_u32 s87, s87, 16384\n"
"v_exp_f32 v21, v21\n"
"v_mfma_f32_16x16x32_bf16 v[148:151], a[180:183], v[248:251], v[148:151]\n"
"v_cvt_pk_bf16_f32 v34, v20, v21\n"
"v_mfma_f32_16x16x32_bf16 v[148:151], a[148:151], v[244:247], v[148:151]\n"
"v_exp_f32 v22, v22\n"
"v_mfma_f32_16x16x32_bf16 v[152:155], a[152:155], v[244:247], v[152:155]\n"
"v_mfma_f32_16x16x32_bf16 v[152:155], a[184:187], v[248:251], v[152:155]\n"
"v_exp_f32 v23, v23\n"
"v_mfma_f32_16x16x32_bf16 v[156:159], a[188:191], v[248:251], v[156:159]\n"
"v_cvt_pk_bf16_f32 v35, v22, v23\n"
"v_mfma_f32_16x16x32_bf16 v[156:159], a[156:159], v[244:247], v[156:159]\n"
"buffer_load_dword v10, off, s[4:7], s9\n"
"s_add_u32 s9, s9, 4096\n"
"v_mfma_f32_16x16x32_bf16 v[16:19], a[80:83], a[64:67], v[56:59]\n"
"s_waitcnt vmcnt(11) lgkmcnt(0)\n"
"s_barrier\n"
"s_cmp_ge_u32 s25, 1020\n"
"s_cbranch_scc0 Lld5_%=\n"
"s_waitcnt vmcnt(8)\n"
"buffer_store_dword a252, v13, s[64:67], 0 offen offset:0 sc1\n"
"buffer_store_dword a253, v13, s[64:67], 0 offen offset:8 sc1\n"
"buffer_store_dword a254, v13, s[64:67], 0 offen offset:16 sc1\n"
"buffer_store_dword a255, v13, s[64:67], 0 offen offset:24 sc1\n"
"Lld5_%=:\n"
"v_exp_f32 v24, v24\n"
"v_mfma_f32_16x16x32_bf16 v[16:19], a[84:87], a[68:71], v[16:19]\n"
"ds_read_b128 a[80:83], v1 offset:0\n"
"v_mfma_f32_16x16x32_bf16 v[16:19], a[88:91], a[72:75], v[16:19]\n"
"v_exp_f32 v25, v25\n"
"v_mfma_f32_16x16x32_bf16 v[16:19], a[92:95], a[76:79], v[16:19]\n"
"ds_read_b128 a[84:87], v2 offset:0\n"
"v_cvt_pk_bf16_f32 v36, v24, v25\n"
"v_mfma_f32_16x16x32_bf16 v[20:23], a[108:111], a[76:79], v[56:59]\n"
"v_exp_f32 v26, v26\n"
"v_mfma_f32_16x16x32_bf16 v[20:23], a[104:107], a[72:75], v[20:23]\n"
"ds_read_b128 a[88:91], v3 offset:0\n"
"v_mfma_f32_16x16x32_bf16 v[20:23], a[100:103], a[68:71], v[20:23]\n"
"v_exp_f32 v27, v27\n"
"v_mfma_f32_16x16x32_bf16 v[20:23], a[96:99], a[64:67], v[20:23]\n"
"ds_read_b128 a[92:95], v4 offset:0\n"
"v_cvt_pk_bf16_f32 v37, v26, v27\n"
"v_mfma_f32_16x16x32_bf16 v[68:71], a[208:211], v[248:251], v[68:71]\n"
"v_exp_f32 v28, v28\n"
"v_mfma_f32_16x16x32_bf16 v[160:163], a[192:195], v[248:251], v[160:163]\n"
"ds_read_b128 a[108:111], v4 offset:4096\n"
"v_mfma_f32_16x16x32_bf16 v[160:163], a[160:163], v[244:247], v[160:163]\n"
"v_exp_f32 v29, v29\n"
"v_mfma_f32_16x16x32_bf16 v[164:167], a[164:167], v[244:247], v[164:167]\n"
"ds_read_b128 a[104:107], v3 offset:4096\n"
"v_cvt_pk_bf16_f32 v38, v28, v29\n"
"v_mfma_f32_16x16x32_bf16 v[164:167], a[196:199], v[248:251], v[164:167]\n"
"v_exp_f32 v30, v30\n"
"v_mfma_f32_16x16x32_bf16 v[168:171], a[200:203], v[248:251], v[168:171]\n"
"ds_read_b128 a[100:103], v2 offset:4096\n"
"v_mfma_f32_16x16x32_bf16 v[168:171], a[168:171], v[244:247], v[168:171]\n"
"v_exp_f32 v31, v31\n"
"v_mfma_f32_16x16x32_bf16 v[172:175], a[172:175], v[244:247], v[172:175]\n"
"s_add_u32 m0, s83, 32768\n"
"ds_read_b128 a[96:99], v1 offset:4096\n"
"v_cvt_pk_bf16_f32 v39, v30, v31\n"
"v_mfma_f32_16x16x32_bf16 v[172:175], a[204:207], v[248:251], v[172:175]\n"
"buffer_load_dwordx4 v240, s[44:47], s85 offen offset:0 nt lds\n"
"v_mfma_f32_16x16x32_bf16 v[24:27], a[112:115], a[64:67], v[56:59]\n"
"v_exp_f32 v16, v16\n"
"v_mfma_f32_16x16x32_bf16 v[24:27], a[116:119], a[68:71], v[24:27]\n"
"ds_read_b128 a[112:115], v1 offset:8192\n"
"v_mfma_f32_16x16x32_bf16 v[24:27], a[120:123], a[72:75], v[24:27]\n"
"v_exp_f32 v17, v17\n"
"v_mfma_f32_16x16x32_bf16 v[24:27], a[124:127], a[76:79], v[24:27]\n"
"ds_read_b128 a[116:119], v2 offset:8192\n"
"v_cvt_pk_bf16_f32 v244, v16, v17\n"
"v_mfma_f32_16x16x32_bf16 v[28:31], a[140:143], a[76:79], v[56:59]\n"
"v_exp_f32 v18, v18\n"
"v_mfma_f32_16x16x32_bf16 v[28:31], a[136:139], a[72:75], v[28:31]\n"
"ds_read_b128 a[120:123], v3 offset:8192\n"
"v_mfma_f32_16x16x32_bf16 v[28:31], a[132:135], a[68:71], v[28:31]\n"
"v_exp_f32 v19, v19\n"
"v_mfma_f32_16x16x32_bf16 v[28:31], a[128:131], a[64:67], v[28:31]\n"
"ds_read_b128 a[124:127], v4 offset:8192\n"
"v_cvt_pk_bf16_f32 v245, v18, v19\n"
"v_mfma_f32_16x16x32_bf16 v[72:75], a[208:211], v[32:35], v[72:75]\n"
"v_exp_f32 v20, v20\n"
"v_mfma_f32_16x16x32_bf16 v[176:179], a[144:147], v[32:35], v[176:179]\n"
"ds_read_b128 a[140:143], v4 offset:12288\n"
"v_mfma_f32_16x16x32_bf16 v[176:179], a[176:179], v[36:39], v[176:179]\n"
"v_exp_f32 v21, v21\n"
"v_mfma_f32_16x16x32_bf16 v[180:183], a[180:183], v[36:39], v[180:183]\n"
"ds_read_b128 a[136:139], v3 offset:12288\n"
"v_cvt_pk_bf16_f32 v246, v20, v21\n"
"v_mfma_f32_16x16x32_bf16 v[180:183], a[148:151], v[32:35], v[180:183]\n"
"v_exp_f32 v22, v22\n"
"v_mfma_f32_16x16x32_bf16 v[184:187], a[152:155], v[32:35], v[184:187]\n"
"ds_read_b128 a[132:135], v2 offset:12288\n"
"v_mfma_f32_16x16x32_bf16 v[184:187], a[184:187], v[36:39], v[184:187]\n"
"v_exp_f32 v23, v23\n"
"v_mfma_f32_16x16x32_bf16 v[188:191], a[188:191], v[36:39], v[188:191]\n"
"ds_read_b128 a[128:131], v1 offset:12288\n"
"v_cvt_pk_bf16_f32 v247, v22, v23\n"
"v_mfma_f32_16x16x32_bf16 v[188:191], a[156:159], v[32:35], v[188:191]\n"
"s_waitcnt lgkmcnt(8)\n"
"buffer_load_dwordx4 v241, s[44:47], s85 offen offset:1024 nt lds\n"
"v_mfma_f32_16x16x32_bf16 v[16:19], a[80:83], a[0:3], v[40:43]\n"
"v_exp_f32 v24, v24\n"
"v_mfma_f32_16x16x32_bf16 v[16:19], a[84:87], a[4:7], v[16:19]\n"
"v_mfma_f32_16x16x32_bf16 v[16:19], a[88:91], a[8:11], v[16:19]\n"
"v_exp_f32 v25, v25\n"
"v_mfma_f32_16x16x32_bf16 v[16:19], a[92:95], a[12:15], v[16:19]\n"
"v_cvt_pk_bf16_f32 v248, v24, v25\n"
"v_mfma_f32_16x16x32_bf16 v[20:23], a[108:111], a[12:15], v[40:43]\n"
"v_exp_f32 v26, v26\n"
"v_mfma_f32_16x16x32_bf16 v[20:23], a[104:107], a[8:11], v[20:23]\n"
"buffer_store_dwordx4 a[236:239], v9, s[56:59], s74 offen sc1\n"
"v_mfma_f32_16x16x32_bf16 v[20:23], a[100:103], a[4:7], v[20:23]\n"
"v_exp_f32 v27, v27\n"
"v_mfma_f32_16x16x32_bf16 v[20:23], a[96:99], a[0:3], v[20:23]\n"
"v_cvt_pk_bf16_f32 v249, v26, v27\n"
"v_mfma_f32_16x16x32_bf16 v[72:75], a[208:211], v[36:39], v[72:75]\n"
"v_exp_f32 v28, v28\n"
"v_mfma_f32_16x16x32_bf16 v[192:195], a[192:195], v[36:39], v[192:195]\n"
"v_mfma_f32_16x16x32_bf16 v[192:195], a[160:163], v[32:35], v[192:195]\n"
"v_exp_f32 v29, v29\n"
"v_mfma_f32_16x16x32_bf16 v[196:199], a[164:167], v[32:35], v[196:199]\n"
"v_cvt_pk_bf16_f32 v250, v28, v29\n"
"v_mfma_f32_16x16x32_bf16 v[196:199], a[196:199], v[36:39], v[196:199]\n"
"v_exp_f32 v30, v30\n"
"v_mfma_f32_16x16x32_bf16 v[200:203], a[200:203], v[36:39], v[200:203]\n"
"buffer_load_dwordx4 v242, s[44:47], s85 offen offset:2048 nt lds\n"
"v_mfma_f32_16x16x32_bf16 v[200:203], a[168:171], v[32:35], v[200:203]\n"
"v_exp_f32 v31, v31\n"
"v_mfma_f32_16x16x32_bf16 v[204:207], a[172:175], v[32:35], v[204:207]\n"
"v_cvt_pk_bf16_f32 v251, v30, v31\n"
"v_mfma_f32_16x16x32_bf16 v[204:207], a[204:207], v[36:39], v[204:207]\n"
"s_waitcnt lgkmcnt(0)\n"
"v_mfma_f32_16x16x32_bf16 v[24:27], a[112:115], a[0:3], v[40:43]\n"
"v_exp_f32 v16, v16\n"
"v_mfma_f32_16x16x32_bf16 v[24:27], a[116:119], a[4:7], v[24:27]\n"
"v_mfma_f32_16x16x32_bf16 v[24:27], a[120:123], a[8:11], v[24:27]\n"
"v_exp_f32 v17, v17\n"
"v_mfma_f32_16x16x32_bf16 v[24:27], a[124:127], a[12:15], v[24:27]\n"
"v_cvt_pk_bf16_f32 v32, v16, v17\n"
"v_mfma_f32_16x16x32_bf16 v[28:31], a[140:143], a[12:15], v[40:43]\n"
"v_exp_f32 v18, v18\n"
"v_mfma_f32_16x16x32_bf16 v[28:31], a[136:139], a[8:11], v[28:31]\n"
"buffer_load_dwordx4 v243, s[44:47], s85 offen offset:3072 nt lds\n"
"v_mfma_f32_16x16x32_bf16 v[28:31], a[132:135], a[4:7], v[28:31]\n"
"s_add_u32 s85, s85, 16384\n"
"v_exp_f32 v19, v19\n"
"v_mfma_f32_16x16x32_bf16 v[28:31], a[128:131], a[0:3], v[28:31]\n"
"v_cvt_pk_bf16_f32 v33, v18, v19\n"
"v_mfma_f32_16x16x32_bf16 v[76:79], a[208:211], v[244:247], v[76:79]\n"
"v_exp_f32 v20, v20\n"
"v_mfma_f32_16x16x32_bf16 v[208:211], a[144:147], v[244:247], v[208:211]\n"
"v_mfma_f32_16x16x32_bf16 v[208:211], a[176:179], v[248:251], v[208:211]\n"
"v_exp_f32 v21, v21\n"
"v_mfma_f32_16x16x32_bf16 v[212:215], a[180:183], v[248:251], v[212:215]\n"
"ds_read_b64_tr_b16 a[144:145], v5 offset:0\n"
"v_cvt_pk_bf16_f32 v34, v20, v21\n"
"v_mfma_f32_16x16x32_bf16 v[212:215], a[148:151], v[244:247], v[212:215]\n"
"v_exp_f32 v22, v22\n"
"v_mfma_f32_16x16x32_bf16 v[216:219], a[152:155], v[244:247], v[216:219]\n"
"ds_read_b64_tr_b16 a[146:147], v5 offset:4096\n"
"v_mfma_f32_16x16x32_bf16 v[216:219], a[184:187], v[248:251], v[216:219]\n"
"v_exp_f32 v23, v23\n"
"v_mfma_f32_16x16x32_bf16 v[220:223], a[188:191], v[248:251], v[220:223]\n"
"ds_read_b64_tr_b16 a[176:177], v5 offset:8192\n"
"v_cvt_pk_bf16_f32 v35, v22, v23\n"
"v_mfma_f32_16x16x32_bf16 v[220:223], a[156:159], v[244:247], v[220:223]\n"
"ds_read_b64_tr_b16 a[178:179], v5 offset:12288\n"
"v_mfma_f32_16x16x32_bf16 v[16:19], a[80:83], a[16:19], v[44:47]\n"
"v_exp_f32 v24, v24\n"
"v_mfma_f32_16x16x32_bf16 v[16:19], a[84:87], a[20:23], v[16:19]\n"
"ds_read_b64_tr_b16 a[148:149], v6 offset:0\n"
"v_mfma_f32_16x16x32_bf16 v[16:19], a[88:91], a[24:27], v[16:19]\n"
"ds_read_b64_tr_b16 a[150:151], v6 offset:4096\n"
"v_exp_f32 v25, v25\n"
"v_mfma_f32_16x16x32_bf16 v[16:19], a[92:95], a[28:31], v[16:19]\n"
"ds_read_b64_tr_b16 a[180:181], v6 offset:8192\n"
"v_cvt_pk_bf16_f32 v36, v24, v25\n"
"v_mfma_f32_16x16x32_bf16 v[20:23], a[108:111], a[28:31], v[44:47]\n"
"v_exp_f32 v26, v26\n"
"v_mfma_f32_16x16x32_bf16 v[20:23], a[104:107], a[24:27], v[20:23]\n"
"ds_read_b64_tr_b16 a[182:183], v6 offset:12288\n"
"v_mfma_f32_16x16x32_bf16 v[20:23], a[100:103], a[20:23], v[20:23]\n"
"v_exp_f32 v27, v27\n"
"v_mfma_f32_16x16x32_bf16 v[20:23], a[96:99], a[16:19], v[20:23]\n"
"ds_read_b64_tr_b16 a[152:153], v5 offset:512\n"
"v_cvt_pk_bf16_f32 v37, v26, v27\n"
"v_mfma_f32_16x16x32_bf16 v[76:79], a[208:211], v[248:251], v[76:79]\n"
"ds_read_b64_tr_b16 a[154:155], v5 offset:4608\n"
"v_exp_f32 v28, v28\n"
"v_mfma_f32_16x16x32_bf16 v[224:227], a[192:195], v[248:251], v[224:227]\n"
"ds_read_b64_tr_b16 a[184:185], v5 offset:8704\n"
"v_mfma_f32_16x16x32_bf16 v[224:227], a[160:163], v[244:247], v[224:227]\n"
"v_exp_f32 v29, v29\n"
"v_mfma_f32_16x16x32_bf16 v[228:231], a[164:167], v[244:247], v[228:231]\n"
"ds_read_b64_tr_b16 a[186:187], v5 offset:12800\n"
"v_cvt_pk_bf16_f32 v38, v28, v29\n"
"v_mfma_f32_16x16x32_bf16 v[228:231], a[196:199], v[248:251], v[228:231]\n"
"v_exp_f32 v30, v30\n"
"v_mfma_f32_16x16x32_bf16 v[232:235], a[200:203], v[248:251], v[232:235]\n"
"ds_read_b64_tr_b16 a[156:157], v6 offset:512\n"
"v_mfma_f32_16x16x32_bf16 v[232:235], a[168:171], v[244:247], v[232:235]\n"
"ds_read_b64_tr_b16 a[158:159], v6 offset:4608\n"
"v_exp_f32 v31, v31\n"
"v_mfma_f32_16x16x32_bf16 v[236:239], a[172:175], v[244:247], v[236:239]\n"
"ds_read_b64_tr_b16 a[188:189], v6 offset:8704\n"
"v_cvt_pk_bf16_f32 v39, v30, v31\n"
"v_mfma_f32_16x16x32_bf16 v[236:239], a[204:207], v[248:251], v[236:239]\n"
"ds_read_b64_tr_b16 a[190:191], v6 offset:12800\n"
"v_mfma_f32_16x16x32_bf16 v[24:27], a[112:115], a[16:19], v[44:47]\n"
"v_exp_f32 v16, v16\n"
"v_mfma_f32_16x16x32_bf16 v[24:27], a[116:119], a[20:23], v[24:27]\n"
"ds_read_b64_tr_b16 a[160:161], v5 offset:1024\n"
"v_mfma_f32_16x16x32_bf16 v[24:27], a[120:123], a[24:27], v[24:27]\n"
"ds_read_b64_tr_b16 a[162:163], v5 offset:5120\n"
"v_exp_f32 v17, v17\n"
"v_mfma_f32_16x16x32_bf16 v[24:27], a[124:127], a[28:31], v[24:27]\n"
"ds_read_b64_tr_b16 a[192:193], v5 offset:9216\n"
"v_cvt_pk_bf16_f32 v244, v16, v17\n"
"v_mfma_f32_16x16x32_bf16 v[28:31], a[140:143], a[28:31], v[44:47]\n"
"v_exp_f32 v18, v18\n"
"v_mfma_f32_16x16x32_bf16 v[28:31], a[136:139], a[24:27], v[28:31]\n"
"ds_read_b64_tr_b16 a[194:195], v5 offset:13312\n"
"v_mfma_f32_16x16x32_bf16 v[28:31], a[132:135], a[20:23], v[28:31]\n"
"v_exp_f32 v19, v19\n"
"v_mfma_f32_16x16x32_bf16 v[28:31], a[128:131], a[16:19], v[28:31]\n"
"ds_read_b64_tr_b16 a[164:165], v6 offset:1024\n"
"v_cvt_pk_bf16_f32 v245, v18, v19\n"
"v_mfma_f32_16x16x32_bf16 v[60:63], a[208:211], v[32:35], v[60:63]\n"
"s_waitcnt lgkmcnt(15)\n"
"ds_read_b64_tr_b16 a[166:167], v6 offset:5120\n"
"v_exp_f32 v20, v20\n"
"v_mfma_f32_16x16x32_bf16 v[80:83], a[144:147], v[32:35], v[80:83]\n"
"ds_read_b64_tr_b16 a[196:197], v6 offset:9216\n"
"v_mfma_f32_16x16x32_bf16 v[80:83], a[176:179], v[36:39], v[80:83]\n"
"s_waitcnt lgkmcnt(15)\n"
"v_exp_f32 v21, v21\n"
"v_mfma_f32_16x16x32_bf16 v[84:87], a[180:183], v[36:39], v[84:87]\n"
"ds_read_b64_tr_b16 a[198:199], v6 offset:13312\n"
"v_cvt_pk_bf16_f32 v246, v20, v21\n"
"v_mfma_f32_16x16x32_bf16 v[84:87], a[148:151], v[32:35], v[84:87]\n"
"s_waitcnt lgkmcnt(12)\n"
"v_exp_f32 v22, v22\n"
"v_mfma_f32_16x16x32_bf16 v[88:91], a[152:155], v[32:35], v[88:91]\n"
"ds_read_b64_tr_b16 a[168:169], v5 offset:1536\n"
"v_mfma_f32_16x16x32_bf16 v[88:91], a[184:187], v[36:39], v[88:91]\n"
"s_waitcnt lgkmcnt(9)\n"
"v_exp_f32 v23, v23\n"
"v_mfma_f32_16x16x32_bf16 v[92:95], a[188:191], v[36:39], v[92:95]\n"
"ds_read_b64_tr_b16 a[170:171], v5 offset:5632\n"
"v_cvt_pk_bf16_f32 v247, v22, v23\n"
"v_mfma_f32_16x16x32_bf16 v[92:95], a[156:159], v[32:35], v[92:95]\n"
"ds_read_b64_tr_b16 a[200:201], v5 offset:9728\n"
"v_mfma_f32_16x16x32_bf16 v[16:19], a[80:83], a[32:35], v[48:51]\n"
"v_exp_f32 v24, v24\n"
"v_mfma_f32_16x16x32_bf16 v[16:19], a[84:87], a[36:39], v[16:19]\n"
"ds_read_b64_tr_b16 a[202:203], v5 offset:13824\n"
"v_mfma_f32_16x16x32_bf16 v[16:19], a[88:91], a[40:43], v[16:19]\n"
"v_exp_f32 v25, v25\n"
"v_mfma_f32_16x16x32_bf16 v[16:19], a[92:95], a[44:47], v[16:19]\n"
"ds_read_b64_tr_b16 a[172:173], v6 offset:1536\n"
"v_cvt_pk_bf16_f32 v248, v24, v25\n"
"v_mfma_f32_16x16x32_bf16 v[20:23], a[108:111], a[44:47], v[48:51]\n"
"v_exp_f32 v26, v26\n"
"v_mfma_f32_16x16x32_bf16 v[20:23], a[104:107], a[40:43], v[20:23]\n"
"ds_read_b64_tr_b16 a[174:175], v6 offset:5632\n"
"v_mfma_f32_16x16x32_bf16 v[20:23], a[100:103], a[36:39], v[20:23]\n"
"v_exp_f32 v27, v27\n"
"v_mfma_f32_16x16x32_bf16 v[20:23], a[96:99], a[32:35], v[20:23]\n"
"ds_read_b64_tr_b16 a[204:205], v6 offset:9728\n"
"v_cvt_pk_bf16_f32 v249, v26, v27\n"
"v_mfma_f32_16x16x32_bf16 v[60:63], a[208:211], v[36:39], v[60:63]\n"
"s_waitcnt lgkmcnt(11)\n"
"v_exp_f32 v28, v28\n"
"v_mfma_f32_16x16x32_bf16 v[96:99], a[192:195], v[36:39], v[96:99]\n"
"ds_read_b64_tr_b16 a[206:207], v6 offset:13824\n"
"v_mfma_f32_16x16x32_bf16 v[96:99], a[160:163], v[32:35], v[96:99]\n"
"s_waitcnt lgkmcnt(8)\n"
"v_exp_f32 v29, v29\n"
"v_mfma_f32_16x16x32_bf16 v[100:103], a[164:167], v[32:35], v[100:103]\n"
"v_cvt_pk_bf16_f32 v250, v28, v29\n"
"v_mfma_f32_16x16x32_bf16 v[100:103], a[196:199], v[36:39], v[100:103]\n"
"s_waitcnt lgkmcnt(4)\n"
"v_exp_f32 v30, v30\n"
"v_mfma_f32_16x16x32_bf16 v[104:107], a[200:203], v[36:39], v[104:107]\n"
"buffer_store_dwordx4 a[240:243], v9, s[56:59], s75 offen sc1\n"
"v_mfma_f32_16x16x32_bf16 v[104:107], a[168:171], v[32:35], v[104:107]\n"
"s_waitcnt lgkmcnt(0)\n"
"v_exp_f32 v31, v31\n"
"v_mfma_f32_16x16x32_bf16 v[108:111], a[172:175], v[32:35], v[108:111]\n"
"v_cvt_pk_bf16_f32 v251, v30, v31\n"
"v_mfma_f32_16x16x32_bf16 v[108:111], a[204:207], v[36:39], v[108:111]\n"
"v_mfma_f32_16x16x32_bf16 v[24:27], a[112:115], a[32:35], v[48:51]\n"
"v_exp_f32 v16, v16\n"
"v_mfma_f32_16x16x32_bf16 v[24:27], a[116:119], a[36:39], v[24:27]\n"
"v_mfma_f32_16x16x32_bf16 v[24:27], a[120:123], a[40:43], v[24:27]\n"
"v_exp_f32 v17, v17\n"
"v_mfma_f32_16x16x32_bf16 v[24:27], a[124:127], a[44:47], v[24:27]\n"
"v_cvt_pk_bf16_f32 v32, v16, v17\n"
"v_mfma_f32_16x16x32_bf16 v[28:31], a[140:143], a[44:47], v[48:51]\n"
"v_exp_f32 v18, v18\n"
"v_mfma_f32_16x16x32_bf16 v[28:31], a[136:139], a[40:43], v[28:31]\n"
"v_mfma_f32_16x16x32_bf16 v[28:31], a[132:135], a[36:39], v[28:31]\n"
"v_exp_f32 v19, v19\n"
"v_mfma_f32_16x16x32_bf16 v[28:31], a[128:131], a[32:35], v[28:31]\n"
"v_cvt_pk_bf16_f32 v33, v18, v19\n"
"v_mfma_f32_16x16x32_bf16 v[64:67], a[208:211], v[244:247], v[64:67]\n"
"s_add_u32 m0, s84, 32768\n"
"v_exp_f32 v20, v20\n"
"v_mfma_f32_16x16x32_bf16 v[112:115], a[144:147], v[244:247], v[112:115]\n"
"buffer_load_dwordx4 v8, s[48:51], s86 offen offset:0 nt lds\n"
"v_mfma_f32_16x16x32_bf16 v[112:115], a[176:179], v[248:251], v[112:115]\n"
"v_exp_f32 v21, v21\n"
"v_mfma_f32_16x16x32_bf16 v[116:119], a[180:183], v[248:251], v[116:119]\n"
"v_cvt_pk_bf16_f32 v34, v20, v21\n"
"v_mfma_f32_16x16x32_bf16 v[116:119], a[148:151], v[244:247], v[116:119]\n"
"v_exp_f32 v22, v22\n"
"v_mfma_f32_16x16x32_bf16 v[120:123], a[152:155], v[244:247], v[120:123]\n"
"v_mfma_f32_16x16x32_bf16 v[120:123], a[184:187], v[248:251], v[120:123]\n"
"v_exp_f32 v23, v23\n"
"v_mfma_f32_16x16x32_bf16 v[124:127], a[188:191], v[248:251], v[124:127]\n"
"v_cvt_pk_bf16_f32 v35, v22, v23\n"
"v_mfma_f32_16x16x32_bf16 v[124:127], a[156:159], v[244:247], v[124:127]\n"
"buffer_load_dwordx4 v8, s[48:51], s87 offen offset:1024 nt lds\n"
"v_mfma_f32_16x16x32_bf16 v[16:19], a[80:83], a[48:51], v[52:55]\n"
"v_exp_f32 v24, v24\n"
"v_mfma_f32_16x16x32_bf16 v[16:19], a[84:87], a[52:55], v[16:19]\n"
"v_mfma_f32_16x16x32_bf16 v[16:19], a[88:91], a[56:59], v[16:19]\n"
"v_exp_f32 v25, v25\n"
"v_mfma_f32_16x16x32_bf16 v[16:19], a[92:95], a[60:63], v[16:19]\n"
"v_cvt_pk_bf16_f32 v36, v24, v25\n"
"v_mfma_f32_16x16x32_bf16 v[20:23], a[108:111], a[60:63], v[52:55]\n"
"v_exp_f32 v26, v26\n"
"v_mfma_f32_16x16x32_bf16 v[20:23], a[104:107], a[56:59], v[20:23]\n"
"buffer_store_dwordx4 a[244:247], v9, s[56:59], s76 offen sc1\n"
"v_mfma_f32_16x16x32_bf16 v[20:23], a[100:103], a[52:55], v[20:23]\n"
"v_exp_f32 v27, v27\n"
"v_mfma_f32_16x16x32_bf16 v[20:23], a[96:99], a[48:51], v[20:23]\n"
"v_cvt_pk_bf16_f32 v37, v26, v27\n"
"v_mfma_f32_16x16x32_bf16 v[64:67], a[208:211], v[248:251], v[64:67]\n"
"v_exp_f32 v28, v28\n"
"v_mfma_f32_16x16x32_bf16 v[128:131], a[192:195], v[248:251], v[128:131]\n"
"v_mfma_f32_16x16x32_bf16 v[128:131], a[160:163], v[244:247], v[128:131]\n"
"v_exp_f32 v29, v29\n"
"v_mfma_f32_16x16x32_bf16 v[132:135], a[164:167], v[244:247], v[132:135]\n"
"v_cvt_pk_bf16_f32 v38, v28, v29\n"
"v_mfma_f32_16x16x32_bf16 v[132:135], a[196:199], v[248:251], v[132:135]\n"
"v_exp_f32 v30, v30\n"
"v_mfma_f32_16x16x32_bf16 v[136:139], a[200:203], v[248:251], v[136:139]\n"
"v_mfma_f32_16x16x32_bf16 v[136:139], a[168:171], v[244:247], v[136:139]\n"
"v_exp_f32 v31, v31\n"
"v_mfma_f32_16x16x32_bf16 v[140:143], a[172:175], v[244:247], v[140:143]\n"
"v_cvt_pk_bf16_f32 v39, v30, v31\n"
"v_mfma_f32_16x16x32_bf16 v[140:143], a[204:207], v[248:251], v[140:143]\n"
"buffer_load_dwordx4 v8, s[48:51], s86 offen offset:2048 nt lds\n"
"v_mfma_f32_16x16x32_bf16 v[24:27], a[112:115], a[48:51], v[52:55]\n"
"v_exp_f32 v16, v16\n"
"v_mfma_f32_16x16x32_bf16 v[24:27], a[116:119], a[52:55], v[24:27]\n"
"v_mfma_f32_16x16x32_bf16 v[24:27], a[120:123], a[56:59], v[24:27]\n"
"v_exp_f32 v17, v17\n"
"v_mfma_f32_16x16x32_bf16 v[24:27], a[124:127], a[60:63], v[24:27]\n"
"v_cvt_pk_bf16_f32 v244, v16, v17\n"
"v_mfma_f32_16x16x32_bf16 v[28:31], a[140:143], a[60:63], v[52:55]\n"
"v_exp_f32 v18, v18\n"
"v_mfma_f32_16x16x32_bf16 v[28:31], a[136:139], a[56:59], v[28:31]\n"
"v_mfma_f32_16x16x32_bf16 v[28:31], a[132:135], a[52:55], v[28:31]\n"
"v_exp_f32 v19, v19\n"
"v_mfma_f32_16x16x32_bf16 v[28:31], a[128:131], a[48:51], v[28:31]\n"
"v_cvt_pk_bf16_f32 v245, v18, v19\n"
"v_mfma_f32_16x16x32_bf16 v[68:71], a[208:211], v[32:35], v[68:71]\n"
"v_exp_f32 v20, v20\n"
"v_mfma_f32_16x16x32_bf16 v[144:147], a[144:147], v[32:35], v[144:147]\n"
"buffer_load_dwordx4 v8, s[48:51], s87 offen offset:3072 nt lds\n"
"v_mfma_f32_16x16x32_bf16 v[144:147], a[176:179], v[36:39], v[144:147]\n"
"s_add_u32 s86, s86, 16384\n"
"s_add_u32 s87, s87, 16384\n"
"v_exp_f32 v21, v21\n"
"v_mfma_f32_16x16x32_bf16 v[148:151], a[180:183], v[36:39], v[148:151]\n"
"v_cvt_pk_bf16_f32 v246, v20, v21\n"
"v_mfma_f32_16x16x32_bf16 v[148:151], a[148:151], v[32:35], v[148:151]\n"
"v_exp_f32 v22, v22\n"
"v_mfma_f32_16x16x32_bf16 v[152:155], a[152:155], v[32:35], v[152:155]\n"
"v_mfma_f32_16x16x32_bf16 v[152:155], a[184:187], v[36:39], v[152:155]\n"
"v_exp_f32 v23, v23\n"
"v_mfma_f32_16x16x32_bf16 v[156:159], a[188:191], v[36:39], v[156:159]\n"
"v_cvt_pk_bf16_f32 v247, v22, v23\n"
"v_mfma_f32_16x16x32_bf16 v[156:159], a[156:159], v[32:35], v[156:159]\n"
"buffer_load_dword v10, off, s[4:7], s9\n"
"s_add_u32 s9, s9, 4096\n"
"s_mov_b32 s88, 2\n"
"Lloop_%=:\n"
"v_mfma_f32_16x16x32_bf16 v[16:19], a[80:83], a[64:67], v[56:59]\n"
"s_cmp_eq_u32 s88, 2\n"
"s_cbranch_scc1 Lh5_%=\n"
"s_waitcnt vmcnt(8)\n"
"Lh5_%=:\n"
"s_waitcnt vmcnt(11) lgkmcnt(0)\n"
"s_barrier\n"
"s_cmp_eq_u32 s88, 1\n"
"s_cbranch_scc0 Lwf_%=\n"
"v_cmp_ne_u32 vcc, v12, v14\n"
"s_lshl_b32 s96, s25, 2\n"
"s_add_u32 s96, s96, 20480\n"
"s_cbranch_vccnz Lhslow_%=\n"
"Lhok_%=:\n"
"v_mov_b32 v12, s96\n"
"v_mov_b32 v7, s30\n"
"v_mov_b32 v14, 1\n"
"buffer_store_dword v13, v12, s[64:67], 0 offen sc1\n"
"buffer_store_dword v14, v7, s[64:67], 0 offen sc1\n"
"v_lshlrev_b32 v14, 2, v0\n"
"v_add_u32 v14, s31, v14\n"
"Lwf_%=:\n"
"v_exp_f32 v24, v24\n"
"v_mfma_f32_16x16x32_bf16 v[16:19], a[84:87], a[68:71], v[16:19]\n"
"ds_read_b128 a[80:83], v1 offset:16384\n"
"v_mfma_f32_16x16x32_bf16 v[16:19], a[88:91], a[72:75], v[16:19]\n"
"v_exp_f32 v25, v25\n"
"v_mfma_f32_16x16x32_bf16 v[16:19], a[92:95], a[76:79], v[16:19]\n"
"ds_read_b128 a[84:87], v2 offset:16384\n"
"v_cvt_pk_bf16_f32 v248, v24, v25\n"
"v_mfma_f32_16x16x32_bf16 v[20:23], a[108:111], a[76:79], v[56:59]\n"
"v_exp_f32 v26, v26\n"
"v_mfma_f32_16x16x32_bf16 v[20:23], a[104:107], a[72:75], v[20:23]\n"
"ds_read_b128 a[88:91], v3 offset:16384\n"
"v_mfma_f32_16x16x32_bf16 v[20:23], a[100:103], a[68:71], v[20:23]\n"
"v_exp_f32 v27, v27\n"
"v_mfma_f32_16x16x32_bf16 v[20:23], a[96:99], a[64:67], v[20:23]\n"
"ds_read_b128 a[92:95], v4 offset:16384\n"
"v_cvt_pk_bf16_f32 v249, v26, v27\n"
"v_mfma_f32_16x16x32_bf16 v[68:71], a[208:211], v[36:39], v[68:71]\n"
"v_exp_f32 v28, v28\n"
"v_mfma_f32_16x16x32_bf16 v[160:163], a[192:195], v[36:39], v[160:163]\n"
"ds_read_b128 a[108:111], v4 offset:20480\n"
"v_mfma_f32_16x16x32_bf16 v[160:163], a[160:163], v[32:35], v[160:163]\n"
"v_exp_f32 v29, v29\n"
"v_mfma_f32_16x16x32_bf16 v[164:167], a[164:167], v[32:35], v[164:167]\n"
"ds_read_b128 a[104:107], v3 offset:20480\n"
"v_cvt_pk_bf16_f32 v250, v28, v29\n"
"v_mfma_f32_16x16x32_bf16 v[164:167], a[196:199], v[36:39], v[164:167]\n"
"v_exp_f32 v30, v30\n"
"v_mfma_f32_16x16x32_bf16 v[168:171], a[200:203], v[36:39], v[168:171]\n"
"ds_read_b128 a[100:103], v2 offset:20480\n"
"v_mfma_f32_16x16x32_bf16 v[168:171], a[168:171], v[32:35], v[168:171]\n"
"v_exp_f32 v31, v31\n"
"v_mfma_f32_16x16x32_bf16 v[172:175], a[172:175], v[32:35], v[172:175]\n"
"s_add_u32 m0, s83, 49152\n"
"ds_read_b128 a[96:99], v1 offset:20480\n"
"v_cvt_pk_bf16_f32 v251, v30, v31\n"
"v_mfma_f32_16x16x32_bf16 v[172:175], a[204:207], v[36:39], v[172:175]\n"
"buffer_load_dwordx4 v240, s[44:47], s85 offen offset:0 nt lds\n"
"v_mfma_f32_16x16x32_bf16 v[24:27], a[112:115], a[64:67], v[56:59]\n"
"v_exp_f32 v16, v16\n"
"v_mfma_f32_16x16x32_bf16 v[24:27], a[116:119], a[68:71], v[24:27]\n"
"ds_read_b128 a[112:115], v1 offset:24576\n"
"v_mfma_f32_16x16x32_bf16 v[24:27], a[120:123], a[72:75], v[24:27]\n"
"v_exp_f32 v17, v17\n"
"v_mfma_f32_16x16x32_bf16 v[24:27], a[124:127], a[76:79], v[24:27]\n"
"ds_read_b128 a[116:119], v2 offset:24576\n"
"v_cvt_pk_bf16_f32 v32, v16, v17\n"
"v_mfma_f32_16x16x32_bf16 v[28:31], a[140:143], a[76:79], v[56:59]\n"
"v_exp_f32 v18, v18\n"
"v_mfma_f32_16x16x32_bf16 v[28:31], a[136:139], a[72:75], v[28:31]\n"
"ds_read_b128 a[120:123], v3 offset:24576\n"
"v_mfma_f32_16x16x32_bf16 v[28:31], a[132:135], a[68:71], v[28:31]\n"
"v_exp_f32 v19, v19\n"
"v_mfma_f32_16x16x32_bf16 v[28:31], a[128:131], a[64:67], v[28:31]\n"
"ds_read_b128 a[124:127], v4 offset:24576\n"
"v_cvt_pk_bf16_f32 v33, v18, v19\n"
"v_mfma_f32_16x16x32_bf16 v[72:75], a[208:211], v[244:247], v[72:75]\n"
"v_exp_f32 v20, v20\n"
"v_mfma_f32_16x16x32_bf16 v[176:179], a[144:147], v[244:247], v[176:179]\n"
"ds_read_b128 a[140:143], v4 offset:28672\n"
"v_mfma_f32_16x16x32_bf16 v[176:179], a[176:179], v[248:251], v[176:179]\n"
"v_exp_f32 v21, v21\n"
"v_mfma_f32_16x16x32_bf16 v[180:183], a[180:183], v[248:251], v[180:183]\n"
"ds_read_b128 a[136:139], v3 offset:28672\n"
"v_cvt_pk_bf16_f32 v34, v20, v21\n"
"v_mfma_f32_16x16x32_bf16 v[180:183], a[148:151], v[244:247], v[180:183]\n"
"v_exp_f32 v22, v22\n"
"v_mfma_f32_16x16x32_bf16 v[184:187], a[152:155], v[244:247], v[184:187]\n"
"ds_read_b128 a[132:135], v2 offset:28672\n"
"v_mfma_f32_16x16x32_bf16 v[184:187], a[184:187], v[248:251], v[184:187]\n"
"v_exp_f32 v23, v23\n"
"v_mfma_f32_16x16x32_bf16 v[188:191], a[188:191], v[248:251], v[188:191]\n"
"ds_read_b128 a[128:131], v1 offset:28672\n"
"v_cvt_pk_bf16_f32 v35, v22, v23\n"
"v_mfma_f32_16x16x32_bf16 v[188:191], a[156:159], v[244:247], v[188:191]\n"
"s_waitcnt lgkmcnt(8)\n"
"buffer_load_dwordx4 v241, s[44:47], s85 offen offset:1024 nt lds\n"
"v_mfma_f32_16x16x32_bf16 v[16:19], a[80:83], a[0:3], v[40:43]\n"
"v_exp_f32 v24, v24\n"
"v_mfma_f32_16x16x32_bf16 v[16:19], a[84:87], a[4:7], v[16:19]\n"
"v_mfma_f32_16x16x32_bf16 v[16:19], a[88:91], a[8:11], v[16:19]\n"
"v_exp_f32 v25, v25\n"
"v_mfma_f32_16x16x32_bf16 v[16:19], a[92:95], a[12:15], v[16:19]\n"
"v_cvt_pk_bf16_f32 v36, v24, v25\n"
"v_mfma_f32_16x16x32_bf16 v[20:23], a[108:111], a[12:15], v[40:43]\n"
"v_exp_f32 v26, v26\n"
"v_mfma_f32_16x16x32_bf16 v[20:23], a[104:107], a[8:11], v[20:23]\n"
"buffer_load_dwordx4 v242, s[44:47], s85 offen offset:2048 nt lds\n"
"v_mfma_f32_16x16x32_bf16 v[20:23], a[100:103], a[4:7], v[20:23]\n"
"v_exp_f32 v27, v27\n"
"v_mfma_f32_16x16x32_bf16 v[20:23], a[96:99], a[0:3], v[20:23]\n"
"v_cvt_pk_bf16_f32 v37, v26, v27\n"
"v_mfma_f32_16x16x32_bf16 v[72:75], a[208:211], v[248:251], v[72:75]\n"
"v_exp_f32 v28, v28\n"
"v_mfma_f32_16x16x32_bf16 v[192:195], a[192:195], v[248:251], v[192:195]\n"
"v_mfma_f32_16x16x32_bf16 v[192:195], a[160:163], v[244:247], v[192:195]\n"
"v_exp_f32 v29, v29\n"
"v_mfma_f32_16x16x32_bf16 v[196:199], a[164:167], v[244:247], v[196:199]\n"
"v_cvt_pk_bf16_f32 v38, v28, v29\n"
"v_mfma_f32_16x16x32_bf16 v[196:199], a[196:199], v[248:251], v[196:199]\n"
"v_exp_f32 v30, v30\n"
"v_mfma_f32_16x16x32_bf16 v[200:203], a[200:203], v[248:251], v[200:203]\n"
"buffer_load_dwordx4 v243, s[44:47], s85 offen offset:3072 nt lds\n"
"v_mfma_f32_16x16x32_bf16 v[200:203], a[168:171], v[244:247], v[200:203]\n"
"s_add_u32 s85, s85, 16384\n"
"v_exp_f32 v31, v31\n"
"v_mfma_f32_16x16x32_bf16 v[204:207], a[172:175], v[244:247], v[204:207]\n"
"v_cvt_pk_bf16_f32 v39, v30, v31\n"
"v_mfma_f32_16x16x32_bf16 v[204:207], a[204:207], v[248:251], v[204:207]\n"
"s_waitcnt lgkmcnt(0)\n"
"v_mfma_f32_16x16x32_bf16 v[24:27], a[112:115], a[0:3], v[40:43]\n"
"v_exp_f32 v16, v16\n"
"v_mfma_f32_16x16x32_bf16 v[24:27], a[116:119], a[4:7], v[24:27]\n"
"v_mfma_f32_16x16x32_bf16 v[24:27], a[120:123], a[8:11], v[24:27]\n"
"v_exp_f32 v17, v17\n"
"v_mfma_f32_16x16x32_bf16 v[24:27], a[124:127], a[12:15], v[24:27]\n"
"v_cvt_pk_bf16_f32 v244, v16, v17\n"
"v_mfma_f32_16x16x32_bf16 v[28:31], a[140:143], a[12:15], v[40:43]\n"
"v_exp_f32 v18, v18\n"
"v_mfma_f32_16x16x32_bf16 v[28:31], a[136:139], a[8:11], v[28:31]\n"
"v_mfma_f32_16x16x32_bf16 v[28:31], a[132:135], a[4:7], v[28:31]\n"
"v_exp_f32 v19, v19\n"
"v_mfma_f32_16x16x32_bf16 v[28:31], a[128:131], a[0:3], v[28:31]\n"
"v_cvt_pk_bf16_f32 v245, v18, v19\n"
"v_mfma_f32_16x16x32_bf16 v[76:79], a[208:211], v[32:35], v[76:79]\n"
"v_exp_f32 v20, v20\n"
"v_mfma_f32_16x16x32_bf16 v[208:211], a[144:147], v[32:35], v[208:211]\n"
"v_mfma_f32_16x16x32_bf16 v[208:211], a[176:179], v[36:39], v[208:211]\n"
"v_exp_f32 v21, v21\n"
"v_mfma_f32_16x16x32_bf16 v[212:215], a[180:183], v[36:39], v[212:215]\n"
"ds_read_b64_tr_b16 a[144:145], v5 offset:16384\n"
"v_cvt_pk_bf16_f32 v246, v20, v21\n"
"v_mfma_f32_16x16x32_bf16 v[212:215], a[148:151], v[32:35], v[212:215]\n"
"v_exp_f32 v22, v22\n"
"v_mfma_f32_16x16x32_bf16 v[216:219], a[152:155], v[32:35], v[216:219]\n"
"ds_read_b64_tr_b16 a[146:147], v5 offset:20480\n"
"v_mfma_f32_16x16x32_bf16 v[216:219], a[184:187], v[36:39], v[216:219]\n"
"v_exp_f32 v23, v23\n"
"v_mfma_f32_16x16x32_bf16 v[220:223], a[188:191], v[36:39], v[220:223]\n"
"ds_read_b64_tr_b16 a[176:177], v5 offset:24576\n"
"v_cvt_pk_bf16_f32 v247, v22, v23\n"
"v_mfma_f32_16x16x32_bf16 v[220:223], a[156:159], v[32:35], v[220:223]\n"
"ds_read_b64_tr_b16 a[178:179], v5 offset:28672\n"
"v_mfma_f32_16x16x32_bf16 v[16:19], a[80:83], a[16:19], v[44:47]\n"
"v_exp_f32 v24, v24\n"
"v_mfma_f32_16x16x32_bf16 v[16:19], a[84:87], a[20:23], v[16:19]\n"
"ds_read_b64_tr_b16 a[148:149], v6 offset:16384\n"
"v_mfma_f32_16x16x32_bf16 v[16:19], a[88:91], a[24:27], v[16:19]\n"
"ds_read_b64_tr_b16 a[150:151], v6 offset:20480\n"
"v_exp_f32 v25, v25\n"
"v_mfma_f32_16x16x32_bf16 v[16:19], a[92:95], a[28:31], v[16:19]\n"
"ds_read_b64_tr_b16 a[180:181], v6 offset:24576\n"
"v_cvt_pk_bf16_f32 v248, v24, v25\n"
"v_mfma_f32_16x16x32_bf16 v[20:23], a[108:111], a[28:31], v[44:47]\n"
"v_exp_f32 v26, v26\n"
"v_mfma_f32_16x16x32_bf16 v[20:23], a[104:107], a[24:27], v[20:23]\n"
"ds_read_b64_tr_b16 a[182:183], v6 offset:28672\n"
"v_mfma_f32_16x16x32_bf16 v[20:23], a[100:103], a[20:23], v[20:23]\n"
"v_exp_f32 v27, v27\n"
"v_mfma_f32_16x16x32_bf16 v[20:23], a[96:99], a[16:19], v[20:23]\n"
"ds_read_b64_tr_b16 a[152:153], v5 offset:16896\n"
"v_cvt_pk_bf16_f32 v249, v26, v27\n"
"v_mfma_f32_16x16x32_bf16 v[76:79], a[208:211], v[36:39], v[76:79]\n"
"ds_read_b64_tr_b16 a[154:155], v5 offset:20992\n"
"v_exp_f32 v28, v28\n"
"v_mfma_f32_16x16x32_bf16 v[224:227], a[192:195], v[36:39], v[224:227]\n"
"ds_read_b64_tr_b16 a[184:185], v5 offset:25088\n"
"v_mfma_f32_16x16x32_bf16 v[224:227], a[160:163], v[32:35], v[224:227]\n"
"v_exp_f32 v29, v29\n"
"v_mfma_f32_16x16x32_bf16 v[228:231], a[164:167], v[32:35], v[228:231]\n"
"ds_read_b64_tr_b16 a[186:187], v5 offset:29184\n"
"v_cvt_pk_bf16_f32 v250, v28, v29\n"
"v_mfma_f32_16x16x32_bf16 v[228:231], a[196:199], v[36:39], v[228:231]\n"
"v_exp_f32 v30, v30\n"
"v_mfma_f32_16x16x32_bf16 v[232:235], a[200:203], v[36:39], v[232:235]\n"
"ds_read_b64_tr_b16 a[156:157], v6 offset:16896\n"
"v_mfma_f32_16x16x32_bf16 v[232:235], a[168:171], v[32:35], v[232:235]\n"
"ds_read_b64_tr_b16 a[158:159], v6 offset:20992\n"
"v_exp_f32 v31, v31\n"
"v_mfma_f32_16x16x32_bf16 v[236:239], a[172:175], v[32:35], v[236:239]\n"
"ds_read_b64_tr_b16 a[188:189], v6 offset:25088\n"
"v_cvt_pk_bf16_f32 v251, v30, v31\n"
"v_mfma_f32_16x16x32_bf16 v[236:239], a[204:207], v[36:39], v[236:239]\n"
"ds_read_b64_tr_b16 a[190:191], v6 offset:29184\n"
"v_mfma_f32_16x16x32_bf16 v[24:27], a[112:115], a[16:19], v[44:47]\n"
"v_exp_f32 v16, v16\n"
"v_mfma_f32_16x16x32_bf16 v[24:27], a[116:119], a[20:23], v[24:27]\n"
"ds_read_b64_tr_b16 a[160:161], v5 offset:17408\n"
"v_mfma_f32_16x16x32_bf16 v[24:27], a[120:123], a[24:27], v[24:27]\n"
"ds_read_b64_tr_b16 a[162:163], v5 offset:21504\n"
"v_exp_f32 v17, v17\n"
"v_mfma_f32_16x16x32_bf16 v[24:27], a[124:127], a[28:31], v[24:27]\n"
"ds_read_b64_tr_b16 a[192:193], v5 offset:25600\n"
"v_cvt_pk_bf16_f32 v32, v16, v17\n"
"v_mfma_f32_16x16x32_bf16 v[28:31], a[140:143], a[28:31], v[44:47]\n"
"v_exp_f32 v18, v18\n"
"v_mfma_f32_16x16x32_bf16 v[28:31], a[136:139], a[24:27], v[28:31]\n"
"ds_read_b64_tr_b16 a[194:195], v5 offset:29696\n"
"v_mfma_f32_16x16x32_bf16 v[28:31], a[132:135], a[20:23], v[28:31]\n"
"v_exp_f32 v19, v19\n"
"v_mfma_f32_16x16x32_bf16 v[28:31], a[128:131], a[16:19], v[28:31]\n"
"ds_read_b64_tr_b16 a[164:165], v6 offset:17408\n"
"v_cvt_pk_bf16_f32 v33, v18, v19\n"
"v_mfma_f32_16x16x32_bf16 v[60:63], a[208:211], v[244:247], v[60:63]\n"
"s_waitcnt lgkmcnt(15)\n"
"ds_read_b64_tr_b16 a[166:167], v6 offset:21504\n"
"v_exp_f32 v20, v20\n"
"v_mfma_f32_16x16x32_bf16 v[80:83], a[144:147], v[244:247], v[80:83]\n"
"ds_read_b64_tr_b16 a[196:197], v6 offset:25600\n"
"v_mfma_f32_16x16x32_bf16 v[80:83], a[176:179], v[248:251], v[80:83]\n"
"s_waitcnt lgkmcnt(15)\n"
"v_exp_f32 v21, v21\n"
"v_mfma_f32_16x16x32_bf16 v[84:87], a[180:183], v[248:251], v[84:87]\n"
"ds_read_b64_tr_b16 a[198:199], v6 offset:29696\n"
"v_cvt_pk_bf16_f32 v34, v20, v21\n"
"v_mfma_f32_16x16x32_bf16 v[84:87], a[148:151], v[244:247], v[84:87]\n"
"s_waitcnt lgkmcnt(12)\n"
"v_exp_f32 v22, v22\n"
"v_mfma_f32_16x16x32_bf16 v[88:91], a[152:155], v[244:247], v[88:91]\n"
"ds_read_b64_tr_b16 a[168:169], v5 offset:17920\n"
"v_mfma_f32_16x16x32_bf16 v[88:91], a[184:187], v[248:251], v[88:91]\n"
"s_waitcnt lgkmcnt(9)\n"
"v_exp_f32 v23, v23\n"
"v_mfma_f32_16x16x32_bf16 v[92:95], a[188:191], v[248:251], v[92:95]\n"
"ds_read_b64_tr_b16 a[170:171], v5 offset:22016\n"
"v_cvt_pk_bf16_f32 v35, v22, v23\n"
"v_mfma_f32_16x16x32_bf16 v[92:95], a[156:159], v[244:247], v[92:95]\n"
"ds_read_b64_tr_b16 a[200:201], v5 offset:26112\n"
"v_mfma_f32_16x16x32_bf16 v[16:19], a[80:83], a[32:35], v[48:51]\n"
"v_exp_f32 v24, v24\n"
"v_mfma_f32_16x16x32_bf16 v[16:19], a[84:87], a[36:39], v[16:19]\n"
"ds_read_b64_tr_b16 a[202:203], v5 offset:30208\n"
"v_mfma_f32_16x16x32_bf16 v[16:19], a[88:91], a[40:43], v[16:19]\n"
"v_exp_f32 v25, v25\n"
"v_mfma_f32_16x16x32_bf16 v[16:19], a[92:95], a[44:47], v[16:19]\n"
"ds_read_b64_tr_b16 a[172:173], v6 offset:17920\n"
"v_cvt_pk_bf16_f32 v36, v24, v25\n"
"v_mfma_f32_16x16x32_bf16 v[20:23], a[108:111], a[44:47], v[48:51]\n"
"v_exp_f32 v26, v26\n"
"v_mfma_f32_16x16x32_bf16 v[20:23], a[104:107], a[40:43], v[20:23]\n"
"ds_read_b64_tr_b16 a[174:175], v6 offset:22016\n"
"v_mfma_f32_16x16x32_bf16 v[20:23], a[100:103], a[36:39], v[20:23]\n"
"v_exp_f32 v27, v27\n"
"v_mfma_f32_16x16x32_bf16 v[20:23], a[96:99], a[32:35], v[20:23]\n"
"ds_read_b64_tr_b16 a[204:205], v6 offset:26112\n"
"v_cvt_pk_bf16_f32 v37, v26, v27\n"
"v_mfma_f32_16x16x32_bf16 v[60:63], a[208:211], v[248:251], v[60:63]\n"
"s_waitcnt lgkmcnt(11)\n"
"v_exp_f32 v28, v28\n"
"v_mfma_f32_16x16x32_bf16 v[96:99], a[192:195], v[248:251], v[96:99]\n"
"ds_read_b64_tr_b16 a[206:207], v6 offset:30208\n"
"v_mfma_f32_16x16x32_bf16 v[96:99], a[160:163], v[244:247], v[96:99]\n"
"s_waitcnt lgkmcnt(8)\n"
"v_exp_f32 v29, v29\n"
"v_mfma_f32_16x16x32_bf16 v[100:103], a[164:167], v[244:247], v[100:103]\n"
"v_cvt_pk_bf16_f32 v38, v28, v29\n"
"v_mfma_f32_16x16x32_bf16 v[100:103], a[196:199], v[248:251], v[100:103]\n"
"s_waitcnt lgkmcnt(4)\n"
"s_add_u32 m0, s84, 49152\n"
"v_exp_f32 v30, v30\n"
"v_mfma_f32_16x16x32_bf16 v[104:107], a[200:203], v[248:251], v[104:107]\n"
"buffer_load_dwordx4 v8, s[48:51], s86 offen offset:0 nt lds\n"
"v_mfma_f32_16x16x32_bf16 v[104:107], a[168:171], v[244:247], v[104:107]\n"
"s_waitcnt lgkmcnt(0)\n"
"v_exp_f32 v31, v31\n"
"v_mfma_f32_16x16x32_bf16 v[108:111], a[172:175], v[244:247], v[108:111]\n"
"v_cvt_pk_bf16_f32 v39, v30, v31\n"
"v_mfma_f32_16x16x32_bf16 v[108:111], a[204:207], v[248:251], v[108:111]\n"
"v_mfma_f32_16x16x32_bf16 v[24:27], a[112:115], a[32:35], v[48:51]\n"
"v_exp_f32 v16, v16\n"
"v_mfma_f32_16x16x32_bf16 v[24:27], a[116:119], a[36:39], v[24:27]\n"
"v_mfma_f32_16x16x32_bf16 v[24:27], a[120:123], a[40:43], v[24:27]\n"
"v_exp_f32 v17, v17\n"
"v_mfma_f32_16x16x32_bf16 v[24:27], a[124:127], a[44:47], v[24:27]\n"
"v_cvt_pk_bf16_f32 v244, v16, v17\n"
"v_mfma_f32_16x16x32_bf16 v[28:31], a[140:143], a[44:47], v[48:51]\n"
"v_exp_f32 v18, v18\n"
"v_mfma_f32_16x16x32_bf16 v[28:31], a[136:139], a[40:43], v[28:31]\n"
"v_mfma_f32_16x16x32_bf16 v[28:31], a[132:135], a[36:39], v[28:31]\n"
"v_exp_f32 v19, v19\n"
"v_mfma_f32_16x16x32_bf16 v[28:31], a[128:131], a[32:35], v[28:31]\n"
"v_cvt_pk_bf16_f32 v245, v18, v19\n"
"v_mfma_f32_16x16x32_bf16 v[64:67], a[208:211], v[32:35], v[64:67]\n"
"v_exp_f32 v20, v20\n"
"v_mfma_f32_16x16x32_bf16 v[112:115], a[144:147], v[32:35], v[112:115]\n"
"buffer_load_dwordx4 v8, s[48:51], s87 offen offset:1024 nt lds\n"
"v_mfma_f32_16x16x32_bf16 v[112:115], a[176:179], v[36:39], v[112:115]\n"
"v_exp_f32 v21, v21\n"
"v_mfma_f32_16x16x32_bf16 v[116:119], a[180:183], v[36:39], v[116:119]\n"
"v_cvt_pk_bf16_f32 v246, v20, v21\n"
"v_mfma_f32_16x16x32_bf16 v[116:119], a[148:151], v[32:35], v[116:119]\n"
"v_exp_f32 v22, v22\n"
"v_mfma_f32_16x16x32_bf16 v[120:123], a[152:155], v[32:35], v[120:123]\n"
"v_mfma_f32_16x16x32_bf16 v[120:123], a[184:187], v[36:39], v[120:123]\n"
"v_exp_f32 v23, v23\n"
"v_mfma_f32_16x16x32_bf16 v[124:127], a[188:191], v[36:39], v[124:127]\n"
"v_cvt_pk_bf16_f32 v247, v22, v23\n"
"v_mfma_f32_16x16x32_bf16 v[124:127], a[156:159], v[32:35], v[124:127]\n"
"v_mfma_f32_16x16x32_bf16 v[16:19], a[80:83], a[48:51], v[52:55]\n"
"v_exp_f32 v24, v24\n"
"v_mfma_f32_16x16x32_bf16 v[16:19], a[84:87], a[52:55], v[16:19]\n"
"v_mfma_f32_16x16x32_bf16 v[16:19], a[88:91], a[56:59], v[16:19]\n"
"v_exp_f32 v25, v25\n"
"v_mfma_f32_16x16x32_bf16 v[16:19], a[92:95], a[60:63], v[16:19]\n"
"v_cvt_pk_bf16_f32 v248, v24, v25\n"
"v_mfma_f32_16x16x32_bf16 v[20:23], a[108:111], a[60:63], v[52:55]\n"
"v_exp_f32 v26, v26\n"
"v_mfma_f32_16x16x32_bf16 v[20:23], a[104:107], a[56:59], v[20:23]\n"
"buffer_load_dwordx4 v8, s[48:51], s86 offen offset:2048 nt lds\n"
"v_mfma_f32_16x16x32_bf16 v[20:23], a[100:103], a[52:55], v[20:23]\n"
"v_exp_f32 v27, v27\n"
"v_mfma_f32_16x16x32_bf16 v[20:23], a[96:99], a[48:51], v[20:23]\n"
"v_cvt_pk_bf16_f32 v249, v26, v27\n"
"v_mfma_f32_16x16x32_bf16 v[64:67], a[208:211], v[36:39], v[64:67]\n"
"v_exp_f32 v28, v28\n"
"v_mfma_f32_16x16x32_bf16 v[128:131], a[192:195], v[36:39], v[128:131]\n"
"v_mfma_f32_16x16x32_bf16 v[128:131], a[160:163], v[32:35], v[128:131]\n"
"v_exp_f32 v29, v29\n"
"v_mfma_f32_16x16x32_bf16 v[132:135], a[164:167], v[32:35], v[132:135]\n"
"v_cvt_pk_bf16_f32 v250, v28, v29\n"
"v_mfma_f32_16x16x32_bf16 v[132:135], a[196:199], v[36:39], v[132:135]\n"
"v_exp_f32 v30, v30\n"
"v_mfma_f32_16x16x32_bf16 v[136:139], a[200:203], v[36:39], v[136:139]\n"
"v_mfma_f32_16x16x32_bf16 v[136:139], a[168:171], v[32:35], v[136:139]\n"
"v_exp_f32 v31, v31\n"
"v_mfma_f32_16x16x32_bf16 v[140:143], a[172:175], v[32:35], v[140:143]\n"
"v_cvt_pk_bf16_f32 v251, v30, v31\n"
"v_mfma_f32_16x16x32_bf16 v[140:143], a[204:207], v[36:39], v[140:143]\n"
"buffer_load_dwordx4 v8, s[48:51], s87 offen offset:3072 nt lds\n"
"v_mfma_f32_16x16x32_bf16 v[24:27], a[112:115], a[48:51], v[52:55]\n"
"s_add_u32 s86, s86, 16384\n"
"s_add_u32 s87, s87, 16384\n"
"v_exp_f32 v16, v16\n"
"v_mfma_f32_16x16x32_bf16 v[24:27], a[116:119], a[52:55], v[24:27]\n"
"v_mfma_f32_16x16x32_bf16 v[24:27], a[120:123], a[56:59], v[24:27]\n"
"v_exp_f32 v17, v17\n"
"v_mfma_f32_16x16x32_bf16 v[24:27], a[124:127], a[60:63], v[24:27]\n"
"v_cvt_pk_bf16_f32 v32, v16, v17\n"
"v_mfma_f32_16x16x32_bf16 v[28:31], a[140:143], a[60:63], v[52:55]\n"
"v_exp_f32 v18, v18\n"
"v_mfma_f32_16x16x32_bf16 v[28:31], a[136:139], a[56:59], v[28:31]\n"
"v_mfma_f32_16x16x32_bf16 v[28:31], a[132:135], a[52:55], v[28:31]\n"
"v_exp_f32 v19, v19\n"
"v_mfma_f32_16x16x32_bf16 v[28:31], a[128:131], a[48:51], v[28:31]\n"
"v_cvt_pk_bf16_f32 v33, v18, v19\n"
"v_mfma_f32_16x16x32_bf16 v[68:71], a[208:211], v[244:247], v[68:71]\n"
"v_exp_f32 v20, v20\n"
"v_mfma_f32_16x16x32_bf16 v[144:147], a[144:147], v[244:247], v[144:147]\n"
"buffer_load_dword v10, off, s[4:7], s9\n"
"v_mfma_f32_16x16x32_bf16 v[144:147], a[176:179], v[248:251], v[144:147]\n"
"s_add_u32 s9, s9, 4096\n"
"v_exp_f32 v21, v21\n"
"v_mfma_f32_16x16x32_bf16 v[148:151], a[180:183], v[248:251], v[148:151]\n"
"v_cvt_pk_bf16_f32 v34, v20, v21\n"
"v_mfma_f32_16x16x32_bf16 v[148:151], a[148:151], v[244:247], v[148:151]\n"
"v_exp_f32 v22, v22\n"
"v_mfma_f32_16x16x32_bf16 v[152:155], a[152:155], v[244:247], v[152:155]\n"
"v_mfma_f32_16x16x32_bf16 v[152:155], a[184:187], v[248:251], v[152:155]\n"
"v_exp_f32 v23, v23\n"
"v_mfma_f32_16x16x32_bf16 v[156:159], a[188:191], v[248:251], v[156:159]\n"
"v_cvt_pk_bf16_f32 v35, v22, v23\n"
"v_mfma_f32_16x16x32_bf16 v[156:159], a[156:159], v[244:247], v[156:159]\n"
"v_mfma_f32_16x16x32_bf16 v[16:19], a[80:83], a[64:67], v[56:59]\n"
"s_waitcnt vmcnt(8) lgkmcnt(0)\n"
"s_barrier\n"
"v_exp_f32 v24, v24\n"
"v_mfma_f32_16x16x32_bf16 v[16:19], a[84:87], a[68:71], v[16:19]\n"
"ds_read_b128 a[80:83], v1 offset:32768\n"
"v_mfma_f32_16x16x32_bf16 v[16:19], a[88:91], a[72:75], v[16:19]\n"
"v_exp_f32 v25, v25\n"
"v_mfma_f32_16x16x32_bf16 v[16:19], a[92:95], a[76:79], v[16:19]\n"
"ds_read_b128 a[84:87], v2 offset:32768\n"
"v_cvt_pk_bf16_f32 v36, v24, v25\n"
"v_mfma_f32_16x16x32_bf16 v[20:23], a[108:111], a[76:79], v[56:59]\n"
"v_exp_f32 v26, v26\n"
"v_mfma_f32_16x16x32_bf16 v[20:23], a[104:107], a[72:75], v[20:23]\n"
"ds_read_b128 a[88:91], v3 offset:32768\n"
"v_mfma_f32_16x16x32_bf16 v[20:23], a[100:103], a[68:71], v[20:23]\n"
"v_exp_f32 v27, v27\n"
"v_mfma_f32_16x16x32_bf16 v[20:23], a[96:99], a[64:67], v[20:23]\n"
"ds_read_b128 a[92:95], v4 offset:32768\n"
"v_cvt_pk_bf16_f32 v37, v26, v27\n"
"v_mfma_f32_16x16x32_bf16 v[68:71], a[208:211], v[248:251], v[68:71]\n"
"v_exp_f32 v28, v28\n"
"v_mfma_f32_16x16x32_bf16 v[160:163], a[192:195], v[248:251], v[160:163]\n"
"ds_read_b128 a[108:111], v4 offset:36864\n"
"v_mfma_f32_16x16x32_bf16 v[160:163], a[160:163], v[244:247], v[160:163]\n"
"v_exp_f32 v29, v29\n"
"v_mfma_f32_16x16x32_bf16 v[164:167], a[164:167], v[244:247], v[164:167]\n"
"ds_read_b128 a[104:107], v3 offset:36864\n"
"v_cvt_pk_bf16_f32 v38, v28, v29\n"
"v_mfma_f32_16x16x32_bf16 v[164:167], a[196:199], v[248:251], v[164:167]\n"
"v_exp_f32 v30, v30\n"
"v_mfma_f32_16x16x32_bf16 v[168:171], a[200:203], v[248:251], v[168:171]\n"
"ds_read_b128 a[100:103], v2 offset:36864\n"
"v_mfma_f32_16x16x32_bf16 v[168:171], a[168:171], v[244:247], v[168:171]\n"
"v_exp_f32 v31, v31\n"
"v_mfma_f32_16x16x32_bf16 v[172:175], a[172:175], v[244:247], v[172:175]\n"
"s_add_u32 m0, s83, 0\n"
"ds_read_b128 a[96:99], v1 offset:36864\n"
"v_cvt_pk_bf16_f32 v39, v30, v31\n"
"v_mfma_f32_16x16x32_bf16 v[172:175], a[204:207], v[248:251], v[172:175]\n"
"buffer_load_dwordx4 v240, s[44:47], s85 offen offset:0 nt lds\n"
"v_mfma_f32_16x16x32_bf16 v[24:27], a[112:115], a[64:67], v[56:59]\n"
"v_exp_f32 v16, v16\n"
"v_mfma_f32_16x16x32_bf16 v[24:27], a[116:119], a[68:71], v[24:27]\n"
"ds_read_b128 a[112:115], v1 offset:40960\n"
"v_mfma_f32_16x16x32_bf16 v[24:27], a[120:123], a[72:75], v[24:27]\n"
"v_exp_f32 v17, v17\n"
"v_mfma_f32_16x16x32_bf16 v[24:27], a[124:127], a[76:79], v[24:27]\n"
"ds_read_b128 a[116:119], v2 offset:40960\n"
"v_cvt_pk_bf16_f32 v244, v16, v17\n"
"v_mfma_f32_16x16x32_bf16 v[28:31], a[140:143], a[76:79], v[56:59]\n"
"v_exp_f32 v18, v18\n"
"v_mfma_f32_16x16x32_bf16 v[28:31], a[136:139], a[72:75], v[28:31]\n"
"ds_read_b128 a[120:123], v3 offset:40960\n"
"v_mfma_f32_16x16x32_bf16 v[28:31], a[132:135], a[68:71], v[28:31]\n"
"v_exp_f32 v19, v19\n"
"v_mfma_f32_16x16x32_bf16 v[28:31], a[128:131], a[64:67], v[28:31]\n"
"ds_read_b128 a[124:127], v4 offset:40960\n"
"v_cvt_pk_bf16_f32 v245, v18, v19\n"
"v_mfma_f32_16x16x32_bf16 v[72:75], a[208:211], v[32:35], v[72:75]\n"
"v_exp_f32 v20, v20\n"
"v_mfma_f32_16x16x32_bf16 v[176:179], a[144:147], v[32:35], v[176:179]\n"
"ds_read_b128 a[140:143], v4 offset:45056\n"
"v_mfma_f32_16x16x32_bf16 v[176:179], a[176:179], v[36:39], v[176:179]\n"
"v_exp_f32 v21, v21\n"
"v_mfma_f32_16x16x32_bf16 v[180:183], a[180:183], v[36:39], v[180:183]\n"
"ds_read_b128 a[136:139], v3 offset:45056\n"
"v_cvt_pk_bf16_f32 v246, v20, v21\n"
"v_mfma_f32_16x16x32_bf16 v[180:183], a[148:151], v[32:35], v[180:183]\n"
"v_exp_f32 v22, v22\n"
"v_mfma_f32_16x16x32_bf16 v[184:187], a[152:155], v[32:35], v[184:187]\n"
"ds_read_b128 a[132:135], v2 offset:45056\n"
"v_mfma_f32_16x16x32_bf16 v[184:187], a[184:187], v[36:39], v[184:187]\n"
"v_exp_f32 v23, v23\n"
"v_mfma_f32_16x16x32_bf16 v[188:191], a[188:191], v[36:39], v[188:191]\n"
"ds_read_b128 a[128:131], v1 offset:45056\n"
"v_cvt_pk_bf16_f32 v247, v22, v23\n"
"v_mfma_f32_16x16x32_bf16 v[188:191], a[156:159], v[32:35], v[188:191]\n"
"s_waitcnt lgkmcnt(8)\n"
"buffer_load_dwordx4 v241, s[44:47], s85 offen offset:1024 nt lds\n"
"v_mfma_f32_16x16x32_bf16 v[16:19], a[80:83], a[0:3], v[40:43]\n"
"v_exp_f32 v24, v24\n"
"v_mfma_f32_16x16x32_bf16 v[16:19], a[84:87], a[4:7], v[16:19]\n"
"v_mfma_f32_16x16x32_bf16 v[16:19], a[88:91], a[8:11], v[16:19]\n"
"v_exp_f32 v25, v25\n"
"v_mfma_f32_16x16x32_bf16 v[16:19], a[92:95], a[12:15], v[16:19]\n"
"v_cvt_pk_bf16_f32 v248, v24, v25\n"
"v_mfma_f32_16x16x32_bf16 v[20:23], a[108:111], a[12:15], v[40:43]\n"
"v_exp_f32 v26, v26\n"
"v_mfma_f32_16x16x32_bf16 v[20:23], a[104:107], a[8:11], v[20:23]\n"
"buffer_load_dwordx4 v242, s[44:47], s85 offen offset:2048 nt lds\n"
"v_mfma_f32_16x16x32_bf16 v[20:23], a[100:103], a[4:7], v[20:23]\n"
"v_exp_f32 v27, v27\n"
"v_mfma_f32_16x16x32_bf16 v[20:23], a[96:99], a[0:3], v[20:23]\n"
"v_cvt_pk_bf16_f32 v249, v26, v27\n"
"v_mfma_f32_16x16x32_bf16 v[72:75], a[208:211], v[36:39], v[72:75]\n"
"v_exp_f32 v28, v28\n"
"v_mfma_f32_16x16x32_bf16 v[192:195], a[192:195], v[36:39], v[192:195]\n"
"v_mfma_f32_16x16x32_bf16 v[192:195], a[160:163], v[32:35], v[192:195]\n"
"v_exp_f32 v29, v29\n"
"v_mfma_f32_16x16x32_bf16 v[196:199], a[164:167], v[32:35], v[196:199]\n"
"v_cvt_pk_bf16_f32 v250, v28, v29\n"
"v_mfma_f32_16x16x32_bf16 v[196:199], a[196:199], v[36:39], v[196:199]\n"
"v_exp_f32 v30, v30\n"
"v_mfma_f32_16x16x32_bf16 v[200:203], a[200:203], v[36:39], v[200:203]\n"
"buffer_load_dwordx4 v243, s[44:47], s85 offen offset:3072 nt lds\n"
"v_mfma_f32_16x16x32_bf16 v[200:203], a[168:171], v[32:35], v[200:203]\n"
"s_add_u32 s85, s85, 16384\n"
"v_exp_f32 v31, v31\n"
"v_mfma_f32_16x16x32_bf16 v[204:207], a[172:175], v[32:35], v[204:207]\n"
"v_cvt_pk_bf16_f32 v251, v30, v31\n"
"v_mfma_f32_16x16x32_bf16 v[204:207], a[204:207], v[36:39], v[204:207]\n"
"s_waitcnt lgkmcnt(0)\n"
"v_mfma_f32_16x16x32_bf16 v[24:27], a[112:115], a[0:3], v[40:43]\n"
"v_exp_f32 v16, v16\n"
"v_mfma_f32_16x16x32_bf16 v[24:27], a[116:119], a[4:7], v[24:27]\n"
"v_mfma_f32_16x16x32_bf16 v[24:27], a[120:123], a[8:11], v[24:27]\n"
"v_exp_f32 v17, v17\n"
"v_mfma_f32_16x16x32_bf16 v[24:27], a[124:127], a[12:15], v[24:27]\n"
"v_cvt_pk_bf16_f32 v32, v16, v17\n"
"v_mfma_f32_16x16x32_bf16 v[28:31], a[140:143], a[12:15], v[40:43]\n"
"v_exp_f32 v18, v18\n"
"v_mfma_f32_16x16x32_bf16 v[28:31], a[136:139], a[8:11], v[28:31]\n"
"v_mfma_f32_16x16x32_bf16 v[28:31], a[132:135], a[4:7], v[28:31]\n"
"v_exp_f32 v19, v19\n"
"v_mfma_f32_16x16x32_bf16 v[28:31], a[128:131], a[0:3], v[28:31]\n"
"v_cvt_pk_bf16_f32 v33, v18, v19\n"
"v_mfma_f32_16x16x32_bf16 v[76:79], a[208:211], v[244:247], v[76:79]\n"
"v_exp_f32 v20, v20\n"
"v_mfma_f32_16x16x32_bf16 v[208:211], a[144:147], v[244:247], v[208:211]\n"
"v_mfma_f32_16x16x32_bf16 v[208:211], a[176:179], v[248:251], v[208:211]\n"
"v_exp_f32 v21, v21\n"
"v_mfma_f32_16x16x32_bf16 v[212:215], a[180:183], v[248:251], v[212:215]\n"
"ds_read_b64_tr_b16 a[144:145], v5 offset:32768\n"
"v_cvt_pk_bf16_f32 v34, v20, v21\n"
"v_mfma_f32_16x16x32_bf16 v[212:215], a[148:151], v[244:247], v[212:215]\n"
"v_exp_f32 v22, v22\n"
"v_mfma_f32_16x16x32_bf16 v[216:219], a[152:155], v[244:247], v[216:219]\n"
"ds_read_b64_tr_b16 a[146:147], v5 offset:36864\n"
"v_mfma_f32_16x16x32_bf16 v[216:219], a[184:187], v[248:251], v[216:219]\n"
"v_exp_f32 v23, v23\n"
"v_mfma_f32_16x16x32_bf16 v[220:223], a[188:191], v[248:251], v[220:223]\n"
"ds_read_b64_tr_b16 a[176:177], v5 offset:40960\n"
"v_cvt_pk_bf16_f32 v35, v22, v23\n"
"v_mfma_f32_16x16x32_bf16 v[220:223], a[156:159], v[244:247], v[220:223]\n"
"ds_read_b64_tr_b16 a[178:179], v5 offset:45056\n"
"v_mfma_f32_16x16x32_bf16 v[16:19], a[80:83], a[16:19], v[44:47]\n"
"v_exp_f32 v24, v24\n"
"v_mfma_f32_16x16x32_bf16 v[16:19], a[84:87], a[20:23], v[16:19]\n"
"ds_read_b64_tr_b16 a[148:149], v6 offset:32768\n"
"v_mfma_f32_16x16x32_bf16 v[16:19], a[88:91], a[24:27], v[16:19]\n"
"ds_read_b64_tr_b16 a[150:151], v6 offset:36864\n"
"v_exp_f32 v25, v25\n"
"v_mfma_f32_16x16x32_bf16 v[16:19], a[92:95], a[28:31], v[16:19]\n"
"ds_read_b64_tr_b16 a[180:181], v6 offset:40960\n"
"v_cvt_pk_bf16_f32 v36, v24, v25\n"
"v_mfma_f32_16x16x32_bf16 v[20:23], a[108:111], a[28:31], v[44:47]\n"
"v_exp_f32 v26, v26\n"
"v_mfma_f32_16x16x32_bf16 v[20:23], a[104:107], a[24:27], v[20:23]\n"
"ds_read_b64_tr_b16 a[182:183], v6 offset:45056\n"
"v_mfma_f32_16x16x32_bf16 v[20:23], a[100:103], a[20:23], v[20:23]\n"
"v_exp_f32 v27, v27\n"
"v_mfma_f32_16x16x32_bf16 v[20:23], a[96:99], a[16:19], v[20:23]\n"
"ds_read_b64_tr_b16 a[152:153], v5 offset:33280\n"
"v_cvt_pk_bf16_f32 v37, v26, v27\n"
"v_mfma_f32_16x16x32_bf16 v[76:79], a[208:211], v[248:251], v[76:79]\n"
"ds_read_b64_tr_b16 a[154:155], v5 offset:37376\n"
"v_exp_f32 v28, v28\n"
"v_mfma_f32_16x16x32_bf16 v[224:227], a[192:195], v[248:251], v[224:227]\n"
"ds_read_b64_tr_b16 a[184:185], v5 offset:41472\n"
"v_mfma_f32_16x16x32_bf16 v[224:227], a[160:163], v[244:247], v[224:227]\n"
"v_exp_f32 v29, v29\n"
"v_mfma_f32_16x16x32_bf16 v[228:231], a[164:167], v[244:247], v[228:231]\n"
"ds_read_b64_tr_b16 a[186:187], v5 offset:45568\n"
"v_cvt_pk_bf16_f32 v38, v28, v29\n"
"v_mfma_f32_16x16x32_bf16 v[228:231], a[196:199], v[248:251], v[228:231]\n"
"v_exp_f32 v30, v30\n"
"v_mfma_f32_16x16x32_bf16 v[232:235], a[200:203], v[248:251], v[232:235]\n"
"ds_read_b64_tr_b16 a[156:157], v6 offset:33280\n"
"v_mfma_f32_16x16x32_bf16 v[232:235], a[168:171], v[244:247], v[232:235]\n"
"ds_read_b64_tr_b16 a[158:159], v6 offset:37376\n"
"v_exp_f32 v31, v31\n"
"v_mfma_f32_16x16x32_bf16 v[236:239], a[172:175], v[244:247], v[236:239]\n"
"ds_read_b64_tr_b16 a[188:189], v6 offset:41472\n"
"v_cvt_pk_bf16_f32 v39, v30, v31\n"
"v_mfma_f32_16x16x32_bf16 v[236:239], a[204:207], v[248:251], v[236:239]\n"
"ds_read_b64_tr_b16 a[190:191], v6 offset:45568\n"
"v_mfma_f32_16x16x32_bf16 v[24:27], a[112:115], a[16:19], v[44:47]\n"
"v_exp_f32 v16, v16\n"
"v_mfma_f32_16x16x32_bf16 v[24:27], a[116:119], a[20:23], v[24:27]\n"
"ds_read_b64_tr_b16 a[160:161], v5 offset:33792\n"
"v_mfma_f32_16x16x32_bf16 v[24:27], a[120:123], a[24:27], v[24:27]\n"
"ds_read_b64_tr_b16 a[162:163], v5 offset:37888\n"
"v_exp_f32 v17, v17\n"
"v_mfma_f32_16x16x32_bf16 v[24:27], a[124:127], a[28:31], v[24:27]\n"
"ds_read_b64_tr_b16 a[192:193], v5 offset:41984\n"
"v_cvt_pk_bf16_f32 v244, v16, v17\n"
"v_mfma_f32_16x16x32_bf16 v[28:31], a[140:143], a[28:31], v[44:47]\n"
"v_exp_f32 v18, v18\n"
"v_mfma_f32_16x16x32_bf16 v[28:31], a[136:139], a[24:27], v[28:31]\n"
"ds_read_b64_tr_b16 a[194:195], v5 offset:46080\n"
"v_mfma_f32_16x16x32_bf16 v[28:31], a[132:135], a[20:23], v[28:31]\n"
"v_exp_f32 v19, v19\n"
"v_mfma_f32_16x16x32_bf16 v[28:31], a[128:131], a[16:19], v[28:31]\n"
"ds_read_b64_tr_b16 a[164:165], v6 offset:33792\n"
"v_cvt_pk_bf16_f32 v245, v18, v19\n"
"v_mfma_f32_16x16x32_bf16 v[60:63], a[208:211], v[32:35], v[60:63]\n"
"s_waitcnt lgkmcnt(15)\n"
"ds_read_b64_tr_b16 a[166:167], v6 offset:37888\n"
"v_exp_f32 v20, v20\n"
"v_mfma_f32_16x16x32_bf16 v[80:83], a[144:147], v[32:35], v[80:83]\n"
"ds_read_b64_tr_b16 a[196:197], v6 offset:41984\n"
"v_mfma_f32_16x16x32_bf16 v[80:83], a[176:179], v[36:39], v[80:83]\n"
"s_waitcnt lgkmcnt(15)\n"
"v_exp_f32 v21, v21\n"
"v_mfma_f32_16x16x32_bf16 v[84:87], a[180:183], v[36:39], v[84:87]\n"
"ds_read_b64_tr_b16 a[198:199], v6 offset:46080\n"
"v_cvt_pk_bf16_f32 v246, v20, v21\n"
"v_mfma_f32_16x16x32_bf16 v[84:87], a[148:151], v[32:35], v[84:87]\n"
"s_waitcnt lgkmcnt(12)\n"
"v_exp_f32 v22, v22\n"
"v_mfma_f32_16x16x32_bf16 v[88:91], a[152:155], v[32:35], v[88:91]\n"
"ds_read_b64_tr_b16 a[168:169], v5 offset:34304\n"
"v_mfma_f32_16x16x32_bf16 v[88:91], a[184:187], v[36:39], v[88:91]\n"
"s_waitcnt lgkmcnt(9)\n"
"v_exp_f32 v23, v23\n"
"v_mfma_f32_16x16x32_bf16 v[92:95], a[188:191], v[36:39], v[92:95]\n"
"ds_read_b64_tr_b16 a[170:171], v5 offset:38400\n"
"v_cvt_pk_bf16_f32 v247, v22, v23\n"
"v_mfma_f32_16x16x32_bf16 v[92:95], a[156:159], v[32:35], v[92:95]\n"
"ds_read_b64_tr_b16 a[200:201], v5 offset:42496\n"
"v_mfma_f32_16x16x32_bf16 v[16:19], a[80:83], a[32:35], v[48:51]\n"
"v_exp_f32 v24, v24\n"
"v_mfma_f32_16x16x32_bf16 v[16:19], a[84:87], a[36:39], v[16:19]\n"
"ds_read_b64_tr_b16 a[202:203], v5 offset:46592\n"
"v_mfma_f32_16x16x32_bf16 v[16:19], a[88:91], a[40:43], v[16:19]\n"
"v_exp_f32 v25, v25\n"
"v_mfma_f32_16x16x32_bf16 v[16:19], a[92:95], a[44:47], v[16:19]\n"
"ds_read_b64_tr_b16 a[172:173], v6 offset:34304\n"
"v_cvt_pk_bf16_f32 v248, v24, v25\n"
"v_mfma_f32_16x16x32_bf16 v[20:23], a[108:111], a[44:47], v[48:51]\n"
"v_exp_f32 v26, v26\n"
"v_mfma_f32_16x16x32_bf16 v[20:23], a[104:107], a[40:43], v[20:23]\n"
"ds_read_b64_tr_b16 a[174:175], v6 offset:38400\n"
"v_mfma_f32_16x16x32_bf16 v[20:23], a[100:103], a[36:39], v[20:23]\n"
"v_exp_f32 v27, v27\n"
"v_mfma_f32_16x16x32_bf16 v[20:23], a[96:99], a[32:35], v[20:23]\n"
"ds_read_b64_tr_b16 a[204:205], v6 offset:42496\n"
"v_cvt_pk_bf16_f32 v249, v26, v27\n"
"v_mfma_f32_16x16x32_bf16 v[60:63], a[208:211], v[36:39], v[60:63]\n"
"s_waitcnt lgkmcnt(11)\n"
"v_exp_f32 v28, v28\n"
"v_mfma_f32_16x16x32_bf16 v[96:99], a[192:195], v[36:39], v[96:99]\n"
"ds_read_b64_tr_b16 a[206:207], v6 offset:46592\n"
"v_mfma_f32_16x16x32_bf16 v[96:99], a[160:163], v[32:35], v[96:99]\n"
"s_waitcnt lgkmcnt(8)\n"
"v_exp_f32 v29, v29\n"
"v_mfma_f32_16x16x32_bf16 v[100:103], a[164:167], v[32:35], v[100:103]\n"
"v_cvt_pk_bf16_f32 v250, v28, v29\n"
"v_mfma_f32_16x16x32_bf16 v[100:103], a[196:199], v[36:39], v[100:103]\n"
"s_waitcnt lgkmcnt(4)\n"
"s_add_u32 m0, s84, 0\n"
"v_exp_f32 v30, v30\n"
"v_mfma_f32_16x16x32_bf16 v[104:107], a[200:203], v[36:39], v[104:107]\n"
"buffer_load_dwordx4 v8, s[48:51], s86 offen offset:0 nt lds\n"
"v_mfma_f32_16x16x32_bf16 v[104:107], a[168:171], v[32:35], v[104:107]\n"
"s_waitcnt lgkmcnt(0)\n"
"v_exp_f32 v31, v31\n"
"v_mfma_f32_16x16x32_bf16 v[108:111], a[172:175], v[32:35], v[108:111]\n"
"v_cvt_pk_bf16_f32 v251, v30, v31\n"
"v_mfma_f32_16x16x32_bf16 v[108:111], a[204:207], v[36:39], v[108:111]\n"
"v_mfma_f32_16x16x32_bf16 v[24:27], a[112:115], a[32:35], v[48:51]\n"
"v_exp_f32 v16, v16\n"
"v_mfma_f32_16x16x32_bf16 v[24:27], a[116:119], a[36:39], v[24:27]\n"
"v_mfma_f32_16x16x32_bf16 v[24:27], a[120:123], a[40:43], v[24:27]\n"
"v_exp_f32 v17, v17\n"
"v_mfma_f32_16x16x32_bf16 v[24:27], a[124:127], a[44:47], v[24:27]\n"
"v_cvt_pk_bf16_f32 v32, v16, v17\n"
"v_mfma_f32_16x16x32_bf16 v[28:31], a[140:143], a[44:47], v[48:51]\n"
"v_exp_f32 v18, v18\n"
"v_mfma_f32_16x16x32_bf16 v[28:31], a[136:139], a[40:43], v[28:31]\n"
"v_mfma_f32_16x16x32_bf16 v[28:31], a[132:135], a[36:39], v[28:31]\n"
"v_exp_f32 v19, v19\n"
"v_mfma_f32_16x16x32_bf16 v[28:31], a[128:131], a[32:35], v[28:31]\n"
"v_cvt_pk_bf16_f32 v33, v18, v19\n"
"v_mfma_f32_16x16x32_bf16 v[64:67], a[208:211], v[244:247], v[64:67]\n"
"v_exp_f32 v20, v20\n"
"v_mfma_f32_16x16x32_bf16 v[112:115], a[144:147], v[244:247], v[112:115]\n"
"buffer_load_dwordx4 v8, s[48:51], s87 offen offset:1024 nt lds\n"
"v_mfma_f32_16x16x32_bf16 v[112:115], a[176:179], v[248:251], v[112:115]\n"
"v_exp_f32 v21, v21\n"
"v_mfma_f32_16x16x32_bf16 v[116:119], a[180:183], v[248:251], v[116:119]\n"
"v_cvt_pk_bf16_f32 v34, v20, v21\n"
"v_mfma_f32_16x16x32_bf16 v[116:119], a[148:151], v[244:247], v[116:119]\n"
"v_exp_f32 v22, v22\n"
"v_mfma_f32_16x16x32_bf16 v[120:123], a[152:155], v[244:247], v[120:123]\n"
"v_mfma_f32_16x16x32_bf16 v[120:123], a[184:187], v[248:251], v[120:123]\n"
"v_exp_f32 v23, v23\n"
"v_mfma_f32_16x16x32_bf16 v[124:127], a[188:191], v[248:251], v[124:127]\n"
"v_cvt_pk_bf16_f32 v35, v22, v23\n"
"v_mfma_f32_16x16x32_bf16 v[124:127], a[156:159], v[244:247], v[124:127]\n"
"v_mfma_f32_16x16x32_bf16 v[16:19], a[80:83], a[48:51], v[52:55]\n"
"v_exp_f32 v24, v24\n"
"v_mfma_f32_16x16x32_bf16 v[16:19], a[84:87], a[52:55], v[16:19]\n"
"v_mfma_f32_16x16x32_bf16 v[16:19], a[88:91], a[56:59], v[16:19]\n"
"v_exp_f32 v25, v25\n"
"v_mfma_f32_16x16x32_bf16 v[16:19], a[92:95], a[60:63], v[16:19]\n"
"v_cvt_pk_bf16_f32 v36, v24, v25\n"
"v_mfma_f32_16x16x32_bf16 v[20:23], a[108:111], a[60:63], v[52:55]\n"
"v_exp_f32 v26, v26\n"
"v_mfma_f32_16x16x32_bf16 v[20:23], a[104:107], a[56:59], v[20:23]\n"
"buffer_load_dwordx4 v8, s[48:51], s86 offen offset:2048 nt lds\n"
"v_mfma_f32_16x16x32_bf16 v[20:23], a[100:103], a[52:55], v[20:23]\n"
"v_exp_f32 v27, v27\n"
"v_mfma_f32_16x16x32_bf16 v[20:23], a[96:99], a[48:51], v[20:23]\n"
"v_cvt_pk_bf16_f32 v37, v26, v27\n"
"v_mfma_f32_16x16x32_bf16 v[64:67], a[208:211], v[248:251], v[64:67]\n"
"v_exp_f32 v28, v28\n"
"v_mfma_f32_16x16x32_bf16 v[128:131], a[192:195], v[248:251], v[128:131]\n"
"v_mfma_f32_16x16x32_bf16 v[128:131], a[160:163], v[244:247], v[128:131]\n"
"v_exp_f32 v29, v29\n"
"v_mfma_f32_16x16x32_bf16 v[132:135], a[164:167], v[244:247], v[132:135]\n"
"v_cvt_pk_bf16_f32 v38, v28, v29\n"
"v_mfma_f32_16x16x32_bf16 v[132:135], a[196:199], v[248:251], v[132:135]\n"
"v_exp_f32 v30, v30\n"
"v_mfma_f32_16x16x32_bf16 v[136:139], a[200:203], v[248:251], v[136:139]\n"
"v_mfma_f32_16x16x32_bf16 v[136:139], a[168:171], v[244:247], v[136:139]\n"
"v_exp_f32 v31, v31\n"
"v_mfma_f32_16x16x32_bf16 v[140:143], a[172:175], v[244:247], v[140:143]\n"
"v_cvt_pk_bf16_f32 v39, v30, v31\n"
"v_mfma_f32_16x16x32_bf16 v[140:143], a[204:207], v[248:251], v[140:143]\n"
"buffer_load_dwordx4 v8, s[48:51], s87 offen offset:3072 nt lds\n"
"v_mfma_f32_16x16x32_bf16 v[24:27], a[112:115], a[48:51], v[52:55]\n"
"s_add_u32 s86, s86, 16384\n"
"s_add_u32 s87, s87, 16384\n"
"v_exp_f32 v16, v16\n"
"v_mfma_f32_16x16x32_bf16 v[24:27], a[116:119], a[52:55], v[24:27]\n"
"v_mfma_f32_16x16x32_bf16 v[24:27], a[120:123], a[56:59], v[24:27]\n"
"v_exp_f32 v17, v17\n"
"v_mfma_f32_16x16x32_bf16 v[24:27], a[124:127], a[60:63], v[24:27]\n"
"v_cvt_pk_bf16_f32 v244, v16, v17\n"
"v_mfma_f32_16x16x32_bf16 v[28:31], a[140:143], a[60:63], v[52:55]\n"
"v_exp_f32 v18, v18\n"
"v_mfma_f32_16x16x32_bf16 v[28:31], a[136:139], a[56:59], v[28:31]\n"
"v_mfma_f32_16x16x32_bf16 v[28:31], a[132:135], a[52:55], v[28:31]\n"
"v_exp_f32 v19, v19\n"
"v_mfma_f32_16x16x32_bf16 v[28:31], a[128:131], a[48:51], v[28:31]\n"
"v_cvt_pk_bf16_f32 v245, v18, v19\n"
"v_mfma_f32_16x16x32_bf16 v[68:71], a[208:211], v[32:35], v[68:71]\n"
"v_exp_f32 v20, v20\n"
"v_mfma_f32_16x16x32_bf16 v[144:147], a[144:147], v[32:35], v[144:147]\n"
"buffer_load_dword v10, off, s[4:7], s9\n"
"v_mfma_f32_16x16x32_bf16 v[144:147], a[176:179], v[36:39], v[144:147]\n"
"s_add_u32 s9, s9, 4096\n"
"v_exp_f32 v21, v21\n"
"v_mfma_f32_16x16x32_bf16 v[148:151], a[180:183], v[36:39], v[148:151]\n"
"v_cvt_pk_bf16_f32 v246, v20, v21\n"
"v_mfma_f32_16x16x32_bf16 v[148:151], a[148:151], v[32:35], v[148:151]\n"
"v_exp_f32 v22, v22\n"
"v_mfma_f32_16x16x32_bf16 v[152:155], a[152:155], v[32:35], v[152:155]\n"
"v_mfma_f32_16x16x32_bf16 v[152:155], a[184:187], v[36:39], v[152:155]\n"
"v_exp_f32 v23, v23\n"
"v_mfma_f32_16x16x32_bf16 v[156:159], a[188:191], v[36:39], v[156:159]\n"
"v_cvt_pk_bf16_f32 v247, v22, v23\n"
"v_mfma_f32_16x16x32_bf16 v[156:159], a[156:159], v[32:35], v[156:159]\n"
"v_mfma_f32_16x16x32_bf16 v[16:19], a[80:83], a[64:67], v[56:59]\n"
"s_waitcnt vmcnt(8) lgkmcnt(0)\n"
"s_barrier\n"
"s_cmp_eq_u32 s88, 2\n"
"s_cbranch_scc0 Lhq_%=\n"
"buffer_load_dwordx2 v[12:13], v7, s[64:67], 0 offen sc1\n"
"Lhq_%=:\n"
"v_exp_f32 v24, v24\n"
"v_mfma_f32_16x16x32_bf16 v[16:19], a[84:87], a[68:71], v[16:19]\n"
"ds_read_b128 a[80:83], v1 offset:49152\n"
"v_mfma_f32_16x16x32_bf16 v[16:19], a[88:91], a[72:75], v[16:19]\n"
"v_exp_f32 v25, v25\n"
"v_mfma_f32_16x16x32_bf16 v[16:19], a[92:95], a[76:79], v[16:19]\n"
"ds_read_b128 a[84:87], v2 offset:49152\n"
"v_cvt_pk_bf16_f32 v248, v24, v25\n"
"v_mfma_f32_16x16x32_bf16 v[20:23], a[108:111], a[76:79], v[56:59]\n"
"v_exp_f32 v26, v26\n"
"v_mfma_f32_16x16x32_bf16 v[20:23], a[104:107], a[72:75], v[20:23]\n"
"ds_read_b128 a[88:91], v3 offset:49152\n"
"v_mfma_f32_16x16x32_bf16 v[20:23], a[100:103], a[68:71], v[20:23]\n"
"v_exp_f32 v27, v27\n"
"v_mfma_f32_16x16x32_bf16 v[20:23], a[96:99], a[64:67], v[20:23]\n"
"ds_read_b128 a[92:95], v4 offset:49152\n"
"v_cvt_pk_bf16_f32 v249, v26, v27\n"
"v_mfma_f32_16x16x32_bf16 v[68:71], a[208:211], v[36:39], v[68:71]\n"
"v_exp_f32 v28, v28\n"
"v_mfma_f32_16x16x32_bf16 v[160:163], a[192:195], v[36:39], v[160:163]\n"
"ds_read_b128 a[108:111], v4 offset:53248\n"
"v_mfma_f32_16x16x32_bf16 v[160:163], a[160:163], v[32:35], v[160:163]\n"
"v_exp_f32 v29, v29\n"
"v_mfma_f32_16x16x32_bf16 v[164:167], a[164:167], v[32:35], v[164:167]\n"
"ds_read_b128 a[104:107], v3 offset:53248\n"
"v_cvt_pk_bf16_f32 v250, v28, v29\n"
"v_mfma_f32_16x16x32_bf16 v[164:167], a[196:199], v[36:39], v[164:167]\n"
"v_exp_f32 v30, v30\n"
"v_mfma_f32_16x16x32_bf16 v[168:171], a[200:203], v[36:39], v[168:171]\n"
"ds_read_b128 a[100:103], v2 offset:53248\n"
"v_mfma_f32_16x16x32_bf16 v[168:171], a[168:171], v[32:35], v[168:171]\n"
"v_exp_f32 v31, v31\n"
"v_mfma_f32_16x16x32_bf16 v[172:175], a[172:175], v[32:35], v[172:175]\n"
"s_add_u32 m0, s83, 16384\n"
"ds_read_b128 a[96:99], v1 offset:53248\n"
"v_cvt_pk_bf16_f32 v251, v30, v31\n"
"v_mfma_f32_16x16x32_bf16 v[172:175], a[204:207], v[36:39], v[172:175]\n"
"buffer_load_dwordx4 v240, s[44:47], s85 offen offset:0 nt lds\n"
"v_mfma_f32_16x16x32_bf16 v[24:27], a[112:115], a[64:67], v[56:59]\n"
"v_exp_f32 v16, v16\n"
"v_mfma_f32_16x16x32_bf16 v[24:27], a[116:119], a[68:71], v[24:27]\n"
"ds_read_b128 a[112:115], v1 offset:57344\n"
"v_mfma_f32_16x16x32_bf16 v[24:27], a[120:123], a[72:75], v[24:27]\n"
"v_exp_f32 v17, v17\n"
"v_mfma_f32_16x16x32_bf16 v[24:27], a[124:127], a[76:79], v[24:27]\n"
"ds_read_b128 a[116:119], v2 offset:57344\n"
"v_cvt_pk_bf16_f32 v32, v16, v17\n"
"v_mfma_f32_16x16x32_bf16 v[28:31], a[140:143], a[76:79], v[56:59]\n"
"v_exp_f32 v18, v18\n"
"v_mfma_f32_16x16x32_bf16 v[28:31], a[136:139], a[72:75], v[28:31]\n"
"ds_read_b128 a[120:123], v3 offset:57344\n"
"v_mfma_f32_16x16x32_bf16 v[28:31], a[132:135], a[68:71], v[28:31]\n"
"v_exp_f32 v19, v19\n"
"v_mfma_f32_16x16x32_bf16 v[28:31], a[128:131], a[64:67], v[28:31]\n"
"ds_read_b128 a[124:127], v4 offset:57344\n"
"v_cvt_pk_bf16_f32 v33, v18, v19\n"
"v_mfma_f32_16x16x32_bf16 v[72:75], a[208:211], v[244:247], v[72:75]\n"
"v_exp_f32 v20, v20\n"
"v_mfma_f32_16x16x32_bf16 v[176:179], a[144:147], v[244:247], v[176:179]\n"
"ds_read_b128 a[140:143], v4 offset:61440\n"
"v_mfma_f32_16x16x32_bf16 v[176:179], a[176:179], v[248:251], v[176:179]\n"
"v_exp_f32 v21, v21\n"
"v_mfma_f32_16x16x32_bf16 v[180:183], a[180:183], v[248:251], v[180:183]\n"
"ds_read_b128 a[136:139], v3 offset:61440\n"
"v_cvt_pk_bf16_f32 v34, v20, v21\n"
"v_mfma_f32_16x16x32_bf16 v[180:183], a[148:151], v[244:247], v[180:183]\n"
"v_exp_f32 v22, v22\n"
"v_mfma_f32_16x16x32_bf16 v[184:187], a[152:155], v[244:247], v[184:187]\n"
"ds_read_b128 a[132:135], v2 offset:61440\n"
"v_mfma_f32_16x16x32_bf16 v[184:187], a[184:187], v[248:251], v[184:187]\n"
"v_exp_f32 v23, v23\n"
"v_mfma_f32_16x16x32_bf16 v[188:191], a[188:191], v[248:251], v[188:191]\n"
"ds_read_b128 a[128:131], v1 offset:61440\n"
"v_cvt_pk_bf16_f32 v35, v22, v23\n"
"v_mfma_f32_16x16x32_bf16 v[188:191], a[156:159], v[244:247], v[188:191]\n"
"s_waitcnt lgkmcnt(8)\n"
"buffer_load_dwordx4 v241, s[44:47], s85 offen offset:1024 nt lds\n"
"v_mfma_f32_16x16x32_bf16 v[16:19], a[80:83], a[0:3], v[40:43]\n"
"v_exp_f32 v24, v24\n"
"v_mfma_f32_16x16x32_bf16 v[16:19], a[84:87], a[4:7], v[16:19]\n"
"v_mfma_f32_16x16x32_bf16 v[16:19], a[88:91], a[8:11], v[16:19]\n"
"v_exp_f32 v25, v25\n"
"v_mfma_f32_16x16x32_bf16 v[16:19], a[92:95], a[12:15], v[16:19]\n"
"v_cvt_pk_bf16_f32 v36, v24, v25\n"
"v_mfma_f32_16x16x32_bf16 v[20:23], a[108:111], a[12:15], v[40:43]\n"
"v_exp_f32 v26, v26\n"
"v_mfma_f32_16x16x32_bf16 v[20:23], a[104:107], a[8:11], v[20:23]\n"
"buffer_load_dwordx4 v242, s[44:47], s85 offen offset:2048 nt lds\n"
"v_mfma_f32_16x16x32_bf16 v[20:23], a[100:103], a[4:7], v[20:23]\n"
"v_exp_f32 v27, v27\n"
"v_mfma_f32_16x16x32_bf16 v[20:23], a[96:99], a[0:3], v[20:23]\n"
"v_cvt_pk_bf16_f32 v37, v26, v27\n"
"v_mfma_f32_16x16x32_bf16 v[72:75], a[208:211], v[248:251], v[72:75]\n"
"v_exp_f32 v28, v28\n"
"v_mfma_f32_16x16x32_bf16 v[192:195], a[192:195], v[248:251], v[192:195]\n"
"v_mfma_f32_16x16x32_bf16 v[192:195], a[160:163], v[244:247], v[192:195]\n"
"v_exp_f32 v29, v29\n"
"v_mfma_f32_16x16x32_bf16 v[196:199], a[164:167], v[244:247], v[196:199]\n"
"v_cvt_pk_bf16_f32 v38, v28, v29\n"
"v_mfma_f32_16x16x32_bf16 v[196:199], a[196:199], v[248:251], v[196:199]\n"
"v_exp_f32 v30, v30\n"
"v_mfma_f32_16x16x32_bf16 v[200:203], a[200:203], v[248:251], v[200:203]\n"
"buffer_load_dwordx4 v243, s[44:47], s85 offen offset:3072 nt lds\n"
"v_mfma_f32_16x16x32_bf16 v[200:203], a[168:171], v[244:247], v[200:203]\n"
"s_add_u32 s85, s85, 16384\n"
"v_exp_f32 v31, v31\n"
"v_mfma_f32_16x16x32_bf16 v[204:207], a[172:175], v[244:247], v[204:207]\n"
"v_cvt_pk_bf16_f32 v39, v30, v31\n"
"v_mfma_f32_16x16x32_bf16 v[204:207], a[204:207], v[248:251], v[204:207]\n"
"s_waitcnt lgkmcnt(0)\n"
"v_mfma_f32_16x16x32_bf16 v[24:27], a[112:115], a[0:3], v[40:43]\n"
"v_exp_f32 v16, v16\n"
"v_mfma_f32_16x16x32_bf16 v[24:27], a[116:119], a[4:7], v[24:27]\n"
"v_mfma_f32_16x16x32_bf16 v[24:27], a[120:123], a[8:11], v[24:27]\n"
"v_exp_f32 v17, v17\n"
"v_mfma_f32_16x16x32_bf16 v[24:27], a[124:127], a[12:15], v[24:27]\n"
"v_cvt_pk_bf16_f32 v244, v16, v17\n"
"v_mfma_f32_16x16x32_bf16 v[28:31], a[140:143], a[12:15], v[40:43]\n"
"v_exp_f32 v18, v18\n"
"v_mfma_f32_16x16x32_bf16 v[28:31], a[136:139], a[8:11], v[28:31]\n"
"v_mfma_f32_16x16x32_bf16 v[28:31], a[132:135], a[4:7], v[28:31]\n"
"v_exp_f32 v19, v19\n"
"v_mfma_f32_16x16x32_bf16 v[28:31], a[128:131], a[0:3], v[28:31]\n"
"v_cvt_pk_bf16_f32 v245, v18, v19\n"
"v_mfma_f32_16x16x32_bf16 v[76:79], a[208:211], v[32:35], v[76:79]\n"
"v_exp_f32 v20, v20\n"
"v_mfma_f32_16x16x32_bf16 v[208:211], a[144:147], v[32:35], v[208:211]\n"
"v_mfma_f32_16x16x32_bf16 v[208:211], a[176:179], v[36:39], v[208:211]\n"
"v_exp_f32 v21, v21\n"
"v_mfma_f32_16x16x32_bf16 v[212:215], a[180:183], v[36:39], v[212:215]\n"
"ds_read_b64_tr_b16 a[144:145], v5 offset:49152\n"
"v_cvt_pk_bf16_f32 v246, v20, v21\n"
"v_mfma_f32_16x16x32_bf16 v[212:215], a[148:151], v[32:35], v[212:215]\n"
"v_exp_f32 v22, v22\n"
"v_mfma_f32_16x16x32_bf16 v[216:219], a[152:155], v[32:35], v[216:219]\n"
"ds_read_b64_tr_b16 a[146:147], v5 offset:53248\n"
"v_mfma_f32_16x16x32_bf16 v[216:219], a[184:187], v[36:39], v[216:219]\n"
"v_exp_f32 v23, v23\n"
"v_mfma_f32_16x16x32_bf16 v[220:223], a[188:191], v[36:39], v[220:223]\n"
"ds_read_b64_tr_b16 a[176:177], v5 offset:57344\n"
"v_cvt_pk_bf16_f32 v247, v22, v23\n"
"v_mfma_f32_16x16x32_bf16 v[220:223], a[156:159], v[32:35], v[220:223]\n"
"ds_read_b64_tr_b16 a[178:179], v5 offset:61440\n"
"v_mfma_f32_16x16x32_bf16 v[16:19], a[80:83], a[16:19], v[44:47]\n"
"v_exp_f32 v24, v24\n"
"v_mfma_f32_16x16x32_bf16 v[16:19], a[84:87], a[20:23], v[16:19]\n"
"ds_read_b64_tr_b16 a[148:149], v6 offset:49152\n"
"v_mfma_f32_16x16x32_bf16 v[16:19], a[88:91], a[24:27], v[16:19]\n"
"ds_read_b64_tr_b16 a[150:151], v6 offset:53248\n"
"v_exp_f32 v25, v25\n"
"v_mfma_f32_16x16x32_bf16 v[16:19], a[92:95], a[28:31], v[16:19]\n"
"ds_read_b64_tr_b16 a[180:181], v6 offset:57344\n"
"v_cvt_pk_bf16_f32 v248, v24, v25\n"
"v_mfma_f32_16x16x32_bf16 v[20:23], a[108:111], a[28:31], v[44:47]\n"
"v_exp_f32 v26, v26\n"
"v_mfma_f32_16x16x32_bf16 v[20:23], a[104:107], a[24:27], v[20:23]\n"
"ds_read_b64_tr_b16 a[182:183], v6 offset:61440\n"
"v_mfma_f32_16x16x32_bf16 v[20:23], a[100:103], a[20:23], v[20:23]\n"
"v_exp_f32 v27, v27\n"
"v_mfma_f32_16x16x32_bf16 v[20:23], a[96:99], a[16:19], v[20:23]\n"
"ds_read_b64_tr_b16 a[152:153], v5 offset:49664\n"
"v_cvt_pk_bf16_f32 v249, v26, v27\n"
"v_mfma_f32_16x16x32_bf16 v[76:79], a[208:211], v[36:39], v[76:79]\n"
"ds_read_b64_tr_b16 a[154:155], v5 offset:53760\n"
"v_exp_f32 v28, v28\n"
"v_mfma_f32_16x16x32_bf16 v[224:227], a[192:195], v[36:39], v[224:227]\n"
"ds_read_b64_tr_b16 a[184:185], v5 offset:57856\n"
"v_mfma_f32_16x16x32_bf16 v[224:227], a[160:163], v[32:35], v[224:227]\n"
"v_exp_f32 v29, v29\n"
"v_mfma_f32_16x16x32_bf16 v[228:231], a[164:167], v[32:35], v[228:231]\n"
"ds_read_b64_tr_b16 a[186:187], v5 offset:61952\n"
"v_cvt_pk_bf16_f32 v250, v28, v29\n"
"v_mfma_f32_16x16x32_bf16 v[228:231], a[196:199], v[36:39], v[228:231]\n"
"v_exp_f32 v30, v30\n"
"v_mfma_f32_16x16x32_bf16 v[232:235], a[200:203], v[36:39], v[232:235]\n"
"ds_read_b64_tr_b16 a[156:157], v6 offset:49664\n"
"v_mfma_f32_16x16x32_bf16 v[232:235], a[168:171], v[32:35], v[232:235]\n"
"ds_read_b64_tr_b16 a[158:159], v6 offset:53760\n"
"v_exp_f32 v31, v31\n"
"v_mfma_f32_16x16x32_bf16 v[236:239], a[172:175], v[32:35], v[236:239]\n"
"ds_read_b64_tr_b16 a[188:189], v6 offset:57856\n"
"v_cvt_pk_bf16_f32 v251, v30, v31\n"
"v_mfma_f32_16x16x32_bf16 v[236:239], a[204:207], v[36:39], v[236:239]\n"
"ds_read_b64_tr_b16 a[190:191], v6 offset:61952\n"
"v_mfma_f32_16x16x32_bf16 v[24:27], a[112:115], a[16:19], v[44:47]\n"
"v_exp_f32 v16, v16\n"
"v_mfma_f32_16x16x32_bf16 v[24:27], a[116:119], a[20:23], v[24:27]\n"
"ds_read_b64_tr_b16 a[160:161], v5 offset:50176\n"
"v_mfma_f32_16x16x32_bf16 v[24:27], a[120:123], a[24:27], v[24:27]\n"
"ds_read_b64_tr_b16 a[162:163], v5 offset:54272\n"
"v_exp_f32 v17, v17\n"
"v_mfma_f32_16x16x32_bf16 v[24:27], a[124:127], a[28:31], v[24:27]\n"
"ds_read_b64_tr_b16 a[192:193], v5 offset:58368\n"
"v_cvt_pk_bf16_f32 v32, v16, v17\n"
"v_mfma_f32_16x16x32_bf16 v[28:31], a[140:143], a[28:31], v[44:47]\n"
"v_exp_f32 v18, v18\n"
"v_mfma_f32_16x16x32_bf16 v[28:31], a[136:139], a[24:27], v[28:31]\n"
"ds_read_b64_tr_b16 a[194:195], v5 offset:62464\n"
"v_mfma_f32_16x16x32_bf16 v[28:31], a[132:135], a[20:23], v[28:31]\n"
"v_exp_f32 v19, v19\n"
"v_mfma_f32_16x16x32_bf16 v[28:31], a[128:131], a[16:19], v[28:31]\n"
"ds_read_b64_tr_b16 a[164:165], v6 offset:50176\n"
"v_cvt_pk_bf16_f32 v33, v18, v19\n"
"v_mfma_f32_16x16x32_bf16 v[60:63], a[208:211], v[244:247], v[60:63]\n"
"s_waitcnt lgkmcnt(15)\n"
"ds_read_b64_tr_b16 a[166:167], v6 offset:54272\n"
"v_exp_f32 v20, v20\n"
"v_mfma_f32_16x16x32_bf16 v[80:83], a[144:147], v[244:247], v[80:83]\n"
"ds_read_b64_tr_b16 a[196:197], v6 offset:58368\n"
"v_mfma_f32_16x16x32_bf16 v[80:83], a[176:179], v[248:251], v[80:83]\n"
"s_waitcnt lgkmcnt(15)\n"
"v_exp_f32 v21, v21\n"
"v_mfma_f32_16x16x32_bf16 v[84:87], a[180:183], v[248:251], v[84:87]\n"
"ds_read_b64_tr_b16 a[198:199], v6 offset:62464\n"
"v_cvt_pk_bf16_f32 v34, v20, v21\n"
"v_mfma_f32_16x16x32_bf16 v[84:87], a[148:151], v[244:247], v[84:87]\n"
"s_waitcnt lgkmcnt(12)\n"
"v_exp_f32 v22, v22\n"
"v_mfma_f32_16x16x32_bf16 v[88:91], a[152:155], v[244:247], v[88:91]\n"
"ds_read_b64_tr_b16 a[168:169], v5 offset:50688\n"
"v_mfma_f32_16x16x32_bf16 v[88:91], a[184:187], v[248:251], v[88:91]\n"
"s_waitcnt lgkmcnt(9)\n"
"v_exp_f32 v23, v23\n"
"v_mfma_f32_16x16x32_bf16 v[92:95], a[188:191], v[248:251], v[92:95]\n"
"ds_read_b64_tr_b16 a[170:171], v5 offset:54784\n"
"v_cvt_pk_bf16_f32 v35, v22, v23\n"
"v_mfma_f32_16x16x32_bf16 v[92:95], a[156:159], v[244:247], v[92:95]\n"
"ds_read_b64_tr_b16 a[200:201], v5 offset:58880\n"
"v_mfma_f32_16x16x32_bf16 v[16:19], a[80:83], a[32:35], v[48:51]\n"
"v_exp_f32 v24, v24\n"
"v_mfma_f32_16x16x32_bf16 v[16:19], a[84:87], a[36:39], v[16:19]\n"
"ds_read_b64_tr_b16 a[202:203], v5 offset:62976\n"
"v_mfma_f32_16x16x32_bf16 v[16:19], a[88:91], a[40:43], v[16:19]\n"
"v_exp_f32 v25, v25\n"
"v_mfma_f32_16x16x32_bf16 v[16:19], a[92:95], a[44:47], v[16:19]\n"
"ds_read_b64_tr_b16 a[172:173], v6 offset:50688\n"
"v_cvt_pk_bf16_f32 v36, v24, v25\n"
"v_mfma_f32_16x16x32_bf16 v[20:23], a[108:111], a[44:47], v[48:51]\n"
"v_exp_f32 v26, v26\n"
"v_mfma_f32_16x16x32_bf16 v[20:23], a[104:107], a[40:43], v[20:23]\n"
"ds_read_b64_tr_b16 a[174:175], v6 offset:54784\n"
"v_mfma_f32_16x16x32_bf16 v[20:23], a[100:103], a[36:39], v[20:23]\n"
"v_exp_f32 v27, v27\n"
"v_mfma_f32_16x16x32_bf16 v[20:23], a[96:99], a[32:35], v[20:23]\n"
"ds_read_b64_tr_b16 a[204:205], v6 offset:58880\n"
"v_cvt_pk_bf16_f32 v37, v26, v27\n"
"v_mfma_f32_16x16x32_bf16 v[60:63], a[208:211], v[248:251], v[60:63]\n"
"s_waitcnt lgkmcnt(11)\n"
"v_exp_f32 v28, v28\n"
"v_mfma_f32_16x16x32_bf16 v[96:99], a[192:195], v[248:251], v[96:99]\n"
"ds_read_b64_tr_b16 a[206:207], v6 offset:62976\n"
"v_mfma_f32_16x16x32_bf16 v[96:99], a[160:163], v[244:247], v[96:99]\n"
"s_waitcnt lgkmcnt(8)\n"
"v_exp_f32 v29, v29\n"
"v_mfma_f32_16x16x32_bf16 v[100:103], a[164:167], v[244:247], v[100:103]\n"
"v_cvt_pk_bf16_f32 v38, v28, v29\n"
"v_mfma_f32_16x16x32_bf16 v[100:103], a[196:199], v[248:251], v[100:103]\n"
"s_waitcnt lgkmcnt(4)\n"
"s_add_u32 m0, s84, 16384\n"
"v_exp_f32 v30, v30\n"
"v_mfma_f32_16x16x32_bf16 v[104:107], a[200:203], v[248:251], v[104:107]\n"
"buffer_load_dwordx4 v8, s[48:51], s86 offen offset:0 nt lds\n"
"v_mfma_f32_16x16x32_bf16 v[104:107], a[168:171], v[244:247], v[104:107]\n"
"s_waitcnt lgkmcnt(0)\n"
"v_exp_f32 v31, v31\n"
"v_mfma_f32_16x16x32_bf16 v[108:111], a[172:175], v[244:247], v[108:111]\n"
"v_cvt_pk_bf16_f32 v39, v30, v31\n"
"v_mfma_f32_16x16x32_bf16 v[108:111], a[204:207], v[248:251], v[108:111]\n"
"v_mfma_f32_16x16x32_bf16 v[24:27], a[112:115], a[32:35], v[48:51]\n"
"v_exp_f32 v16, v16\n"
"v_mfma_f32_16x16x32_bf16 v[24:27], a[116:119], a[36:39], v[24:27]\n"
"v_mfma_f32_16x16x32_bf16 v[24:27], a[120:123], a[40:43], v[24:27]\n"
"v_exp_f32 v17, v17\n"
"v_mfma_f32_16x16x32_bf16 v[24:27], a[124:127], a[44:47], v[24:27]\n"
"v_cvt_pk_bf16_f32 v244, v16, v17\n"
"v_mfma_f32_16x16x32_bf16 v[28:31], a[140:143], a[44:47], v[48:51]\n"
"v_exp_f32 v18, v18\n"
"v_mfma_f32_16x16x32_bf16 v[28:31], a[136:139], a[40:43], v[28:31]\n"
"v_mfma_f32_16x16x32_bf16 v[28:31], a[132:135], a[36:39], v[28:31]\n"
"v_exp_f32 v19, v19\n"
"v_mfma_f32_16x16x32_bf16 v[28:31], a[128:131], a[32:35], v[28:31]\n"
"v_cvt_pk_bf16_f32 v245, v18, v19\n"
"v_mfma_f32_16x16x32_bf16 v[64:67], a[208:211], v[32:35], v[64:67]\n"
"v_exp_f32 v20, v20\n"
"v_mfma_f32_16x16x32_bf16 v[112:115], a[144:147], v[32:35], v[112:115]\n"
"buffer_load_dwordx4 v8, s[48:51], s87 offen offset:1024 nt lds\n"
"v_mfma_f32_16x16x32_bf16 v[112:115], a[176:179], v[36:39], v[112:115]\n"
"v_exp_f32 v21, v21\n"
"v_mfma_f32_16x16x32_bf16 v[116:119], a[180:183], v[36:39], v[116:119]\n"
"v_cvt_pk_bf16_f32 v246, v20, v21\n"
"v_mfma_f32_16x16x32_bf16 v[116:119], a[148:151], v[32:35], v[116:119]\n"
"v_exp_f32 v22, v22\n"
"v_mfma_f32_16x16x32_bf16 v[120:123], a[152:155], v[32:35], v[120:123]\n"
"v_mfma_f32_16x16x32_bf16 v[120:123], a[184:187], v[36:39], v[120:123]\n"
"v_exp_f32 v23, v23\n"
"v_mfma_f32_16x16x32_bf16 v[124:127], a[188:191], v[36:39], v[124:127]\n"
"v_cvt_pk_bf16_f32 v247, v22, v23\n"
"v_mfma_f32_16x16x32_bf16 v[124:127], a[156:159], v[32:35], v[124:127]\n"
"v_mfma_f32_16x16x32_bf16 v[16:19], a[80:83], a[48:51], v[52:55]\n"
"v_exp_f32 v24, v24\n"
"v_mfma_f32_16x16x32_bf16 v[16:19], a[84:87], a[52:55], v[16:19]\n"
"v_mfma_f32_16x16x32_bf16 v[16:19], a[88:91], a[56:59], v[16:19]\n"
"v_exp_f32 v25, v25\n"
"v_mfma_f32_16x16x32_bf16 v[16:19], a[92:95], a[60:63], v[16:19]\n"
"v_cvt_pk_bf16_f32 v248, v24, v25\n"
"v_mfma_f32_16x16x32_bf16 v[20:23], a[108:111], a[60:63], v[52:55]\n"
"v_exp_f32 v26, v26\n"
"v_mfma_f32_16x16x32_bf16 v[20:23], a[104:107], a[56:59], v[20:23]\n"
"buffer_load_dwordx4 v8, s[48:51], s86 offen offset:2048 nt lds\n"
"v_mfma_f32_16x16x32_bf16 v[20:23], a[100:103], a[52:55], v[20:23]\n"
"v_exp_f32 v27, v27\n"
"v_mfma_f32_16x16x32_bf16 v[20:23], a[96:99], a[48:51], v[20:23]\n"
"v_cvt_pk_bf16_f32 v249, v26, v27\n"
"v_mfma_f32_16x16x32_bf16 v[64:67], a[208:211], v[36:39], v[64:67]\n"
"v_exp_f32 v28, v28\n"
"v_mfma_f32_16x16x32_bf16 v[128:131], a[192:195], v[36:39], v[128:131]\n"
"v_mfma_f32_16x16x32_bf16 v[128:131], a[160:163], v[32:35], v[128:131]\n"
"v_exp_f32 v29, v29\n"
"v_mfma_f32_16x16x32_bf16 v[132:135], a[164:167], v[32:35], v[132:135]\n"
"v_cvt_pk_bf16_f32 v250, v28, v29\n"
"v_mfma_f32_16x16x32_bf16 v[132:135], a[196:199], v[36:39], v[132:135]\n"
"v_exp_f32 v30, v30\n"
"v_mfma_f32_16x16x32_bf16 v[136:139], a[200:203], v[36:39], v[136:139]\n"
"v_mfma_f32_16x16x32_bf16 v[136:139], a[168:171], v[32:35], v[136:139]\n"
"v_exp_f32 v31, v31\n"
"v_mfma_f32_16x16x32_bf16 v[140:143], a[172:175], v[32:35], v[140:143]\n"
"v_cvt_pk_bf16_f32 v251, v30, v31\n"
"v_mfma_f32_16x16x32_bf16 v[140:143], a[204:207], v[36:39], v[140:143]\n"
"buffer_load_dwordx4 v8, s[48:51], s87 offen offset:3072 nt lds\n"
"v_mfma_f32_16x16x32_bf16 v[24:27], a[112:115], a[48:51], v[52:55]\n"
"s_add_u32 s86, s86, 16384\n"
"s_add_u32 s87, s87, 16384\n"
"v_exp_f32 v16, v16\n"
"v_mfma_f32_16x16x32_bf16 v[24:27], a[116:119], a[52:55], v[24:27]\n"
"v_mfma_f32_16x16x32_bf16 v[24:27], a[120:123], a[56:59], v[24:27]\n"
"v_exp_f32 v17, v17\n"
"v_mfma_f32_16x16x32_bf16 v[24:27], a[124:127], a[60:63], v[24:27]\n"
"v_cvt_pk_bf16_f32 v32, v16, v17\n"
"v_mfma_f32_16x16x32_bf16 v[28:31], a[140:143], a[60:63], v[52:55]\n"
"v_exp_f32 v18, v18\n"
"v_mfma_f32_16x16x32_bf16 v[28:31], a[136:139], a[56:59], v[28:31]\n"
"v_mfma_f32_16x16x32_bf16 v[28:31], a[132:135], a[52:55], v[28:31]\n"
"v_exp_f32 v19, v19\n"
"v_mfma_f32_16x16x32_bf16 v[28:31], a[128:131], a[48:51], v[28:31]\n"
"v_cvt_pk_bf16_f32 v33, v18, v19\n"
"v_mfma_f32_16x16x32_bf16 v[68:71], a[208:211], v[244:247], v[68:71]\n"
"v_exp_f32 v20, v20\n"
"v_mfma_f32_16x16x32_bf16 v[144:147], a[144:147], v[244:247], v[144:147]\n"
"buffer_load_dword v10, off, s[4:7], s9\n"
"v_mfma_f32_16x16x32_bf16 v[144:147], a[176:179], v[248:251], v[144:147]\n"
"s_add_u32 s9, s9, 4096\n"
"v_exp_f32 v21, v21\n"
"v_mfma_f32_16x16x32_bf16 v[148:151], a[180:183], v[248:251], v[148:151]\n"
"v_cvt_pk_bf16_f32 v34, v20, v21\n"
"v_mfma_f32_16x16x32_bf16 v[148:151], a[148:151], v[244:247], v[148:151]\n"
"v_exp_f32 v22, v22\n"
"v_mfma_f32_16x16x32_bf16 v[152:155], a[152:155], v[244:247], v[152:155]\n"
"v_mfma_f32_16x16x32_bf16 v[152:155], a[184:187], v[248:251], v[152:155]\n"
"v_exp_f32 v23, v23\n"
"v_mfma_f32_16x16x32_bf16 v[156:159], a[188:191], v[248:251], v[156:159]\n"
"v_cvt_pk_bf16_f32 v35, v22, v23\n"
"v_mfma_f32_16x16x32_bf16 v[156:159], a[156:159], v[244:247], v[156:159]\n"
"v_mfma_f32_16x16x32_bf16 v[16:19], a[80:83], a[64:67], v[56:59]\n"
"s_waitcnt vmcnt(8) lgkmcnt(0)\n"
"s_barrier\n"
"s_cmp_eq_u32 s88, 1\n"
"s_cbranch_scc0 Lwsm_%=\n"
"buffer_load_dword v12, v14, s[64:67], 0 offen sc1\n"
"Lwsm_%=:\n"
"v_exp_f32 v24, v24\n"
"v_mfma_f32_16x16x32_bf16 v[16:19], a[84:87], a[68:71], v[16:19]\n"
"ds_read_b128 a[80:83], v1 offset:0\n"
"v_mfma_f32_16x16x32_bf16 v[16:19], a[88:91], a[72:75], v[16:19]\n"
"v_exp_f32 v25, v25\n"
"v_mfma_f32_16x16x32_bf16 v[16:19], a[92:95], a[76:79], v[16:19]\n"
"ds_read_b128 a[84:87], v2 offset:0\n"
"v_cvt_pk_bf16_f32 v36, v24, v25\n"
"v_mfma_f32_16x16x32_bf16 v[20:23], a[108:111], a[76:79], v[56:59]\n"
"v_exp_f32 v26, v26\n"
"v_mfma_f32_16x16x32_bf16 v[20:23], a[104:107], a[72:75], v[20:23]\n"
"ds_read_b128 a[88:91], v3 offset:0\n"
"v_mfma_f32_16x16x32_bf16 v[20:23], a[100:103], a[68:71], v[20:23]\n"
"v_exp_f32 v27, v27\n"
"v_mfma_f32_16x16x32_bf16 v[20:23], a[96:99], a[64:67], v[20:23]\n"
"ds_read_b128 a[92:95], v4 offset:0\n"
"v_cvt_pk_bf16_f32 v37, v26, v27\n"
"v_mfma_f32_16x16x32_bf16 v[68:71], a[208:211], v[248:251], v[68:71]\n"
"v_exp_f32 v28, v28\n"
"v_mfma_f32_16x16x32_bf16 v[160:163], a[192:195], v[248:251], v[160:163]\n"
"ds_read_b128 a[108:111], v4 offset:4096\n"
"v_mfma_f32_16x16x32_bf16 v[160:163], a[160:163], v[244:247], v[160:163]\n"
"v_exp_f32 v29, v29\n"
"v_mfma_f32_16x16x32_bf16 v[164:167], a[164:167], v[244:247], v[164:167]\n"
"ds_read_b128 a[104:107], v3 offset:4096\n"
"v_cvt_pk_bf16_f32 v38, v28, v29\n"
"v_mfma_f32_16x16x32_bf16 v[164:167], a[196:199], v[248:251], v[164:167]\n"
"v_exp_f32 v30, v30\n"
"v_mfma_f32_16x16x32_bf16 v[168:171], a[200:203], v[248:251], v[168:171]\n"
"ds_read_b128 a[100:103], v2 offset:4096\n"
"v_mfma_f32_16x16x32_bf16 v[168:171], a[168:171], v[244:247], v[168:171]\n"
"v_exp_f32 v31, v31\n"
"v_mfma_f32_16x16x32_bf16 v[172:175], a[172:175], v[244:247], v[172:175]\n"
"s_add_u32 m0, s83, 32768\n"
"ds_read_b128 a[96:99], v1 offset:4096\n"
"v_cvt_pk_bf16_f32 v39, v30, v31\n"
"v_mfma_f32_16x16x32_bf16 v[172:175], a[204:207], v[248:251], v[172:175]\n"
"buffer_load_dwordx4 v240, s[44:47], s85 offen offset:0 nt lds\n"
"v_mfma_f32_16x16x32_bf16 v[24:27], a[112:115], a[64:67], v[56:59]\n"
"v_exp_f32 v16, v16\n"
"v_mfma_f32_16x16x32_bf16 v[24:27], a[116:119], a[68:71], v[24:27]\n"
"ds_read_b128 a[112:115], v1 offset:8192\n"
"v_mfma_f32_16x16x32_bf16 v[24:27], a[120:123], a[72:75], v[24:27]\n"
"v_exp_f32 v17, v17\n"
"v_mfma_f32_16x16x32_bf16 v[24:27], a[124:127], a[76:79], v[24:27]\n"
"ds_read_b128 a[116:119], v2 offset:8192\n"
"v_cvt_pk_bf16_f32 v244, v16, v17\n"
"v_mfma_f32_16x16x32_bf16 v[28:31], a[140:143], a[76:79], v[56:59]\n"
"v_exp_f32 v18, v18\n"
"v_mfma_f32_16x16x32_bf16 v[28:31], a[136:139], a[72:75], v[28:31]\n"
"ds_read_b128 a[120:123], v3 offset:8192\n"
"v_mfma_f32_16x16x32_bf16 v[28:31], a[132:135], a[68:71], v[28:31]\n"
"v_exp_f32 v19, v19\n"
"v_mfma_f32_16x16x32_bf16 v[28:31], a[128:131], a[64:67], v[28:31]\n"
"ds_read_b128 a[124:127], v4 offset:8192\n"
"v_cvt_pk_bf16_f32 v245, v18, v19\n"
"v_mfma_f32_16x16x32_bf16 v[72:75], a[208:211], v[32:35], v[72:75]\n"
"v_exp_f32 v20, v20\n"
"v_mfma_f32_16x16x32_bf16 v[176:179], a[144:147], v[32:35], v[176:179]\n"
"ds_read_b128 a[140:143], v4 offset:12288\n"
"v_mfma_f32_16x16x32_bf16 v[176:179], a[176:179], v[36:39], v[176:179]\n"
"v_exp_f32 v21, v21\n"
"v_mfma_f32_16x16x32_bf16 v[180:183], a[180:183], v[36:39], v[180:183]\n"
"ds_read_b128 a[136:139], v3 offset:12288\n"
"v_cvt_pk_bf16_f32 v246, v20, v21\n"
"v_mfma_f32_16x16x32_bf16 v[180:183], a[148:151], v[32:35], v[180:183]\n"
"v_exp_f32 v22, v22\n"
"v_mfma_f32_16x16x32_bf16 v[184:187], a[152:155], v[32:35], v[184:187]\n"
"ds_read_b128 a[132:135], v2 offset:12288\n"
"v_mfma_f32_16x16x32_bf16 v[184:187], a[184:187], v[36:39], v[184:187]\n"
"v_exp_f32 v23, v23\n"
"v_mfma_f32_16x16x32_bf16 v[188:191], a[188:191], v[36:39], v[188:191]\n"
"ds_read_b128 a[128:131], v1 offset:12288\n"
"v_cvt_pk_bf16_f32 v247, v22, v23\n"
"v_mfma_f32_16x16x32_bf16 v[188:191], a[156:159], v[32:35], v[188:191]\n"
"s_waitcnt lgkmcnt(8)\n"
"buffer_load_dwordx4 v241, s[44:47], s85 offen offset:1024 nt lds\n"
"v_mfma_f32_16x16x32_bf16 v[16:19], a[80:83], a[0:3], v[40:43]\n"
"v_exp_f32 v24, v24\n"
"v_mfma_f32_16x16x32_bf16 v[16:19], a[84:87], a[4:7], v[16:19]\n"
"v_mfma_f32_16x16x32_bf16 v[16:19], a[88:91], a[8:11], v[16:19]\n"
"v_exp_f32 v25, v25\n"
"v_mfma_f32_16x16x32_bf16 v[16:19], a[92:95], a[12:15], v[16:19]\n"
"v_cvt_pk_bf16_f32 v248, v24, v25\n"
"v_mfma_f32_16x16x32_bf16 v[20:23], a[108:111], a[12:15], v[40:43]\n"
"v_exp_f32 v26, v26\n"
"v_mfma_f32_16x16x32_bf16 v[20:23], a[104:107], a[8:11], v[20:23]\n"
"buffer_load_dwordx4 v242, s[44:47], s85 offen offset:2048 nt lds\n"
"v_mfma_f32_16x16x32_bf16 v[20:23], a[100:103], a[4:7], v[20:23]\n"
"v_exp_f32 v27, v27\n"
"v_mfma_f32_16x16x32_bf16 v[20:23], a[96:99], a[0:3], v[20:23]\n"
"v_cvt_pk_bf16_f32 v249, v26, v27\n"
"v_mfma_f32_16x16x32_bf16 v[72:75], a[208:211], v[36:39], v[72:75]\n"
"v_exp_f32 v28, v28\n"
"v_mfma_f32_16x16x32_bf16 v[192:195], a[192:195], v[36:39], v[192:195]\n"
"v_mfma_f32_16x16x32_bf16 v[192:195], a[160:163], v[32:35], v[192:195]\n"
"v_exp_f32 v29, v29\n"
"v_mfma_f32_16x16x32_bf16 v[196:199], a[164:167], v[32:35], v[196:199]\n"
"v_cvt_pk_bf16_f32 v250, v28, v29\n"
"v_mfma_f32_16x16x32_bf16 v[196:199], a[196:199], v[36:39], v[196:199]\n"
"v_exp_f32 v30, v30\n"
"v_mfma_f32_16x16x32_bf16 v[200:203], a[200:203], v[36:39], v[200:203]\n"
"buffer_load_dwordx4 v243, s[44:47], s85 offen offset:3072 nt lds\n"
"v_mfma_f32_16x16x32_bf16 v[200:203], a[168:171], v[32:35], v[200:203]\n"
"s_add_u32 s85, s85, 16384\n"
"v_exp_f32 v31, v31\n"
"v_mfma_f32_16x16x32_bf16 v[204:207], a[172:175], v[32:35], v[204:207]\n"
"v_cvt_pk_bf16_f32 v251, v30, v31\n"
"v_mfma_f32_16x16x32_bf16 v[204:207], a[204:207], v[36:39], v[204:207]\n"
"s_waitcnt lgkmcnt(0)\n"
"v_mfma_f32_16x16x32_bf16 v[24:27], a[112:115], a[0:3], v[40:43]\n"
"v_exp_f32 v16, v16\n"
"v_mfma_f32_16x16x32_bf16 v[24:27], a[116:119], a[4:7], v[24:27]\n"
"v_mfma_f32_16x16x32_bf16 v[24:27], a[120:123], a[8:11], v[24:27]\n"
"v_exp_f32 v17, v17\n"
"v_mfma_f32_16x16x32_bf16 v[24:27], a[124:127], a[12:15], v[24:27]\n"
"v_cvt_pk_bf16_f32 v32, v16, v17\n"
"v_mfma_f32_16x16x32_bf16 v[28:31], a[140:143], a[12:15], v[40:43]\n"
"v_exp_f32 v18, v18\n"
"v_mfma_f32_16x16x32_bf16 v[28:31], a[136:139], a[8:11], v[28:31]\n"
"v_mfma_f32_16x16x32_bf16 v[28:31], a[132:135], a[4:7], v[28:31]\n"
"v_exp_f32 v19, v19\n"
"v_mfma_f32_16x16x32_bf16 v[28:31], a[128:131], a[0:3], v[28:31]\n"
"v_cvt_pk_bf16_f32 v33, v18, v19\n"
"v_mfma_f32_16x16x32_bf16 v[76:79], a[208:211], v[244:247], v[76:79]\n"
"v_exp_f32 v20, v20\n"
"v_mfma_f32_16x16x32_bf16 v[208:211], a[144:147], v[244:247], v[208:211]\n"
"v_mfma_f32_16x16x32_bf16 v[208:211], a[176:179], v[248:251], v[208:211]\n"
"v_exp_f32 v21, v21\n"
"v_mfma_f32_16x16x32_bf16 v[212:215], a[180:183], v[248:251], v[212:215]\n"
"ds_read_b64_tr_b16 a[144:145], v5 offset:0\n"
"v_cvt_pk_bf16_f32 v34, v20, v21\n"
"v_mfma_f32_16x16x32_bf16 v[212:215], a[148:151], v[244:247], v[212:215]\n"
"v_exp_f32 v22, v22\n"
"v_mfma_f32_16x16x32_bf16 v[216:219], a[152:155], v[244:247], v[216:219]\n"
"ds_read_b64_tr_b16 a[146:147], v5 offset:4096\n"
"v_mfma_f32_16x16x32_bf16 v[216:219], a[184:187], v[248:251], v[216:219]\n"
"v_exp_f32 v23, v23\n"
"v_mfma_f32_16x16x32_bf16 v[220:223], a[188:191], v[248:251], v[220:223]\n"
"ds_read_b64_tr_b16 a[176:177], v5 offset:8192\n"
"v_cvt_pk_bf16_f32 v35, v22, v23\n"
"v_mfma_f32_16x16x32_bf16 v[220:223], a[156:159], v[244:247], v[220:223]\n"
"ds_read_b64_tr_b16 a[178:179], v5 offset:12288\n"
"v_mfma_f32_16x16x32_bf16 v[16:19], a[80:83], a[16:19], v[44:47]\n"
"v_exp_f32 v24, v24\n"
"v_mfma_f32_16x16x32_bf16 v[16:19], a[84:87], a[20:23], v[16:19]\n"
"ds_read_b64_tr_b16 a[148:149], v6 offset:0\n"
"v_mfma_f32_16x16x32_bf16 v[16:19], a[88:91], a[24:27], v[16:19]\n"
"ds_read_b64_tr_b16 a[150:151], v6 offset:4096\n"
"v_exp_f32 v25, v25\n"
"v_mfma_f32_16x16x32_bf16 v[16:19], a[92:95], a[28:31], v[16:19]\n"
"ds_read_b64_tr_b16 a[180:181], v6 offset:8192\n"
"v_cvt_pk_bf16_f32 v36, v24, v25\n"
"v_mfma_f32_16x16x32_bf16 v[20:23], a[108:111], a[28:31], v[44:47]\n"
"v_exp_f32 v26, v26\n"
"v_mfma_f32_16x16x32_bf16 v[20:23], a[104:107], a[24:27], v[20:23]\n"
"ds_read_b64_tr_b16 a[182:183], v6 offset:12288\n"
"v_mfma_f32_16x16x32_bf16 v[20:23], a[100:103], a[20:23], v[20:23]\n"
"v_exp_f32 v27, v27\n"
"v_mfma_f32_16x16x32_bf16 v[20:23], a[96:99], a[16:19], v[20:23]\n"
"ds_read_b64_tr_b16 a[152:153], v5 offset:512\n"
"v_cvt_pk_bf16_f32 v37, v26, v27\n"
"v_mfma_f32_16x16x32_bf16 v[76:79], a[208:211], v[248:251], v[76:79]\n"
"ds_read_b64_tr_b16 a[154:155], v5 offset:4608\n"
"v_exp_f32 v28, v28\n"
"v_mfma_f32_16x16x32_bf16 v[224:227], a[192:195], v[248:251], v[224:227]\n"
"ds_read_b64_tr_b16 a[184:185], v5 offset:8704\n"
"v_mfma_f32_16x16x32_bf16 v[224:227], a[160:163], v[244:247], v[224:227]\n"
"v_exp_f32 v29, v29\n"
"v_mfma_f32_16x16x32_bf16 v[228:231], a[164:167], v[244:247], v[228:231]\n"
"ds_read_b64_tr_b16 a[186:187], v5 offset:12800\n"
"v_cvt_pk_bf16_f32 v38, v28, v29\n"
"v_mfma_f32_16x16x32_bf16 v[228:231], a[196:199], v[248:251], v[228:231]\n"
"v_exp_f32 v30, v30\n"
"v_mfma_f32_16x16x32_bf16 v[232:235], a[200:203], v[248:251], v[232:235]\n"
"ds_read_b64_tr_b16 a[156:157], v6 offset:512\n"
"v_mfma_f32_16x16x32_bf16 v[232:235], a[168:171], v[244:247], v[232:235]\n"
"ds_read_b64_tr_b16 a[158:159], v6 offset:4608\n"
"v_exp_f32 v31, v31\n"
"v_mfma_f32_16x16x32_bf16 v[236:239], a[172:175], v[244:247], v[236:239]\n"
"ds_read_b64_tr_b16 a[188:189], v6 offset:8704\n"
"v_cvt_pk_bf16_f32 v39, v30, v31\n"
"v_mfma_f32_16x16x32_bf16 v[236:239], a[204:207], v[248:251], v[236:239]\n"
"ds_read_b64_tr_b16 a[190:191], v6 offset:12800\n"
"v_mfma_f32_16x16x32_bf16 v[24:27], a[112:115], a[16:19], v[44:47]\n"
"v_exp_f32 v16, v16\n"
"v_mfma_f32_16x16x32_bf16 v[24:27], a[116:119], a[20:23], v[24:27]\n"
"ds_read_b64_tr_b16 a[160:161], v5 offset:1024\n"
"v_mfma_f32_16x16x32_bf16 v[24:27], a[120:123], a[24:27], v[24:27]\n"
"ds_read_b64_tr_b16 a[162:163], v5 offset:5120\n"
"v_exp_f32 v17, v17\n"
"v_mfma_f32_16x16x32_bf16 v[24:27], a[124:127], a[28:31], v[24:27]\n"
"ds_read_b64_tr_b16 a[192:193], v5 offset:9216\n"
"v_cvt_pk_bf16_f32 v244, v16, v17\n"
"v_mfma_f32_16x16x32_bf16 v[28:31], a[140:143], a[28:31], v[44:47]\n"
"v_exp_f32 v18, v18\n"
"v_mfma_f32_16x16x32_bf16 v[28:31], a[136:139], a[24:27], v[28:31]\n"
"ds_read_b64_tr_b16 a[194:195], v5 offset:13312\n"
"v_mfma_f32_16x16x32_bf16 v[28:31], a[132:135], a[20:23], v[28:31]\n"
"v_exp_f32 v19, v19\n"
"v_mfma_f32_16x16x32_bf16 v[28:31], a[128:131], a[16:19], v[28:31]\n"
"ds_read_b64_tr_b16 a[164:165], v6 offset:1024\n"
"v_cvt_pk_bf16_f32 v245, v18, v19\n"
"v_mfma_f32_16x16x32_bf16 v[60:63], a[208:211], v[32:35], v[60:63]\n"
"s_waitcnt lgkmcnt(15)\n"
"ds_read_b64_tr_b16 a[166:167], v6 offset:5120\n"
"v_exp_f32 v20, v20\n"
"v_mfma_f32_16x16x32_bf16 v[80:83], a[144:147], v[32:35], v[80:83]\n"
"ds_read_b64_tr_b16 a[196:197], v6 offset:9216\n"
"v_mfma_f32_16x16x32_bf16 v[80:83], a[176:179], v[36:39], v[80:83]\n"
"s_waitcnt lgkmcnt(15)\n"
"v_exp_f32 v21, v21\n"
"v_mfma_f32_16x16x32_bf16 v[84:87], a[180:183], v[36:39], v[84:87]\n"
"ds_read_b64_tr_b16 a[198:199], v6 offset:13312\n"
"v_cvt_pk_bf16_f32 v246, v20, v21\n"
"v_mfma_f32_16x16x32_bf16 v[84:87], a[148:151], v[32:35], v[84:87]\n"
"s_waitcnt lgkmcnt(12)\n"
"v_exp_f32 v22, v22\n"
"v_mfma_f32_16x16x32_bf16 v[88:91], a[152:155], v[32:35], v[88:91]\n"
"ds_read_b64_tr_b16 a[168:169], v5 offset:1536\n"
"v_mfma_f32_16x16x32_bf16 v[88:91], a[184:187], v[36:39], v[88:91]\n"
"s_waitcnt lgkmcnt(9)\n"
"v_exp_f32 v23, v23\n"
"v_mfma_f32_16x16x32_bf16 v[92:95], a[188:191], v[36:39], v[92:95]\n"
"ds_read_b64_tr_b16 a[170:171], v5 offset:5632\n"
"v_cvt_pk_bf16_f32 v247, v22, v23\n"
"v_mfma_f32_16x16x32_bf16 v[92:95], a[156:159], v[32:35], v[92:95]\n"
"ds_read_b64_tr_b16 a[200:201], v5 offset:9728\n"
"v_mfma_f32_16x16x32_bf16 v[16:19], a[80:83], a[32:35], v[48:51]\n"
"v_exp_f32 v24, v24\n"
"v_mfma_f32_16x16x32_bf16 v[16:19], a[84:87], a[36:39], v[16:19]\n"
"ds_read_b64_tr_b16 a[202:203], v5 offset:13824\n"
"v_mfma_f32_16x16x32_bf16 v[16:19], a[88:91], a[40:43], v[16:19]\n"
"v_exp_f32 v25, v25\n"
"v_mfma_f32_16x16x32_bf16 v[16:19], a[92:95], a[44:47], v[16:19]\n"
"ds_read_b64_tr_b16 a[172:173], v6 offset:1536\n"
"v_cvt_pk_bf16_f32 v248, v24, v25\n"
"v_mfma_f32_16x16x32_bf16 v[20:23], a[108:111], a[44:47], v[48:51]\n"
"v_exp_f32 v26, v26\n"
"v_mfma_f32_16x16x32_bf16 v[20:23], a[104:107], a[40:43], v[20:23]\n"
"ds_read_b64_tr_b16 a[174:175], v6 offset:5632\n"
"v_mfma_f32_16x16x32_bf16 v[20:23], a[100:103], a[36:39], v[20:23]\n"
"v_exp_f32 v27, v27\n"
"v_mfma_f32_16x16x32_bf16 v[20:23], a[96:99], a[32:35], v[20:23]\n"
"ds_read_b64_tr_b16 a[204:205], v6 offset:9728\n"
"v_cvt_pk_bf16_f32 v249, v26, v27\n"
"v_mfma_f32_16x16x32_bf16 v[60:63], a[208:211], v[36:39], v[60:63]\n"
"s_waitcnt lgkmcnt(11)\n"
"v_exp_f32 v28, v28\n"
"v_mfma_f32_16x16x32_bf16 v[96:99], a[192:195], v[36:39], v[96:99]\n"
"ds_read_b64_tr_b16 a[206:207], v6 offset:13824\n"
"v_mfma_f32_16x16x32_bf16 v[96:99], a[160:163], v[32:35], v[96:99]\n"
"s_waitcnt lgkmcnt(8)\n"
"v_exp_f32 v29, v29\n"
"v_mfma_f32_16x16x32_bf16 v[100:103], a[164:167], v[32:35], v[100:103]\n"
"v_cvt_pk_bf16_f32 v250, v28, v29\n"
"v_mfma_f32_16x16x32_bf16 v[100:103], a[196:199], v[36:39], v[100:103]\n"
"s_waitcnt lgkmcnt(4)\n"
"s_add_u32 m0, s84, 32768\n"
"v_exp_f32 v30, v30\n"
"v_mfma_f32_16x16x32_bf16 v[104:107], a[200:203], v[36:39], v[104:107]\n"
"buffer_load_dwordx4 v8, s[48:51], s86 offen offset:0 nt lds\n"
"v_mfma_f32_16x16x32_bf16 v[104:107], a[168:171], v[32:35], v[104:107]\n"
"s_waitcnt lgkmcnt(0)\n"
"v_exp_f32 v31, v31\n"
"v_mfma_f32_16x16x32_bf16 v[108:111], a[172:175], v[32:35], v[108:111]\n"
"v_cvt_pk_bf16_f32 v251, v30, v31\n"
"v_mfma_f32_16x16x32_bf16 v[108:111], a[204:207], v[36:39], v[108:111]\n"
"v_mfma_f32_16x16x32_bf16 v[24:27], a[112:115], a[32:35], v[48:51]\n"
"v_exp_f32 v16, v16\n"
"v_mfma_f32_16x16x32_bf16 v[24:27], a[116:119], a[36:39], v[24:27]\n"
"v_mfma_f32_16x16x32_bf16 v[24:27], a[120:123], a[40:43], v[24:27]\n"
"v_exp_f32 v17, v17\n"
"v_mfma_f32_16x16x32_bf16 v[24:27], a[124:127], a[44:47], v[24:27]\n"
"v_cvt_pk_bf16_f32 v32, v16, v17\n"
"v_mfma_f32_16x16x32_bf16 v[28:31], a[140:143], a[44:47], v[48:51]\n"
"v_exp_f32 v18, v18\n"
"v_mfma_f32_16x16x32_bf16 v[28:31], a[136:139], a[40:43], v[28:31]\n"
"v_mfma_f32_16x16x32_bf16 v[28:31], a[132:135], a[36:39], v[28:31]\n"
"v_exp_f32 v19, v19\n"
"v_mfma_f32_16x16x32_bf16 v[28:31], a[128:131], a[32:35], v[28:31]\n"
"v_cvt_pk_bf16_f32 v33, v18, v19\n"
"v_mfma_f32_16x16x32_bf16 v[64:67], a[208:211], v[244:247], v[64:67]\n"
"v_exp_f32 v20, v20\n"
"v_mfma_f32_16x16x32_bf16 v[112:115], a[144:147], v[244:247], v[112:115]\n"
"buffer_load_dwordx4 v8, s[48:51], s87 offen offset:1024 nt lds\n"
"v_mfma_f32_16x16x32_bf16 v[112:115], a[176:179], v[248:251], v[112:115]\n"
"v_exp_f32 v21, v21\n"
"v_mfma_f32_16x16x32_bf16 v[116:119], a[180:183], v[248:251], v[116:119]\n"
"v_cvt_pk_bf16_f32 v34, v20, v21\n"
"v_mfma_f32_16x16x32_bf16 v[116:119], a[148:151], v[244:247], v[116:119]\n"
"v_exp_f32 v22, v22\n"
"v_mfma_f32_16x16x32_bf16 v[120:123], a[152:155], v[244:247], v[120:123]\n"
"v_mfma_f32_16x16x32_bf16 v[120:123], a[184:187], v[248:251], v[120:123]\n"
"v_exp_f32 v23, v23\n"
"v_mfma_f32_16x16x32_bf16 v[124:127], a[188:191], v[248:251], v[124:127]\n"
"v_cvt_pk_bf16_f32 v35, v22, v23\n"
"v_mfma_f32_16x16x32_bf16 v[124:127], a[156:159], v[244:247], v[124:127]\n"
"v_mfma_f32_16x16x32_bf16 v[16:19], a[80:83], a[48:51], v[52:55]\n"
"v_exp_f32 v24, v24\n"
"v_mfma_f32_16x16x32_bf16 v[16:19], a[84:87], a[52:55], v[16:19]\n"
"v_mfma_f32_16x16x32_bf16 v[16:19], a[88:91], a[56:59], v[16:19]\n"
"v_exp_f32 v25, v25\n"
"v_mfma_f32_16x16x32_bf16 v[16:19], a[92:95], a[60:63], v[16:19]\n"
"v_cvt_pk_bf16_f32 v36, v24, v25\n"
"v_mfma_f32_16x16x32_bf16 v[20:23], a[108:111], a[60:63], v[52:55]\n"
"v_exp_f32 v26, v26\n"
"v_mfma_f32_16x16x32_bf16 v[20:23], a[104:107], a[56:59], v[20:23]\n"
"buffer_load_dwordx4 v8, s[48:51], s86 offen offset:2048 nt lds\n"
"v_mfma_f32_16x16x32_bf16 v[20:23], a[100:103], a[52:55], v[20:23]\n"
"v_exp_f32 v27, v27\n"
"v_mfma_f32_16x16x32_bf16 v[20:23], a[96:99], a[48:51], v[20:23]\n"
"v_cvt_pk_bf16_f32 v37, v26, v27\n"
"v_mfma_f32_16x16x32_bf16 v[64:67], a[208:211], v[248:251], v[64:67]\n"
"v_exp_f32 v28, v28\n"
"v_mfma_f32_16x16x32_bf16 v[128:131], a[192:195], v[248:251], v[128:131]\n"
"v_mfma_f32_16x16x32_bf16 v[128:131], a[160:163], v[244:247], v[128:131]\n"
"v_exp_f32 v29, v29\n"
"v_mfma_f32_16x16x32_bf16 v[132:135], a[164:167], v[244:247], v[132:135]\n"
"v_cvt_pk_bf16_f32 v38, v28, v29\n"
"v_mfma_f32_16x16x32_bf16 v[132:135], a[196:199], v[248:251], v[132:135]\n"
"v_exp_f32 v30, v30\n"
"v_mfma_f32_16x16x32_bf16 v[136:139], a[200:203], v[248:251], v[136:139]\n"
"v_mfma_f32_16x16x32_bf16 v[136:139], a[168:171], v[244:247], v[136:139]\n"
"v_exp_f32 v31, v31\n"
"v_mfma_f32_16x16x32_bf16 v[140:143], a[172:175], v[244:247], v[140:143]\n"
"v_cvt_pk_bf16_f32 v39, v30, v31\n"
"v_mfma_f32_16x16x32_bf16 v[140:143], a[204:207], v[248:251], v[140:143]\n"
"buffer_load_dwordx4 v8, s[48:51], s87 offen offset:3072 nt lds\n"
"v_mfma_f32_16x16x32_bf16 v[24:27], a[112:115], a[48:51], v[52:55]\n"
"s_add_u32 s86, s86, 16384\n"
"s_add_u32 s87, s87, 16384\n"
"v_exp_f32 v16, v16\n"
"v_mfma_f32_16x16x32_bf16 v[24:27], a[116:119], a[52:55], v[24:27]\n"
"v_mfma_f32_16x16x32_bf16 v[24:27], a[120:123], a[56:59], v[24:27]\n"
"v_exp_f32 v17, v17\n"
"v_mfma_f32_16x16x32_bf16 v[24:27], a[124:127], a[60:63], v[24:27]\n"
"v_cvt_pk_bf16_f32 v244, v16, v17\n"
"v_mfma_f32_16x16x32_bf16 v[28:31], a[140:143], a[60:63], v[52:55]\n"
"v_exp_f32 v18, v18\n"
"v_mfma_f32_16x16x32_bf16 v[28:31], a[136:139], a[56:59], v[28:31]\n"
"v_mfma_f32_16x16x32_bf16 v[28:31], a[132:135], a[52:55], v[28:31]\n"
"v_exp_f32 v19, v19\n"
"v_mfma_f32_16x16x32_bf16 v[28:31], a[128:131], a[48:51], v[28:31]\n"
"v_cvt_pk_bf16_f32 v245, v18, v19\n"
"v_mfma_f32_16x16x32_bf16 v[68:71], a[208:211], v[32:35], v[68:71]\n"
"v_exp_f32 v20, v20\n"
"v_mfma_f32_16x16x32_bf16 v[144:147], a[144:147], v[32:35], v[144:147]\n"
"buffer_load_dword v10, off, s[4:7], s9\n"
"v_mfma_f32_16x16x32_bf16 v[144:147], a[176:179], v[36:39], v[144:147]\n"
"s_add_u32 s9, s9, 4096\n"
"v_exp_f32 v21, v21\n"
"v_mfma_f32_16x16x32_bf16 v[148:151], a[180:183], v[36:39], v[148:151]\n"
"v_cvt_pk_bf16_f32 v246, v20, v21\n"
"v_mfma_f32_16x16x32_bf16 v[148:151], a[148:151], v[32:35], v[148:151]\n"
"v_exp_f32 v22, v22\n"
"v_mfma_f32_16x16x32_bf16 v[152:155], a[152:155], v[32:35], v[152:155]\n"
"v_mfma_f32_16x16x32_bf16 v[152:155], a[184:187], v[36:39], v[152:155]\n"
"v_exp_f32 v23, v23\n"
"v_mfma_f32_16x16x32_bf16 v[156:159], a[188:191], v[36:39], v[156:159]\n"
"v_cvt_pk_bf16_f32 v247, v22, v23\n"
"v_mfma_f32_16x16x32_bf16 v[156:159], a[156:159], v[32:35], v[156:159]\n"
"s_sub_u32 s88, s88, 1\n"
"s_cmp_lg_u32 s88, 0\n"
"s_cbranch_scc1 Lloop_%=\n"
"s_mov_b64 s[56:57], s[22:23]\n"
"s_add_u32 s56, s56, s29\n"
"s_addc_u32 s57, s57, 0\n"
"s_and_b32 s57, s57, 0xffff\n"
"s_mov_b32 s58, 98304\n"
"s_mov_b32 s59, 0x20000\n"
"s_cmp_lt_u32 s24, 30\n"
"s_cselect_b32 s58, s58, 0\n"
"s_lshl_b32 s9, s24, 2\n"
"s_add_u32 s9, s9, %[wid]\n"
"s_and_b32 s9, s9, 31\n"
"s_mul_i32 s9, s9, 768\n"
"s_add_u32 s9, s9, Lfbdone_%=-Lpc_%=\n"
"v_min_u32 v252, 5, v0\n"
"v_lshlrev_b32 v252, 7, v252\n"
"v_mfma_f32_16x16x32_bf16 v[16:19], a[80:83], a[64:67], v[56:59]\n"
"s_waitcnt vmcnt(8) lgkmcnt(0)\n"
"s_barrier\n"
"v_cmp_ne_u32 vcc, 1, v12\n"
"s_cbranch_vccnz Lwslow_%=\n"
"Lwok_%=:\n"
"v_exp_f32 v24, v24\n"
"v_mfma_f32_16x16x32_bf16 v[16:19], a[84:87], a[68:71], v[16:19]\n"
"ds_read_b128 a[80:83], v1 offset:16384\n"
"v_mfma_f32_16x16x32_bf16 v[16:19], a[88:91], a[72:75], v[16:19]\n"
"v_exp_f32 v25, v25\n"
"v_mfma_f32_16x16x32_bf16 v[16:19], a[92:95], a[76:79], v[16:19]\n"
"ds_read_b128 a[84:87], v2 offset:16384\n"
"v_cvt_pk_bf16_f32 v248, v24, v25\n"
"v_mfma_f32_16x16x32_bf16 v[20:23], a[108:111], a[76:79], v[56:59]\n"
"v_exp_f32 v26, v26\n"
"v_mfma_f32_16x16x32_bf16 v[20:23], a[104:107], a[72:75], v[20:23]\n"
"ds_read_b128 a[88:91], v3 offset:16384\n"
"v_mfma_f32_16x16x32_bf16 v[20:23], a[100:103], a[68:71], v[20:23]\n"
"v_exp_f32 v27, v27\n"
"v_mfma_f32_16x16x32_bf16 v[20:23], a[96:99], a[64:67], v[20:23]\n"
"ds_read_b128 a[92:95], v4 offset:16384\n"
"v_cvt_pk_bf16_f32 v249, v26, v27\n"
"v_mfma_f32_16x16x32_bf16 v[68:71], a[208:211], v[36:39], v[68:71]\n"
"v_exp_f32 v28, v28\n"
"v_mfma_f32_16x16x32_bf16 v[160:163], a[192:195], v[36:39], v[160:163]\n"
"ds_read_b128 a[108:111], v4 offset:20480\n"
"v_mfma_f32_16x16x32_bf16 v[160:163], a[160:163], v[32:35], v[160:163]\n"
"v_exp_f32 v29, v29\n"
"v_mfma_f32_16x16x32_bf16 v[164:167], a[164:167], v[32:35], v[164:167]\n"
"ds_read_b128 a[104:107], v3 offset:20480\n"
"v_cvt_pk_bf16_f32 v250, v28, v29\n"
"v_mfma_f32_16x16x32_bf16 v[164:167], a[196:199], v[36:39], v[164:167]\n"
"v_exp_f32 v30, v30\n"
"v_mfma_f32_16x16x32_bf16 v[168:171], a[200:203], v[36:39], v[168:171]\n"
"ds_read_b128 a[100:103], v2 offset:20480\n"
"v_mfma_f32_16x16x32_bf16 v[168:171], a[168:171], v[32:35], v[168:171]\n"
"v_exp_f32 v31, v31\n"
"v_mfma_f32_16x16x32_bf16 v[172:175], a[172:175], v[32:35], v[172:175]\n"
"s_add_u32 m0, s83, 49152\n"
"ds_read_b128 a[96:99], v1 offset:20480\n"
"v_cvt_pk_bf16_f32 v251, v30, v31\n"
"v_mfma_f32_16x16x32_bf16 v[172:175], a[204:207], v[36:39], v[172:175]\n"
"buffer_load_dwordx4 v240, s[44:47], s85 offen offset:0 nt lds\n"
"v_mfma_f32_16x16x32_bf16 v[24:27], a[112:115], a[64:67], v[56:59]\n"
"v_exp_f32 v16, v16\n"
"v_mfma_f32_16x16x32_bf16 v[24:27], a[116:119], a[68:71], v[24:27]\n"
"ds_read_b128 a[112:115], v1 offset:24576\n"
"v_mfma_f32_16x16x32_bf16 v[24:27], a[120:123], a[72:75], v[24:27]\n"
"v_exp_f32 v17, v17\n"
"v_mfma_f32_16x16x32_bf16 v[24:27], a[124:127], a[76:79], v[24:27]\n"
"ds_read_b128 a[116:119], v2 offset:24576\n"
"v_cvt_pk_bf16_f32 v32, v16, v17\n"
"v_mfma_f32_16x16x32_bf16 v[28:31], a[140:143], a[76:79], v[56:59]\n"
"v_exp_f32 v18, v18\n"
"v_mfma_f32_16x16x32_bf16 v[28:31], a[136:139], a[72:75], v[28:31]\n"
"ds_read_b128 a[120:123], v3 offset:24576\n"
"v_mfma_f32_16x16x32_bf16 v[28:31], a[132:135], a[68:71], v[28:31]\n"
"v_exp_f32 v19, v19\n"
"v_mfma_f32_16x16x32_bf16 v[28:31], a[128:131], a[64:67], v[28:31]\n"
"ds_read_b128 a[124:127], v4 offset:24576\n"
"v_cvt_pk_bf16_f32 v33, v18, v19\n"
"v_mfma_f32_16x16x32_bf16 v[72:75], a[208:211], v[244:247], v[72:75]\n"
"v_exp_f32 v20, v20\n"
"v_mfma_f32_16x16x32_bf16 v[176:179], a[144:147], v[244:247], v[176:179]\n"
"ds_read_b128 a[140:143], v4 offset:28672\n"
"v_mfma_f32_16x16x32_bf16 v[176:179], a[176:179], v[248:251], v[176:179]\n"
"v_exp_f32 v21, v21\n"
"v_mfma_f32_16x16x32_bf16 v[180:183], a[180:183], v[248:251], v[180:183]\n"
"ds_read_b128 a[136:139], v3 offset:28672\n"
"v_cvt_pk_bf16_f32 v34, v20, v21\n"
"v_mfma_f32_16x16x32_bf16 v[180:183], a[148:151], v[244:247], v[180:183]\n"
"v_exp_f32 v22, v22\n"
"v_mfma_f32_16x16x32_bf16 v[184:187], a[152:155], v[244:247], v[184:187]\n"
"ds_read_b128 a[132:135], v2 offset:28672\n"
"v_mfma_f32_16x16x32_bf16 v[184:187], a[184:187], v[248:251], v[184:187]\n"
"v_exp_f32 v23, v23\n"
"v_mfma_f32_16x16x32_bf16 v[188:191], a[188:191], v[248:251], v[188:191]\n"
"ds_read_b128 a[128:131], v1 offset:28672\n"
"v_cvt_pk_bf16_f32 v35, v22, v23\n"
"v_mfma_f32_16x16x32_bf16 v[188:191], a[156:159], v[244:247], v[188:191]\n"
"s_waitcnt lgkmcnt(8)\n"
"buffer_load_dwordx4 v241, s[44:47], s85 offen offset:1024 nt lds\n"
"v_mfma_f32_16x16x32_bf16 v[16:19], a[80:83], a[0:3], v[40:43]\n"
"v_exp_f32 v24, v24\n"
"v_mfma_f32_16x16x32_bf16 v[16:19], a[84:87], a[4:7], v[16:19]\n"
"v_mfma_f32_16x16x32_bf16 v[16:19], a[88:91], a[8:11], v[16:19]\n"
"v_exp_f32 v25, v25\n"
"v_mfma_f32_16x16x32_bf16 v[16:19], a[92:95], a[12:15], v[16:19]\n"
"v_cvt_pk_bf16_f32 v36, v24, v25\n"
"v_mfma_f32_16x16x32_bf16 v[20:23], a[108:111], a[12:15], v[40:43]\n"
"v_exp_f32 v26, v26\n"
"v_mfma_f32_16x16x32_bf16 v[20:23], a[104:107], a[8:11], v[20:23]\n"
"buffer_load_dwordx4 v242, s[44:47], s85 offen offset:2048 nt lds\n"
"v_mfma_f32_16x16x32_bf16 v[20:23], a[100:103], a[4:7], v[20:23]\n"
"v_exp_f32 v27, v27\n"
"v_mfma_f32_16x16x32_bf16 v[20:23], a[96:99], a[0:3], v[20:23]\n"
"v_cvt_pk_bf16_f32 v37, v26, v27\n"
"v_mfma_f32_16x16x32_bf16 v[72:75], a[208:211], v[248:251], v[72:75]\n"
"v_exp_f32 v28, v28\n"
"v_mfma_f32_16x16x32_bf16 v[192:195], a[192:195], v[248:251], v[192:195]\n"
"buffer_load_dwordx4 v243, s[44:47], s85 offen offset:3072 nt lds\n"
"v_mfma_f32_16x16x32_bf16 v[192:195], a[160:163], v[244:247], v[192:195]\n"
"s_add_u32 s85, s85, 16384\n"
"v_exp_f32 v29, v29\n"
"v_mfma_f32_16x16x32_bf16 v[196:199], a[164:167], v[244:247], v[196:199]\n"
"v_cvt_pk_bf16_f32 v38, v28, v29\n"
"v_mfma_f32_16x16x32_bf16 v[196:199], a[196:199], v[248:251], v[196:199]\n"
"s_add_u32 m0, s84, 49152\n"
"v_exp_f32 v30, v30\n"
"v_mfma_f32_16x16x32_bf16 v[200:203], a[200:203], v[248:251], v[200:203]\n"
"buffer_load_dwordx4 v8, s[48:51], s86 offen offset:0 nt lds\n"
"v_mfma_f32_16x16x32_bf16 v[200:203], a[168:171], v[244:247], v[200:203]\n"
"v_exp_f32 v31, v31\n"
"v_mfma_f32_16x16x32_bf16 v[204:207], a[172:175], v[244:247], v[204:207]\n"
"v_cvt_pk_bf16_f32 v39, v30, v31\n"
"v_mfma_f32_16x16x32_bf16 v[204:207], a[204:207], v[248:251], v[204:207]\n"
"s_waitcnt lgkmcnt(0)\n"
"buffer_load_dwordx4 v8, s[48:51], s87 offen offset:1024 nt lds\n"
"v_mfma_f32_16x16x32_bf16 v[24:27], a[112:115], a[0:3], v[40:43]\n"
"v_exp_f32 v16, v16\n"
"v_mfma_f32_16x16x32_bf16 v[24:27], a[116:119], a[4:7], v[24:27]\n"
"buffer_load_dwordx4 v8, s[48:51], s86 offen offset:2048 nt lds\n"
"v_mfma_f32_16x16x32_bf16 v[24:27], a[120:123], a[8:11], v[24:27]\n"
"v_exp_f32 v17, v17\n"
"v_mfma_f32_16x16x32_bf16 v[24:27], a[124:127], a[12:15], v[24:27]\n"
"v_cvt_pk_bf16_f32 v244, v16, v17\n"
"v_mfma_f32_16x16x32_bf16 v[28:31], a[140:143], a[12:15], v[40:43]\n"
"v_exp_f32 v18, v18\n"
"v_mfma_f32_16x16x32_bf16 v[28:31], a[136:139], a[8:11], v[28:31]\n"
"buffer_load_dwordx4 v8, s[48:51], s87 offen offset:3072 nt lds\n"
"v_mfma_f32_16x16x32_bf16 v[28:31], a[132:135], a[4:7], v[28:31]\n"
"s_add_u32 s86, s86, 16384\n"
"s_add_u32 s87, s87, 16384\n"
"v_exp_f32 v19, v19\n"
"v_mfma_f32_16x16x32_bf16 v[28:31], a[128:131], a[0:3], v[28:31]\n"
"v_cvt_pk_bf16_f32 v245, v18, v19\n"
"v_mfma_f32_16x16x32_bf16 v[76:79], a[208:211], v[32:35], v[76:79]\n"
"v_exp_f32 v20, v20\n"
"v_mfma_f32_16x16x32_bf16 v[208:211], a[144:147], v[32:35], v[208:211]\n"
"v_mfma_f32_16x16x32_bf16 v[208:211], a[176:179], v[36:39], v[208:211]\n"
"v_exp_f32 v21, v21\n"
"v_mfma_f32_16x16x32_bf16 v[212:215], a[180:183], v[36:39], v[212:215]\n"
"ds_read_b64_tr_b16 a[144:145], v5 offset:16384\n"
"v_cvt_pk_bf16_f32 v246, v20, v21\n"
"v_mfma_f32_16x16x32_bf16 v[212:215], a[148:151], v[32:35], v[212:215]\n"
"v_exp_f32 v22, v22\n"
"v_mfma_f32_16x16x32_bf16 v[216:219], a[152:155], v[32:35], v[216:219]\n"
"ds_read_b64_tr_b16 a[146:147], v5 offset:20480\n"
"v_mfma_f32_16x16x32_bf16 v[216:219], a[184:187], v[36:39], v[216:219]\n"
"v_exp_f32 v23, v23\n"
"v_mfma_f32_16x16x32_bf16 v[220:223], a[188:191], v[36:39], v[220:223]\n"
"ds_read_b64_tr_b16 a[176:177], v5 offset:24576\n"
"v_cvt_pk_bf16_f32 v247, v22, v23\n"
"v_mfma_f32_16x16x32_bf16 v[220:223], a[156:159], v[32:35], v[220:223]\n"
"ds_read_b64_tr_b16 a[178:179], v5 offset:28672\n"
"v_mfma_f32_16x16x32_bf16 v[16:19], a[80:83], a[16:19], v[44:47]\n"
"v_exp_f32 v24, v24\n"
"v_mfma_f32_16x16x32_bf16 v[16:19], a[84:87], a[20:23], v[16:19]\n"
"ds_read_b64_tr_b16 a[148:149], v6 offset:16384\n"
"v_mfma_f32_16x16x32_bf16 v[16:19], a[88:91], a[24:27], v[16:19]\n"
"ds_read_b64_tr_b16 a[150:151], v6 offset:20480\n"
"v_exp_f32 v25, v25\n"
"v_mfma_f32_16x16x32_bf16 v[16:19], a[92:95], a[28:31], v[16:19]\n"
"ds_read_b64_tr_b16 a[180:181], v6 offset:24576\n"
"v_cvt_pk_bf16_f32 v248, v24, v25\n"
"v_mfma_f32_16x16x32_bf16 v[20:23], a[108:111], a[28:31], v[44:47]\n"
"v_exp_f32 v26, v26\n"
"v_mfma_f32_16x16x32_bf16 v[20:23], a[104:107], a[24:27], v[20:23]\n"
"ds_read_b64_tr_b16 a[182:183], v6 offset:28672\n"
"v_mfma_f32_16x16x32_bf16 v[20:23], a[100:103], a[20:23], v[20:23]\n"
"v_exp_f32 v27, v27\n"
"v_mfma_f32_16x16x32_bf16 v[20:23], a[96:99], a[16:19], v[20:23]\n"
"ds_read_b64_tr_b16 a[152:153], v5 offset:16896\n"
"v_cvt_pk_bf16_f32 v249, v26, v27\n"
"v_mfma_f32_16x16x32_bf16 v[76:79], a[208:211], v[36:39], v[76:79]\n"
"ds_read_b64_tr_b16 a[154:155], v5 offset:20992\n"
"v_exp_f32 v28, v28\n"
"v_mfma_f32_16x16x32_bf16 v[224:227], a[192:195], v[36:39], v[224:227]\n"
"ds_read_b64_tr_b16 a[184:185], v5 offset:25088\n"
"v_mfma_f32_16x16x32_bf16 v[224:227], a[160:163], v[32:35], v[224:227]\n"
"v_exp_f32 v29, v29\n"
"v_mfma_f32_16x16x32_bf16 v[228:231], a[164:167], v[32:35], v[228:231]\n"
"ds_read_b64_tr_b16 a[186:187], v5 offset:29184\n"
"v_cvt_pk_bf16_f32 v250, v28, v29\n"
"v_mfma_f32_16x16x32_bf16 v[228:231], a[196:199], v[36:39], v[228:231]\n"
"v_exp_f32 v30, v30\n"
"v_mfma_f32_16x16x32_bf16 v[232:235], a[200:203], v[36:39], v[232:235]\n"
"ds_read_b64_tr_b16 a[156:157], v6 offset:16896\n"
"v_mfma_f32_16x16x32_bf16 v[232:235], a[168:171], v[32:35], v[232:235]\n"
"ds_read_b64_tr_b16 a[158:159], v6 offset:20992\n"
"v_exp_f32 v31, v31\n"
"v_mfma_f32_16x16x32_bf16 v[236:239], a[172:175], v[32:35], v[236:239]\n"
"ds_read_b64_tr_b16 a[188:189], v6 offset:25088\n"
"v_cvt_pk_bf16_f32 v251, v30, v31\n"
"v_mfma_f32_16x16x32_bf16 v[236:239], a[204:207], v[36:39], v[236:239]\n"
"ds_read_b64_tr_b16 a[190:191], v6 offset:29184\n"
"v_mfma_f32_16x16x32_bf16 v[24:27], a[112:115], a[16:19], v[44:47]\n"
"v_exp_f32 v16, v16\n"
"v_mfma_f32_16x16x32_bf16 v[24:27], a[116:119], a[20:23], v[24:27]\n"
"ds_read_b64_tr_b16 a[160:161], v5 offset:17408\n"
"v_mfma_f32_16x16x32_bf16 v[24:27], a[120:123], a[24:27], v[24:27]\n"
"ds_read_b64_tr_b16 a[162:163], v5 offset:21504\n"
"v_exp_f32 v17, v17\n"
"v_mfma_f32_16x16x32_bf16 v[24:27], a[124:127], a[28:31], v[24:27]\n"
"ds_read_b64_tr_b16 a[192:193], v5 offset:25600\n"
"v_cvt_pk_bf16_f32 v32, v16, v17\n"
"v_mfma_f32_16x16x32_bf16 v[28:31], a[140:143], a[28:31], v[44:47]\n"
"v_exp_f32 v18, v18\n"
"v_mfma_f32_16x16x32_bf16 v[28:31], a[136:139], a[24:27], v[28:31]\n"
"ds_read_b64_tr_b16 a[194:195], v5 offset:29696\n"
"v_mfma_f32_16x16x32_bf16 v[28:31], a[132:135], a[20:23], v[28:31]\n"
"v_exp_f32 v19, v19\n"
"v_mfma_f32_16x16x32_bf16 v[28:31], a[128:131], a[16:19], v[28:31]\n"
"ds_read_b64_tr_b16 a[164:165], v6 offset:17408\n"
"v_cvt_pk_bf16_f32 v33, v18, v19\n"
"v_mfma_f32_16x16x32_bf16 v[60:63], a[208:211], v[244:247], v[60:63]\n"
"s_waitcnt lgkmcnt(15)\n"
"ds_read_b64_tr_b16 a[166:167], v6 offset:21504\n"
"v_exp_f32 v20, v20\n"
"v_mfma_f32_16x16x32_bf16 v[80:83], a[144:147], v[244:247], v[80:83]\n"
"ds_read_b64_tr_b16 a[196:197], v6 offset:25600\n"
"v_mfma_f32_16x16x32_bf16 v[80:83], a[176:179], v[248:251], v[80:83]\n"
"s_waitcnt lgkmcnt(15)\n"
"v_exp_f32 v21, v21\n"
"v_mfma_f32_16x16x32_bf16 v[84:87], a[180:183], v[248:251], v[84:87]\n"
"ds_read_b64_tr_b16 a[198:199], v6 offset:29696\n"
"v_cvt_pk_bf16_f32 v34, v20, v21\n"
"v_mfma_f32_16x16x32_bf16 v[84:87], a[148:151], v[244:247], v[84:87]\n"
"s_waitcnt lgkmcnt(12)\n"
"v_exp_f32 v22, v22\n"
"v_mfma_f32_16x16x32_bf16 v[88:91], a[152:155], v[244:247], v[88:91]\n"
"ds_read_b64_tr_b16 a[168:169], v5 offset:17920\n"
"v_mfma_f32_16x16x32_bf16 v[88:91], a[184:187], v[248:251], v[88:91]\n"
"s_waitcnt lgkmcnt(9)\n"
"v_exp_f32 v23, v23\n"
"v_mfma_f32_16x16x32_bf16 v[92:95], a[188:191], v[248:251], v[92:95]\n"
"ds_read_b64_tr_b16 a[170:171], v5 offset:22016\n"
"v_cvt_pk_bf16_f32 v35, v22, v23\n"
"v_mfma_f32_16x16x32_bf16 v[92:95], a[156:159], v[244:247], v[92:95]\n"
"ds_read_b64_tr_b16 a[200:201], v5 offset:26112\n"
"v_mfma_f32_16x16x32_bf16 v[16:19], a[80:83], a[32:35], v[48:51]\n"
"v_exp_f32 v24, v24\n"
"v_mfma_f32_16x16x32_bf16 v[16:19], a[84:87], a[36:39], v[16:19]\n"
"ds_read_b64_tr_b16 a[202:203], v5 offset:30208\n"
"v_mfma_f32_16x16x32_bf16 v[16:19], a[88:91], a[40:43], v[16:19]\n"
"v_exp_f32 v25, v25\n"
"v_mfma_f32_16x16x32_bf16 v[16:19], a[92:95], a[44:47], v[16:19]\n"
"ds_read_b64_tr_b16 a[172:173], v6 offset:17920\n"
"v_cvt_pk_bf16_f32 v36, v24, v25\n"
"v_mfma_f32_16x16x32_bf16 v[20:23], a[108:111], a[44:47], v[48:51]\n"
"v_exp_f32 v26, v26\n"
"v_mfma_f32_16x16x32_bf16 v[20:23], a[104:107], a[40:43], v[20:23]\n"
"ds_read_b64_tr_b16 a[174:175], v6 offset:22016\n"
"v_mfma_f32_16x16x32_bf16 v[20:23], a[100:103], a[36:39], v[20:23]\n"
"v_exp_f32 v27, v27\n"
"v_mfma_f32_16x16x32_bf16 v[20:23], a[96:99], a[32:35], v[20:23]\n"
"ds_read_b64_tr_b16 a[204:205], v6 offset:26112\n"
"v_cvt_pk_bf16_f32 v37, v26, v27\n"
"v_mfma_f32_16x16x32_bf16 v[60:63], a[208:211], v[248:251], v[60:63]\n"
"s_waitcnt lgkmcnt(11)\n"
"v_exp_f32 v28, v28\n"
"v_mfma_f32_16x16x32_bf16 v[96:99], a[192:195], v[248:251], v[96:99]\n"
"ds_read_b64_tr_b16 a[206:207], v6 offset:30208\n"
"v_mfma_f32_16x16x32_bf16 v[96:99], a[160:163], v[244:247], v[96:99]\n"
"s_waitcnt lgkmcnt(8)\n"
"v_exp_f32 v29, v29\n"
"v_mfma_f32_16x16x32_bf16 v[100:103], a[164:167], v[244:247], v[100:103]\n"
"v_cvt_pk_bf16_f32 v38, v28, v29\n"
"v_mfma_f32_16x16x32_bf16 v[100:103], a[196:199], v[248:251], v[100:103]\n"
"s_waitcnt lgkmcnt(4)\n"
"s_mov_b32 s95, 11264\n"
"s_add_u32 m0, s94, 0\n"
"v_exp_f32 v30, v30\n"
"v_mfma_f32_16x16x32_bf16 v[104:107], a[200:203], v[248:251], v[104:107]\n"
"buffer_load_dwordx4 v9, s[56:59], s95 offen offset:0 sc1 lds\n"
"v_mfma_f32_16x16x32_bf16 v[104:107], a[168:171], v[244:247], v[104:107]\n"
"s_waitcnt lgkmcnt(0)\n"
"v_exp_f32 v31, v31\n"
"v_mfma_f32_16x16x32_bf16 v[108:111], a[172:175], v[244:247], v[108:111]\n"
"s_mov_b32 s95, 11264\n"
"v_cvt_pk_bf16_f32 v39, v30, v31\n"
"v_mfma_f32_16x16x32_bf16 v[108:111], a[204:207], v[248:251], v[108:111]\n"
"buffer_load_dwordx4 v9, s[56:59], s95 offen offset:1024 sc1 lds\n"
"v_mfma_f32_16x16x32_bf16 v[24:27], a[112:115], a[32:35], v[48:51]\n"
"v_exp_f32 v16, v16\n"
"v_mfma_f32_16x16x32_bf16 v[24:27], a[116:119], a[36:39], v[24:27]\n"
"v_mfma_f32_16x16x32_bf16 v[24:27], a[120:123], a[40:43], v[24:27]\n"
"s_mov_b32 s95, 11264\n"
"v_exp_f32 v17, v17\n"
"v_mfma_f32_16x16x32_bf16 v[24:27], a[124:127], a[44:47], v[24:27]\n"
"buffer_load_dwordx4 v9, s[56:59], s95 offen offset:2048 sc1 lds\n"
"v_cvt_pk_bf16_f32 v244, v16, v17\n"
"v_mfma_f32_16x16x32_bf16 v[28:31], a[140:143], a[44:47], v[48:51]\n"
"v_exp_f32 v18, v18\n"
"v_mfma_f32_16x16x32_bf16 v[28:31], a[136:139], a[40:43], v[28:31]\n"
"v_mfma_f32_16x16x32_bf16 v[28:31], a[132:135], a[36:39], v[28:31]\n"
"v_exp_f32 v19, v19\n"
"v_mfma_f32_16x16x32_bf16 v[28:31], a[128:131], a[32:35], v[28:31]\n"
"v_cvt_pk_bf16_f32 v245, v18, v19\n"
"v_mfma_f32_16x16x32_bf16 v[64:67], a[208:211], v[32:35], v[64:67]\n"
"s_mov_b32 s95, 11264\n"
"v_exp_f32 v20, v20\n"
"v_mfma_f32_16x16x32_bf16 v[112:115], a[144:147], v[32:35], v[112:115]\n"
"buffer_load_dwordx4 v9, s[56:59], s95 offen offset:3072 sc1 lds\n"
"v_mfma_f32_16x16x32_bf16 v[112:115], a[176:179], v[36:39], v[112:115]\n"
"v_exp_f32 v21, v21\n"
"v_mfma_f32_16x16x32_bf16 v[116:119], a[180:183], v[36:39], v[116:119]\n"
"v_cvt_pk_bf16_f32 v246, v20, v21\n"
"v_mfma_f32_16x16x32_bf16 v[116:119], a[148:151], v[32:35], v[116:119]\n"
"s_mov_b32 s95, 15360\n"
"s_add_u32 m0, s94, 4096\n"
"v_exp_f32 v22, v22\n"
"v_mfma_f32_16x16x32_bf16 v[120:123], a[152:155], v[32:35], v[120:123]\n"
"buffer_load_dwordx4 v9, s[56:59], s95 offen offset:0 sc1 lds\n"
"v_mfma_f32_16x16x32_bf16 v[120:123], a[184:187], v[36:39], v[120:123]\n"
"v_exp_f32 v23, v23\n"
"v_mfma_f32_16x16x32_bf16 v[124:127], a[188:191], v[36:39], v[124:127]\n"
"s_mov_b32 s95, 15360\n"
"v_cvt_pk_bf16_f32 v247, v22, v23\n"
"v_mfma_f32_16x16x32_bf16 v[124:127], a[156:159], v[32:35], v[124:127]\n"
"buffer_load_dwordx4 v9, s[56:59], s95 offen offset:1024 sc1 lds\n"
"v_mfma_f32_16x16x32_bf16 v[16:19], a[80:83], a[48:51], v[52:55]\n"
"v_exp_f32 v24, v24\n"
"v_mfma_f32_16x16x32_bf16 v[16:19], a[84:87], a[52:55], v[16:19]\n"
"v_mfma_f32_16x16x32_bf16 v[16:19], a[88:91], a[56:59], v[16:19]\n"
"v_exp_f32 v25, v25\n"
"v_mfma_f32_16x16x32_bf16 v[16:19], a[92:95], a[60:63], v[16:19]\n"
"v_cvt_pk_bf16_f32 v248, v24, v25\n"
"v_mfma_f32_16x16x32_bf16 v[20:23], a[108:111], a[60:63], v[52:55]\n"
"s_mov_b32 s95, 15360\n"
"v_exp_f32 v26, v26\n"
"v_mfma_f32_16x16x32_bf16 v[20:23], a[104:107], a[56:59], v[20:23]\n"
"buffer_load_dwordx4 v9, s[56:59], s95 offen offset:2048 sc1 lds\n"
"v_mfma_f32_16x16x32_bf16 v[20:23], a[100:103], a[52:55], v[20:23]\n"
"v_exp_f32 v27, v27\n"
"v_mfma_f32_16x16x32_bf16 v[20:23], a[96:99], a[48:51], v[20:23]\n"
"v_cvt_pk_bf16_f32 v249, v26, v27\n"
"v_mfma_f32_16x16x32_bf16 v[64:67], a[208:211], v[36:39], v[64:67]\n"
"v_exp_f32 v28, v28\n"
"v_mfma_f32_16x16x32_bf16 v[128:131], a[192:195], v[36:39], v[128:131]\n"
"v_mfma_f32_16x16x32_bf16 v[128:131], a[160:163], v[32:35], v[128:131]\n"
"s_mov_b32 s95, 15360\n"
"v_exp_f32 v29, v29\n"
"v_mfma_f32_16x16x32_bf16 v[132:135], a[164:167], v[32:35], v[132:135]\n"
"buffer_load_dwordx4 v9, s[56:59], s95 offen offset:3072 sc1 lds\n"
"v_cvt_pk_bf16_f32 v250, v28, v29\n"
"v_mfma_f32_16x16x32_bf16 v[132:135], a[196:199], v[36:39], v[132:135]\n"
"v_exp_f32 v30, v30\n"
"v_mfma_f32_16x16x32_bf16 v[136:139], a[200:203], v[36:39], v[136:139]\n"
"v_mfma_f32_16x16x32_bf16 v[136:139], a[168:171], v[32:35], v[136:139]\n"
"v_exp_f32 v31, v31\n"
"v_mfma_f32_16x16x32_bf16 v[140:143], a[172:175], v[32:35], v[140:143]\n"
"s_mov_b32 s95, 0\n"
"v_cvt_pk_bf16_f32 v251, v30, v31\n"
"v_mfma_f32_16x16x32_bf16 v[140:143], a[204:207], v[36:39], v[140:143]\n"
"buffer_load_dwordx4 a[212:215], v9, s[56:59], s95 offen offset:0 sc1\n"
"v_mfma_f32_16x16x32_bf16 v[24:27], a[112:115], a[48:51], v[52:55]\n"
"v_exp_f32 v16, v16\n"
"v_mfma_f32_16x16x32_bf16 v[24:27], a[116:119], a[52:55], v[24:27]\n"
"v_mfma_f32_16x16x32_bf16 v[24:27], a[120:123], a[56:59], v[24:27]\n"
"s_mov_b32 s95, 0\n"
"v_exp_f32 v17, v17\n"
"v_mfma_f32_16x16x32_bf16 v[24:27], a[124:127], a[60:63], v[24:27]\n"
"buffer_load_dwordx4 a[216:219], v9, s[56:59], s95 offen offset:1024 sc1\n"
"v_cvt_pk_bf16_f32 v32, v16, v17\n"
"v_mfma_f32_16x16x32_bf16 v[28:31], a[140:143], a[60:63], v[52:55]\n"
"v_exp_f32 v18, v18\n"
"v_mfma_f32_16x16x32_bf16 v[28:31], a[136:139], a[56:59], v[28:31]\n"
"v_mfma_f32_16x16x32_bf16 v[28:31], a[132:135], a[52:55], v[28:31]\n"
"v_exp_f32 v19, v19\n"
"v_mfma_f32_16x16x32_bf16 v[28:31], a[128:131], a[48:51], v[28:31]\n"
"v_cvt_pk_bf16_f32 v33, v18, v19\n"
"v_mfma_f32_16x16x32_bf16 v[68:71], a[208:211], v[244:247], v[68:71]\n"
"s_mov_b32 s95, 0\n"
"v_exp_f32 v20, v20\n"
"v_mfma_f32_16x16x32_bf16 v[144:147], a[144:147], v[244:247], v[144:147]\n"
"buffer_load_dwordx4 a[220:223], v9, s[56:59], s95 offen offset:2048 sc1\n"
"v_mfma_f32_16x16x32_bf16 v[144:147], a[176:179], v[248:251], v[144:147]\n"
"v_exp_f32 v21, v21\n"
"v_mfma_f32_16x16x32_bf16 v[148:151], a[180:183], v[248:251], v[148:151]\n"
"v_cvt_pk_bf16_f32 v34, v20, v21\n"
"v_mfma_f32_16x16x32_bf16 v[148:151], a[148:151], v[244:247], v[148:151]\n"
"v_exp_f32 v22, v22\n"
"v_mfma_f32_16x16x32_bf16 v[152:155], a[152:155], v[244:247], v[152:155]\n"
"v_mfma_f32_16x16x32_bf16 v[152:155], a[184:187], v[248:251], v[152:155]\n"
"v_exp_f32 v23, v23\n"
"v_mfma_f32_16x16x32_bf16 v[156:159], a[188:191], v[248:251], v[156:159]\n"
"s_mov_b32 s95, 0\n"
"v_cvt_pk_bf16_f32 v35, v22, v23\n"
"v_mfma_f32_16x16x32_bf16 v[156:159], a[156:159], v[244:247], v[156:159]\n"
"buffer_load_dwordx4 a[224:227], v9, s[56:59], s95 offen offset:3072 sc1\n"
"v_mfma_f32_16x16x32_bf16 v[16:19], a[80:83], a[64:67], v[56:59]\n"
"s_waitcnt vmcnt(20) lgkmcnt(0)\n"
"s_barrier\n"
"v_exp_f32 v24, v24\n"
"v_mfma_f32_16x16x32_bf16 v[16:19], a[84:87], a[68:71], v[16:19]\n"
"ds_read_b128 a[80:83], v1 offset:32768\n"
"v_mfma_f32_16x16x32_bf16 v[16:19], a[88:91], a[72:75], v[16:19]\n"
"v_exp_f32 v25, v25\n"
"v_mfma_f32_16x16x32_bf16 v[16:19], a[92:95], a[76:79], v[16:19]\n"
"ds_read_b128 a[84:87], v2 offset:32768\n"
"v_cvt_pk_bf16_f32 v36, v24, v25\n"
"v_mfma_f32_16x16x32_bf16 v[20:23], a[108:111], a[76:79], v[56:59]\n"
"v_exp_f32 v26, v26\n"
"v_mfma_f32_16x16x32_bf16 v[20:23], a[104:107], a[72:75], v[20:23]\n"
"ds_read_b128 a[88:91], v3 offset:32768\n"
"v_mfma_f32_16x16x32_bf16 v[20:23], a[100:103], a[68:71], v[20:23]\n"
"v_exp_f32 v27, v27\n"
"v_mfma_f32_16x16x32_bf16 v[20:23], a[96:99], a[64:67], v[20:23]\n"
"ds_read_b128 a[92:95], v4 offset:32768\n"
"v_cvt_pk_bf16_f32 v37, v26, v27\n"
"v_mfma_f32_16x16x32_bf16 v[68:71], a[208:211], v[248:251], v[68:71]\n"
"v_exp_f32 v28, v28\n"
"v_mfma_f32_16x16x32_bf16 v[160:163], a[192:195], v[248:251], v[160:163]\n"
"ds_read_b128 a[108:111], v4 offset:36864\n"
"v_mfma_f32_16x16x32_bf16 v[160:163], a[160:163], v[244:247], v[160:163]\n"
"v_exp_f32 v29, v29\n"
"v_mfma_f32_16x16x32_bf16 v[164:167], a[164:167], v[244:247], v[164:167]\n"
"ds_read_b128 a[104:107], v3 offset:36864\n"
"v_cvt_pk_bf16_f32 v38, v28, v29\n"
"v_mfma_f32_16x16x32_bf16 v[164:167], a[196:199], v[248:251], v[164:167]\n"
"v_exp_f32 v30, v30\n"
"v_mfma_f32_16x16x32_bf16 v[168:171], a[200:203], v[248:251], v[168:171]\n"
"ds_read_b128 a[100:103], v2 offset:36864\n"
"v_mfma_f32_16x16x32_bf16 v[168:171], a[168:171], v[244:247], v[168:171]\n"
"v_exp_f32 v31, v31\n"
"v_mfma_f32_16x16x32_bf16 v[172:175], a[172:175], v[244:247], v[172:175]\n"
"s_mov_b32 s95, 19456\n"
"s_add_u32 m0, s83, 0\n"
"ds_read_b128 a[96:99], v1 offset:36864\n"
"v_cvt_pk_bf16_f32 v39, v30, v31\n"
"v_mfma_f32_16x16x32_bf16 v[172:175], a[204:207], v[248:251], v[172:175]\n"
"buffer_load_dwordx4 v9, s[56:59], s95 offen offset:0 sc1 lds\n"
"v_mfma_f32_16x16x32_bf16 v[24:27], a[112:115], a[64:67], v[56:59]\n"
"v_exp_f32 v16, v16\n"
"v_mfma_f32_16x16x32_bf16 v[24:27], a[116:119], a[68:71], v[24:27]\n"
"ds_read_b128 a[112:115], v1 offset:40960\n"
"v_mfma_f32_16x16x32_bf16 v[24:27], a[120:123], a[72:75], v[24:27]\n"
"v_exp_f32 v17, v17\n"
"v_mfma_f32_16x16x32_bf16 v[24:27], a[124:127], a[76:79], v[24:27]\n"
"ds_read_b128 a[116:119], v2 offset:40960\n"
"v_cvt_pk_bf16_f32 v244, v16, v17\n"
"v_mfma_f32_16x16x32_bf16 v[28:31], a[140:143], a[76:79], v[56:59]\n"
"v_exp_f32 v18, v18\n"
"v_mfma_f32_16x16x32_bf16 v[28:31], a[136:139], a[72:75], v[28:31]\n"
"ds_read_b128 a[120:123], v3 offset:40960\n"
"v_mfma_f32_16x16x32_bf16 v[28:31], a[132:135], a[68:71], v[28:31]\n"
"v_exp_f32 v19, v19\n"
"v_mfma_f32_16x16x32_bf16 v[28:31], a[128:131], a[64:67], v[28:31]\n"
"ds_read_b128 a[124:127], v4 offset:40960\n"
"v_cvt_pk_bf16_f32 v245, v18, v19\n"
"v_mfma_f32_16x16x32_bf16 v[72:75], a[208:211], v[32:35], v[72:75]\n"
"v_exp_f32 v20, v20\n"
"v_mfma_f32_16x16x32_bf16 v[176:179], a[144:147], v[32:35], v[176:179]\n"
"ds_read_b128 a[140:143], v4 offset:45056\n"
"v_mfma_f32_16x16x32_bf16 v[176:179], a[176:179], v[36:39], v[176:179]\n"
"v_exp_f32 v21, v21\n"
"v_mfma_f32_16x16x32_bf16 v[180:183], a[180:183], v[36:39], v[180:183]\n"
"ds_read_b128 a[136:139], v3 offset:45056\n"
"v_cvt_pk_bf16_f32 v246, v20, v21\n"
"v_mfma_f32_16x16x32_bf16 v[180:183], a[148:151], v[32:35], v[180:183]\n"
"v_exp_f32 v22, v22\n"
"v_mfma_f32_16x16x32_bf16 v[184:187], a[152:155], v[32:35], v[184:187]\n"
"ds_read_b128 a[132:135], v2 offset:45056\n"
"v_mfma_f32_16x16x32_bf16 v[184:187], a[184:187], v[36:39], v[184:187]\n"
"v_exp_f32 v23, v23\n"
"v_mfma_f32_16x16x32_bf16 v[188:191], a[188:191], v[36:39], v[188:191]\n"
"s_mov_b32 s95, 19456\n"
"ds_read_b128 a[128:131], v1 offset:45056\n"
"v_cvt_pk_bf16_f32 v247, v22, v23\n"
"v_mfma_f32_16x16x32_bf16 v[188:191], a[156:159], v[32:35], v[188:191]\n"
"s_waitcnt lgkmcnt(8)\n"
"buffer_load_dwordx4 v9, s[56:59], s95 offen offset:1024 sc1 lds\n"
"v_mfma_f32_16x16x32_bf16 v[16:19], a[80:83], a[0:3], v[40:43]\n"
"v_exp_f32 v24, v24\n"
"v_mfma_f32_16x16x32_bf16 v[16:19], a[84:87], a[4:7], v[16:19]\n"
"v_mfma_f32_16x16x32_bf16 v[16:19], a[88:91], a[8:11], v[16:19]\n"
"v_exp_f32 v25, v25\n"
"v_mfma_f32_16x16x32_bf16 v[16:19], a[92:95], a[12:15], v[16:19]\n"
"v_cvt_pk_bf16_f32 v248, v24, v25\n"
"v_mfma_f32_16x16x32_bf16 v[20:23], a[108:111], a[12:15], v[40:43]\n"
"s_mov_b32 s95, 19456\n"
"v_exp_f32 v26, v26\n"
"v_mfma_f32_16x16x32_bf16 v[20:23], a[104:107], a[8:11], v[20:23]\n"
"buffer_load_dwordx4 v9, s[56:59], s95 offen offset:2048 sc1 lds\n"
"v_mfma_f32_16x16x32_bf16 v[20:23], a[100:103], a[4:7], v[20:23]\n"
"v_exp_f32 v27, v27\n"
"v_mfma_f32_16x16x32_bf16 v[20:23], a[96:99], a[0:3], v[20:23]\n"
"v_cvt_pk_bf16_f32 v249, v26, v27\n"
"v_mfma_f32_16x16x32_bf16 v[72:75], a[208:211], v[36:39], v[72:75]\n"
"s_mov_b32 s95, 19456\n"
"v_exp_f32 v28, v28\n"
"v_mfma_f32_16x16x32_bf16 v[192:195], a[192:195], v[36:39], v[192:195]\n"
"buffer_load_dwordx4 v9, s[56:59], s95 offen offset:3072 sc1 lds\n"
"v_mfma_f32_16x16x32_bf16 v[192:195], a[160:163], v[32:35], v[192:195]\n"
"v_exp_f32 v29, v29\n"
"v_mfma_f32_16x16x32_bf16 v[196:199], a[164:167], v[32:35], v[196:199]\n"
"v_cvt_pk_bf16_f32 v250, v28, v29\n"
"v_mfma_f32_16x16x32_bf16 v[196:199], a[196:199], v[36:39], v[196:199]\n"
"s_mov_b32 s95, 23552\n"
"s_add_u32 m0, s83, 65536\n"
"v_exp_f32 v30, v30\n"
"v_mfma_f32_16x16x32_bf16 v[200:203], a[200:203], v[36:39], v[200:203]\n"
"buffer_load_dwordx4 v9, s[56:59], s95 offen offset:0 sc1 lds\n"
"v_mfma_f32_16x16x32_bf16 v[200:203], a[168:171], v[32:35], v[200:203]\n"
"v_exp_f32 v31, v31\n"
"v_mfma_f32_16x16x32_bf16 v[204:207], a[172:175], v[32:35], v[204:207]\n"
"s_mov_b32 s95, 23552\n"
"v_cvt_pk_bf16_f32 v251, v30, v31\n"
"v_mfma_f32_16x16x32_bf16 v[204:207], a[204:207], v[36:39], v[204:207]\n"
"s_waitcnt lgkmcnt(0)\n"
"buffer_load_dwordx4 v9, s[56:59], s95 offen offset:1024 sc1 lds\n"
"v_mfma_f32_16x16x32_bf16 v[24:27], a[112:115], a[0:3], v[40:43]\n"
"s_mov_b32 s95, 23552\n"
"v_exp_f32 v16, v16\n"
"v_mfma_f32_16x16x32_bf16 v[24:27], a[116:119], a[4:7], v[24:27]\n"
"buffer_load_dwordx4 v9, s[56:59], s95 offen offset:2048 sc1 lds\n"
"v_mfma_f32_16x16x32_bf16 v[24:27], a[120:123], a[8:11], v[24:27]\n"
"v_exp_f32 v17, v17\n"
"v_mfma_f32_16x16x32_bf16 v[24:27], a[124:127], a[12:15], v[24:27]\n"
"v_cvt_pk_bf16_f32 v32, v16, v17\n"
"v_mfma_f32_16x16x32_bf16 v[28:31], a[140:143], a[12:15], v[40:43]\n"
"s_mov_b32 s95, 23552\n"
"v_exp_f32 v18, v18\n"
"v_mfma_f32_16x16x32_bf16 v[28:31], a[136:139], a[8:11], v[28:31]\n"
"buffer_load_dwordx4 v9, s[56:59], s95 offen offset:3072 sc1 lds\n"
"v_mfma_f32_16x16x32_bf16 v[28:31], a[132:135], a[4:7], v[28:31]\n"
"v_exp_f32 v19, v19\n"
"v_mfma_f32_16x16x32_bf16 v[28:31], a[128:131], a[0:3], v[28:31]\n"
"v_cvt_pk_bf16_f32 v33, v18, v19\n"
"v_mfma_f32_16x16x32_bf16 v[76:79], a[208:211], v[244:247], v[76:79]\n"
"v_exp_f32 v20, v20\n"
"v_mfma_f32_16x16x32_bf16 v[208:211], a[144:147], v[244:247], v[208:211]\n"
"v_mfma_f32_16x16x32_bf16 v[208:211], a[176:179], v[248:251], v[208:211]\n"
"v_exp_f32 v21, v21\n"
"v_mfma_f32_16x16x32_bf16 v[212:215], a[180:183], v[248:251], v[212:215]\n"
"ds_read_b64_tr_b16 a[144:145], v5 offset:32768\n"
"v_cvt_pk_bf16_f32 v34, v20, v21\n"
"v_mfma_f32_16x16x32_bf16 v[212:215], a[148:151], v[244:247], v[212:215]\n"
"v_exp_f32 v22, v22\n"
"v_mfma_f32_16x16x32_bf16 v[216:219], a[152:155], v[244:247], v[216:219]\n"
"ds_read_b64_tr_b16 a[146:147], v5 offset:36864\n"
"v_mfma_f32_16x16x32_bf16 v[216:219], a[184:187], v[248:251], v[216:219]\n"
"v_exp_f32 v23, v23\n"
"v_mfma_f32_16x16x32_bf16 v[220:223], a[188:191], v[248:251], v[220:223]\n"
"ds_read_b64_tr_b16 a[176:177], v5 offset:40960\n"
"v_cvt_pk_bf16_f32 v35, v22, v23\n"
"v_mfma_f32_16x16x32_bf16 v[220:223], a[156:159], v[244:247], v[220:223]\n"
"ds_read_b64_tr_b16 a[178:179], v5 offset:45056\n"
"v_mfma_f32_16x16x32_bf16 v[16:19], a[80:83], a[16:19], v[44:47]\n"
"v_exp_f32 v24, v24\n"
"v_mfma_f32_16x16x32_bf16 v[16:19], a[84:87], a[20:23], v[16:19]\n"
"ds_read_b64_tr_b16 a[148:149], v6 offset:32768\n"
"v_mfma_f32_16x16x32_bf16 v[16:19], a[88:91], a[24:27], v[16:19]\n"
"ds_read_b64_tr_b16 a[150:151], v6 offset:36864\n"
"v_exp_f32 v25, v25\n"
"v_mfma_f32_16x16x32_bf16 v[16:19], a[92:95], a[28:31], v[16:19]\n"
"ds_read_b64_tr_b16 a[180:181], v6 offset:40960\n"
"v_cvt_pk_bf16_f32 v36, v24, v25\n"
"v_mfma_f32_16x16x32_bf16 v[20:23], a[108:111], a[28:31], v[44:47]\n"
"v_exp_f32 v26, v26\n"
"v_mfma_f32_16x16x32_bf16 v[20:23], a[104:107], a[24:27], v[20:23]\n"
"ds_read_b64_tr_b16 a[182:183], v6 offset:45056\n"
"v_mfma_f32_16x16x32_bf16 v[20:23], a[100:103], a[20:23], v[20:23]\n"
"v_exp_f32 v27, v27\n"
"v_mfma_f32_16x16x32_bf16 v[20:23], a[96:99], a[16:19], v[20:23]\n"
"ds_read_b64_tr_b16 a[152:153], v5 offset:33280\n"
"v_cvt_pk_bf16_f32 v37, v26, v27\n"
"v_mfma_f32_16x16x32_bf16 v[76:79], a[208:211], v[248:251], v[76:79]\n"
"ds_read_b64_tr_b16 a[154:155], v5 offset:37376\n"
"v_exp_f32 v28, v28\n"
"v_mfma_f32_16x16x32_bf16 v[224:227], a[192:195], v[248:251], v[224:227]\n"
"ds_read_b64_tr_b16 a[184:185], v5 offset:41472\n"
"v_mfma_f32_16x16x32_bf16 v[224:227], a[160:163], v[244:247], v[224:227]\n"
"v_exp_f32 v29, v29\n"
"v_mfma_f32_16x16x32_bf16 v[228:231], a[164:167], v[244:247], v[228:231]\n"
"ds_read_b64_tr_b16 a[186:187], v5 offset:45568\n"
"v_cvt_pk_bf16_f32 v38, v28, v29\n"
"v_mfma_f32_16x16x32_bf16 v[228:231], a[196:199], v[248:251], v[228:231]\n"
"v_exp_f32 v30, v30\n"
"v_mfma_f32_16x16x32_bf16 v[232:235], a[200:203], v[248:251], v[232:235]\n"
"ds_read_b64_tr_b16 a[156:157], v6 offset:33280\n"
"v_mfma_f32_16x16x32_bf16 v[232:235], a[168:171], v[244:247], v[232:235]\n"
"ds_read_b64_tr_b16 a[158:159], v6 offset:37376\n"
"v_exp_f32 v31, v31\n"
"v_mfma_f32_16x16x32_bf16 v[236:239], a[172:175], v[244:247], v[236:239]\n"
"ds_read_b64_tr_b16 a[188:189], v6 offset:41472\n"
"v_cvt_pk_bf16_f32 v39, v30, v31\n"
"v_mfma_f32_16x16x32_bf16 v[236:239], a[204:207], v[248:251], v[236:239]\n"
"ds_read_b64_tr_b16 a[190:191], v6 offset:45568\n"
"v_mfma_f32_16x16x32_bf16 v[24:27], a[112:115], a[16:19], v[44:47]\n"
"v_exp_f32 v16, v16\n"
"v_mfma_f32_16x16x32_bf16 v[24:27], a[116:119], a[20:23], v[24:27]\n"
"ds_read_b64_tr_b16 a[160:161], v5 offset:33792\n"
"v_mfma_f32_16x16x32_bf16 v[24:27], a[120:123], a[24:27], v[24:27]\n"
"ds_read_b64_tr_b16 a[162:163], v5 offset:37888\n"
"v_exp_f32 v17, v17\n"
"v_mfma_f32_16x16x32_bf16 v[24:27], a[124:127], a[28:31], v[24:27]\n"
"ds_read_b64_tr_b16 a[192:193], v5 offset:41984\n"
"v_cvt_pk_bf16_f32 v244, v16, v17\n"
"v_mfma_f32_16x16x32_bf16 v[28:31], a[140:143], a[28:31], v[44:47]\n"
"v_exp_f32 v18, v18\n"
"v_mfma_f32_16x16x32_bf16 v[28:31], a[136:139], a[24:27], v[28:31]\n"
"ds_read_b64_tr_b16 a[194:195], v5 offset:46080\n"
"v_mfma_f32_16x16x32_bf16 v[28:31], a[132:135], a[20:23], v[28:31]\n"
"v_exp_f32 v19, v19\n"
"v_mfma_f32_16x16x32_bf16 v[28:31], a[128:131], a[16:19], v[28:31]\n"
"ds_read_b64_tr_b16 a[164:165], v6 offset:33792\n"
"v_cvt_pk_bf16_f32 v245, v18, v19\n"
"v_mfma_f32_16x16x32_bf16 v[60:63], a[208:211], v[32:35], v[60:63]\n"
"s_waitcnt lgkmcnt(15)\n"
"ds_read_b64_tr_b16 a[166:167], v6 offset:37888\n"
"v_exp_f32 v20, v20\n"
"v_mfma_f32_16x16x32_bf16 v[80:83], a[144:147], v[32:35], v[80:83]\n"
"ds_read_b64_tr_b16 a[196:197], v6 offset:41984\n"
"v_mfma_f32_16x16x32_bf16 v[80:83], a[176:179], v[36:39], v[80:83]\n"
"s_waitcnt lgkmcnt(15)\n"
"v_exp_f32 v21, v21\n"
"v_mfma_f32_16x16x32_bf16 v[84:87], a[180:183], v[36:39], v[84:87]\n"
"ds_read_b64_tr_b16 a[198:199], v6 offset:46080\n"
"v_cvt_pk_bf16_f32 v246, v20, v21\n"
"v_mfma_f32_16x16x32_bf16 v[84:87], a[148:151], v[32:35], v[84:87]\n"
"s_waitcnt lgkmcnt(12)\n"
"v_exp_f32 v22, v22\n"
"v_mfma_f32_16x16x32_bf16 v[88:91], a[152:155], v[32:35], v[88:91]\n"
"ds_read_b64_tr_b16 a[168:169], v5 offset:34304\n"
"v_mfma_f32_16x16x32_bf16 v[88:91], a[184:187], v[36:39], v[88:91]\n"
"s_waitcnt lgkmcnt(9)\n"
"v_exp_f32 v23, v23\n"
"v_mfma_f32_16x16x32_bf16 v[92:95], a[188:191], v[36:39], v[92:95]\n"
"ds_read_b64_tr_b16 a[170:171], v5 offset:38400\n"
"v_cvt_pk_bf16_f32 v247, v22, v23\n"
"v_mfma_f32_16x16x32_bf16 v[92:95], a[156:159], v[32:35], v[92:95]\n"
"ds_read_b64_tr_b16 a[200:201], v5 offset:42496\n"
"v_mfma_f32_16x16x32_bf16 v[16:19], a[80:83], a[32:35], v[48:51]\n"
"v_exp_f32 v24, v24\n"
"v_mfma_f32_16x16x32_bf16 v[16:19], a[84:87], a[36:39], v[16:19]\n"
"ds_read_b64_tr_b16 a[202:203], v5 offset:46592\n"
"v_mfma_f32_16x16x32_bf16 v[16:19], a[88:91], a[40:43], v[16:19]\n"
"v_exp_f32 v25, v25\n"
"v_mfma_f32_16x16x32_bf16 v[16:19], a[92:95], a[44:47], v[16:19]\n"
"ds_read_b64_tr_b16 a[172:173], v6 offset:34304\n"
"v_cvt_pk_bf16_f32 v248, v24, v25\n"
"v_mfma_f32_16x16x32_bf16 v[20:23], a[108:111], a[44:47], v[48:51]\n"
"v_exp_f32 v26, v26\n"
"v_mfma_f32_16x16x32_bf16 v[20:23], a[104:107], a[40:43], v[20:23]\n"
"ds_read_b64_tr_b16 a[174:175], v6 offset:38400\n"
"v_mfma_f32_16x16x32_bf16 v[20:23], a[100:103], a[36:39], v[20:23]\n"
"v_exp_f32 v27, v27\n"
"v_mfma_f32_16x16x32_bf16 v[20:23], a[96:99], a[32:35], v[20:23]\n"
"ds_read_b64_tr_b16 a[204:205], v6 offset:42496\n"
"v_cvt_pk_bf16_f32 v249, v26, v27\n"
"v_mfma_f32_16x16x32_bf16 v[60:63], a[208:211], v[36:39], v[60:63]\n"
"s_waitcnt lgkmcnt(11)\n"
"v_exp_f32 v28, v28\n"
"v_mfma_f32_16x16x32_bf16 v[96:99], a[192:195], v[36:39], v[96:99]\n"
"ds_read_b64_tr_b16 a[206:207], v6 offset:46592\n"
"v_mfma_f32_16x16x32_bf16 v[96:99], a[160:163], v[32:35], v[96:99]\n"
"s_waitcnt lgkmcnt(8)\n"
"v_exp_f32 v29, v29\n"
"v_mfma_f32_16x16x32_bf16 v[100:103], a[164:167], v[32:35], v[100:103]\n"
"v_cvt_pk_bf16_f32 v250, v28, v29\n"
"v_mfma_f32_16x16x32_bf16 v[100:103], a[196:199], v[36:39], v[100:103]\n"
"s_waitcnt lgkmcnt(4)\n"
"s_mov_b32 s95, 27648\n"
"s_add_u32 m0, s83, 16384\n"
"v_exp_f32 v30, v30\n"
"v_mfma_f32_16x16x32_bf16 v[104:107], a[200:203], v[36:39], v[104:107]\n"
"buffer_load_dwordx4 v9, s[56:59], s95 offen offset:0 sc1 lds\n"
"v_mfma_f32_16x16x32_bf16 v[104:107], a[168:171], v[32:35], v[104:107]\n"
"s_waitcnt lgkmcnt(0)\n"
"v_exp_f32 v31, v31\n"
"v_mfma_f32_16x16x32_bf16 v[108:111], a[172:175], v[32:35], v[108:111]\n"
"s_mov_b32 s95, 27648\n"
"v_cvt_pk_bf16_f32 v251, v30, v31\n"
"v_mfma_f32_16x16x32_bf16 v[108:111], a[204:207], v[36:39], v[108:111]\n"
"buffer_load_dwordx4 v9, s[56:59], s95 offen offset:1024 sc1 lds\n"
"v_mfma_f32_16x16x32_bf16 v[24:27], a[112:115], a[32:35], v[48:51]\n"
"v_exp_f32 v16, v16\n"
"v_mfma_f32_16x16x32_bf16 v[24:27], a[116:119], a[36:39], v[24:27]\n"
"v_mfma_f32_16x16x32_bf16 v[24:27], a[120:123], a[40:43], v[24:27]\n"
"s_mov_b32 s95, 27648\n"
"v_exp_f32 v17, v17\n"
"v_mfma_f32_16x16x32_bf16 v[24:27], a[124:127], a[44:47], v[24:27]\n"
"buffer_load_dwordx4 v9, s[56:59], s95 offen offset:2048 sc1 lds\n"
"v_cvt_pk_bf16_f32 v32, v16, v17\n"
"v_mfma_f32_16x16x32_bf16 v[28:31], a[140:143], a[44:47], v[48:51]\n"
"v_exp_f32 v18, v18\n"
"v_mfma_f32_16x16x32_bf16 v[28:31], a[136:139], a[40:43], v[28:31]\n"
"v_mfma_f32_16x16x32_bf16 v[28:31], a[132:135], a[36:39], v[28:31]\n"
"v_exp_f32 v19, v19\n"
"v_mfma_f32_16x16x32_bf16 v[28:31], a[128:131], a[32:35], v[28:31]\n"
"v_cvt_pk_bf16_f32 v33, v18, v19\n"
"v_mfma_f32_16x16x32_bf16 v[64:67], a[208:211], v[244:247], v[64:67]\n"
"s_mov_b32 s95, 27648\n"
"v_exp_f32 v20, v20\n"
"v_mfma_f32_16x16x32_bf16 v[112:115], a[144:147], v[244:247], v[112:115]\n"
"buffer_load_dwordx4 v9, s[56:59], s95 offen offset:3072 sc1 lds\n"
"v_mfma_f32_16x16x32_bf16 v[112:115], a[176:179], v[248:251], v[112:115]\n"
"v_exp_f32 v21, v21\n"
"v_mfma_f32_16x16x32_bf16 v[116:119], a[180:183], v[248:251], v[116:119]\n"
"v_cvt_pk_bf16_f32 v34, v20, v21\n"
"v_mfma_f32_16x16x32_bf16 v[116:119], a[148:151], v[244:247], v[116:119]\n"
"s_mov_b32 s95, 31744\n"
"s_add_u32 m0, s83, 81920\n"
"v_exp_f32 v22, v22\n"
"v_mfma_f32_16x16x32_bf16 v[120:123], a[152:155], v[244:247], v[120:123]\n"
"buffer_load_dwordx4 v9, s[56:59], s95 offen offset:0 sc1 lds\n"
"v_mfma_f32_16x16x32_bf16 v[120:123], a[184:187], v[248:251], v[120:123]\n"
"v_exp_f32 v23, v23\n"
"v_mfma_f32_16x16x32_bf16 v[124:127], a[188:191], v[248:251], v[124:127]\n"
"s_mov_b32 s95, 31744\n"
"v_cvt_pk_bf16_f32 v35, v22, v23\n"
"v_mfma_f32_16x16x32_bf16 v[124:127], a[156:159], v[244:247], v[124:127]\n"
"buffer_load_dwordx4 v9, s[56:59], s95 offen offset:1024 sc1 lds\n"
"v_mfma_f32_16x16x32_bf16 v[16:19], a[80:83], a[48:51], v[52:55]\n"
"s_mov_b32 s95, 77824\n"
"v_exp_f32 v24, v24\n"
"v_mfma_f32_16x16x32_bf16 v[16:19], a[84:87], a[52:55], v[16:19]\n"
"buffer_load_dwordx4 v9, s[56:59], s95 offen offset:2048 sc1 lds\n"
"v_mfma_f32_16x16x32_bf16 v[16:19], a[88:91], a[56:59], v[16:19]\n"
"v_exp_f32 v25, v25\n"
"v_mfma_f32_16x16x32_bf16 v[16:19], a[92:95], a[60:63], v[16:19]\n"
"v_cvt_pk_bf16_f32 v36, v24, v25\n"
"v_mfma_f32_16x16x32_bf16 v[20:23], a[108:111], a[60:63], v[52:55]\n"
"s_mov_b32 s95, 77824\n"
"v_exp_f32 v26, v26\n"
"v_mfma_f32_16x16x32_bf16 v[20:23], a[104:107], a[56:59], v[20:23]\n"
"buffer_load_dwordx4 v9, s[56:59], s95 offen offset:3072 sc1 lds\n"
"v_mfma_f32_16x16x32_bf16 v[20:23], a[100:103], a[52:55], v[20:23]\n"
"v_exp_f32 v27, v27\n"
"v_mfma_f32_16x16x32_bf16 v[20:23], a[96:99], a[48:51], v[20:23]\n"
"v_cvt_pk_bf16_f32 v37, v26, v27\n"
"v_mfma_f32_16x16x32_bf16 v[64:67], a[208:211], v[248:251], v[64:67]\n"
"v_exp_f32 v28, v28\n"
"v_mfma_f32_16x16x32_bf16 v[128:131], a[192:195], v[248:251], v[128:131]\n"
"v_mfma_f32_16x16x32_bf16 v[128:131], a[160:163], v[244:247], v[128:131]\n"
"s_mov_b32 s95, 4096\n"
"v_exp_f32 v29, v29\n"
"v_mfma_f32_16x16x32_bf16 v[132:135], a[164:167], v[244:247], v[132:135]\n"
"buffer_load_dwordx4 a[228:231], v9, s[56:59], s95 offen offset:0 sc1\n"
"v_cvt_pk_bf16_f32 v38, v28, v29\n"
"v_mfma_f32_16x16x32_bf16 v[132:135], a[196:199], v[248:251], v[132:135]\n"
"v_exp_f32 v30, v30\n"
"v_mfma_f32_16x16x32_bf16 v[136:139], a[200:203], v[248:251], v[136:139]\n"
"v_mfma_f32_16x16x32_bf16 v[136:139], a[168:171], v[244:247], v[136:139]\n"
"v_exp_f32 v31, v31\n"
"v_mfma_f32_16x16x32_bf16 v[140:143], a[172:175], v[244:247], v[140:143]\n"
"s_mov_b32 s95, 4096\n"
"v_cvt_pk_bf16_f32 v39, v30, v31\n"
"v_mfma_f32_16x16x32_bf16 v[140:143], a[204:207], v[248:251], v[140:143]\n"
"buffer_load_dwordx4 a[232:235], v9, s[56:59], s95 offen offset:1024 sc1\n"
"v_mfma_f32_16x16x32_bf16 v[24:27], a[112:115], a[48:51], v[52:55]\n"
"v_exp_f32 v16, v16\n"
"v_mfma_f32_16x16x32_bf16 v[24:27], a[116:119], a[52:55], v[24:27]\n"
"v_mfma_f32_16x16x32_bf16 v[24:27], a[120:123], a[56:59], v[24:27]\n"
"s_mov_b32 s95, 6144\n"
"v_exp_f32 v17, v17\n"
"v_mfma_f32_16x16x32_bf16 v[24:27], a[124:127], a[60:63], v[24:27]\n"
"buffer_load_dwordx4 a[236:239], v9, s[56:59], s95 offen offset:0 sc1\n"
"v_cvt_pk_bf16_f32 v244, v16, v17\n"
"v_mfma_f32_16x16x32_bf16 v[28:31], a[140:143], a[60:63], v[52:55]\n"
"v_exp_f32 v18, v18\n"
"v_mfma_f32_16x16x32_bf16 v[28:31], a[136:139], a[56:59], v[28:31]\n"
"v_mfma_f32_16x16x32_bf16 v[28:31], a[132:135], a[52:55], v[28:31]\n"
"v_exp_f32 v19, v19\n"
"v_mfma_f32_16x16x32_bf16 v[28:31], a[128:131], a[48:51], v[28:31]\n"
"v_cvt_pk_bf16_f32 v245, v18, v19\n"
"v_mfma_f32_16x16x32_bf16 v[68:71], a[208:211], v[32:35], v[68:71]\n"
"s_mov_b32 s95, 6144\n"
"v_exp_f32 v20, v20\n"
"v_mfma_f32_16x16x32_bf16 v[144:147], a[144:147], v[32:35], v[144:147]\n"
"buffer_load_dwordx4 a[240:243], v9, s[56:59], s95 offen offset:1024 sc1\n"
"v_mfma_f32_16x16x32_bf16 v[144:147], a[176:179], v[36:39], v[144:147]\n"
"v_exp_f32 v21, v21\n"
"v_mfma_f32_16x16x32_bf16 v[148:151], a[180:183], v[36:39], v[148:151]\n"
"v_cvt_pk_bf16_f32 v246, v20, v21\n"
"v_mfma_f32_16x16x32_bf16 v[148:151], a[148:151], v[32:35], v[148:151]\n"
"v_exp_f32 v22, v22\n"
"v_mfma_f32_16x16x32_bf16 v[152:155], a[152:155], v[32:35], v[152:155]\n"
"v_mfma_f32_16x16x32_bf16 v[152:155], a[184:187], v[36:39], v[152:155]\n"
"v_exp_f32 v23, v23\n"
"v_mfma_f32_16x16x32_bf16 v[156:159], a[188:191], v[36:39], v[156:159]\n"
"v_cvt_pk_bf16_f32 v247, v22, v23\n"
"v_mfma_f32_16x16x32_bf16 v[156:159], a[156:159], v[32:35], v[156:159]\n"
"buffer_load_dword v253, v252, s[4:7], s9 offen\n"
"v_mfma_f32_16x16x32_bf16 v[16:19], a[80:83], a[64:67], v[56:59]\n"
"s_waitcnt vmcnt(33) lgkmcnt(0)\n"
"s_barrier\n"
"v_exp_f32 v24, v24\n"
"v_mfma_f32_16x16x32_bf16 v[16:19], a[84:87], a[68:71], v[16:19]\n"
"ds_read_b128 a[80:83], v1 offset:49152\n"
"v_mfma_f32_16x16x32_bf16 v[16:19], a[88:91], a[72:75], v[16:19]\n"
"v_exp_f32 v25, v25\n"
"v_mfma_f32_16x16x32_bf16 v[16:19], a[92:95], a[76:79], v[16:19]\n"
"ds_read_b128 a[84:87], v2 offset:49152\n"
"v_cvt_pk_bf16_f32 v248, v24, v25\n"
"v_mfma_f32_16x16x32_bf16 v[20:23], a[108:111], a[76:79], v[56:59]\n"
"v_exp_f32 v26, v26\n"
"v_mfma_f32_16x16x32_bf16 v[20:23], a[104:107], a[72:75], v[20:23]\n"
"ds_read_b128 a[88:91], v3 offset:49152\n"
"v_mfma_f32_16x16x32_bf16 v[20:23], a[100:103], a[68:71], v[20:23]\n"
"v_exp_f32 v27, v27\n"
"v_mfma_f32_16x16x32_bf16 v[20:23], a[96:99], a[64:67], v[20:23]\n"
"ds_read_b128 a[92:95], v4 offset:49152\n"
"v_cvt_pk_bf16_f32 v249, v26, v27\n"
"v_mfma_f32_16x16x32_bf16 v[68:71], a[208:211], v[36:39], v[68:71]\n"
"v_exp_f32 v28, v28\n"
"v_mfma_f32_16x16x32_bf16 v[160:163], a[192:195], v[36:39], v[160:163]\n"
"ds_read_b128 a[108:111], v4 offset:53248\n"
"v_mfma_f32_16x16x32_bf16 v[160:163], a[160:163], v[32:35], v[160:163]\n"
"v_exp_f32 v29, v29\n"
"v_mfma_f32_16x16x32_bf16 v[164:167], a[164:167], v[32:35], v[164:167]\n"
"ds_read_b128 a[104:107], v3 offset:53248\n"
"v_cvt_pk_bf16_f32 v250, v28, v29\n"
"v_mfma_f32_16x16x32_bf16 v[164:167], a[196:199], v[36:39], v[164:167]\n"
"v_exp_f32 v30, v30\n"
"v_mfma_f32_16x16x32_bf16 v[168:171], a[200:203], v[36:39], v[168:171]\n"
"ds_read_b128 a[100:103], v2 offset:53248\n"
"v_mfma_f32_16x16x32_bf16 v[168:171], a[168:171], v[32:35], v[168:171]\n"
"v_exp_f32 v31, v31\n"
"v_mfma_f32_16x16x32_bf16 v[172:175], a[172:175], v[32:35], v[172:175]\n"
"s_mov_b32 s95, 81920\n"
"s_add_u32 m0, s83, 32768\n"
"ds_read_b128 a[96:99], v1 offset:53248\n"
"v_cvt_pk_bf16_f32 v251, v30, v31\n"
"v_mfma_f32_16x16x32_bf16 v[172:175], a[204:207], v[36:39], v[172:175]\n"
"buffer_load_dwordx4 v9, s[56:59], s95 offen offset:0 sc1 lds\n"
"v_mfma_f32_16x16x32_bf16 v[24:27], a[112:115], a[64:67], v[56:59]\n"
"v_exp_f32 v16, v16\n"
"v_mfma_f32_16x16x32_bf16 v[24:27], a[116:119], a[68:71], v[24:27]\n"
"ds_read_b128 a[112:115], v1 offset:57344\n"
"v_mfma_f32_16x16x32_bf16 v[24:27], a[120:123], a[72:75], v[24:27]\n"
"v_exp_f32 v17, v17\n"
"v_mfma_f32_16x16x32_bf16 v[24:27], a[124:127], a[76:79], v[24:27]\n"
"ds_read_b128 a[116:119], v2 offset:57344\n"
"v_cvt_pk_bf16_f32 v32, v16, v17\n"
"v_mfma_f32_16x16x32_bf16 v[28:31], a[140:143], a[76:79], v[56:59]\n"
"v_exp_f32 v18, v18\n"
"v_mfma_f32_16x16x32_bf16 v[28:31], a[136:139], a[72:75], v[28:31]\n"
"ds_read_b128 a[120:123], v3 offset:57344\n"
"v_mfma_f32_16x16x32_bf16 v[28:31], a[132:135], a[68:71], v[28:31]\n"
"v_exp_f32 v19, v19\n"
"v_mfma_f32_16x16x32_bf16 v[28:31], a[128:131], a[64:67], v[28:31]\n"
"ds_read_b128 a[124:127], v4 offset:57344\n"
"v_cvt_pk_bf16_f32 v33, v18, v19\n"
"v_mfma_f32_16x16x32_bf16 v[72:75], a[208:211], v[244:247], v[72:75]\n"
"v_exp_f32 v20, v20\n"
"v_mfma_f32_16x16x32_bf16 v[176:179], a[144:147], v[244:247], v[176:179]\n"
"ds_read_b128 a[140:143], v4 offset:61440\n"
"v_mfma_f32_16x16x32_bf16 v[176:179], a[176:179], v[248:251], v[176:179]\n"
"v_exp_f32 v21, v21\n"
"v_mfma_f32_16x16x32_bf16 v[180:183], a[180:183], v[248:251], v[180:183]\n"
"ds_read_b128 a[136:139], v3 offset:61440\n"
"v_cvt_pk_bf16_f32 v34, v20, v21\n"
"v_mfma_f32_16x16x32_bf16 v[180:183], a[148:151], v[244:247], v[180:183]\n"
"v_exp_f32 v22, v22\n"
"v_mfma_f32_16x16x32_bf16 v[184:187], a[152:155], v[244:247], v[184:187]\n"
"ds_read_b128 a[132:135], v2 offset:61440\n"
"v_mfma_f32_16x16x32_bf16 v[184:187], a[184:187], v[248:251], v[184:187]\n"
"v_exp_f32 v23, v23\n"
"v_mfma_f32_16x16x32_bf16 v[188:191], a[188:191], v[248:251], v[188:191]\n"
"s_mov_b32 s95, 81920\n"
"ds_read_b128 a[128:131], v1 offset:61440\n"
"v_cvt_pk_bf16_f32 v35, v22, v23\n"
"v_mfma_f32_16x16x32_bf16 v[188:191], a[156:159], v[244:247], v[188:191]\n"
"s_waitcnt lgkmcnt(8)\n"
"buffer_load_dwordx4 v9, s[56:59], s95 offen offset:1024 sc1 lds\n"
"v_mfma_f32_16x16x32_bf16 v[16:19], a[80:83], a[0:3], v[40:43]\n"
"v_exp_f32 v24, v24\n"
"v_mfma_f32_16x16x32_bf16 v[16:19], a[84:87], a[4:7], v[16:19]\n"
"v_mfma_f32_16x16x32_bf16 v[16:19], a[88:91], a[8:11], v[16:19]\n"
"v_exp_f32 v25, v25\n"
"v_mfma_f32_16x16x32_bf16 v[16:19], a[92:95], a[12:15], v[16:19]\n"
"v_cvt_pk_bf16_f32 v36, v24, v25\n"
"v_mfma_f32_16x16x32_bf16 v[20:23], a[108:111], a[12:15], v[40:43]\n"
"s_mov_b32 s95, 81920\n"
"v_exp_f32 v26, v26\n"
"v_mfma_f32_16x16x32_bf16 v[20:23], a[104:107], a[8:11], v[20:23]\n"
"buffer_load_dwordx4 v9, s[56:59], s95 offen offset:2048 sc1 lds\n"
"v_mfma_f32_16x16x32_bf16 v[20:23], a[100:103], a[4:7], v[20:23]\n"
"v_exp_f32 v27, v27\n"
"v_mfma_f32_16x16x32_bf16 v[20:23], a[96:99], a[0:3], v[20:23]\n"
"v_cvt_pk_bf16_f32 v37, v26, v27\n"
"v_mfma_f32_16x16x32_bf16 v[72:75], a[208:211], v[248:251], v[72:75]\n"
"v_exp_f32 v28, v28\n"
"v_mfma_f32_16x16x32_bf16 v[192:195], a[192:195], v[248:251], v[192:195]\n"
"v_mfma_f32_16x16x32_bf16 v[192:195], a[160:163], v[244:247], v[192:195]\n"
"v_exp_f32 v29, v29\n"
"v_mfma_f32_16x16x32_bf16 v[196:199], a[164:167], v[244:247], v[196:199]\n"
"v_cvt_pk_bf16_f32 v38, v28, v29\n"
"v_mfma_f32_16x16x32_bf16 v[196:199], a[196:199], v[248:251], v[196:199]\n"
"s_mov_b32 s95, 81920\n"
"v_exp_f32 v30, v30\n"
"v_mfma_f32_16x16x32_bf16 v[200:203], a[200:203], v[248:251], v[200:203]\n"
"buffer_load_dwordx4 v9, s[56:59], s95 offen offset:3072 sc1 lds\n"
"v_mfma_f32_16x16x32_bf16 v[200:203], a[168:171], v[244:247], v[200:203]\n"
"v_exp_f32 v31, v31\n"
"v_mfma_f32_16x16x32_bf16 v[204:207], a[172:175], v[244:247], v[204:207]\n"
"s_mov_b32 s95, 6144\n"
"v_cvt_pk_bf16_f32 v39, v30, v31\n"
"v_mfma_f32_16x16x32_bf16 v[204:207], a[204:207], v[248:251], v[204:207]\n"
"s_waitcnt lgkmcnt(0)\n"
"buffer_load_dwordx4 a[244:247], v9, s[56:59], s95 offen offset:2048 sc1\n"
"v_mfma_f32_16x16x32_bf16 v[24:27], a[112:115], a[0:3], v[40:43]\n"
"v_exp_f32 v16, v16\n"
"v_mfma_f32_16x16x32_bf16 v[24:27], a[116:119], a[4:7], v[24:27]\n"
"v_mfma_f32_16x16x32_bf16 v[24:27], a[120:123], a[8:11], v[24:27]\n"
"v_exp_f32 v17, v17\n"
"v_mfma_f32_16x16x32_bf16 v[24:27], a[124:127], a[12:15], v[24:27]\n"
"v_cvt_pk_bf16_f32 v244, v16, v17\n"
"v_mfma_f32_16x16x32_bf16 v[28:31], a[140:143], a[12:15], v[40:43]\n"
"s_mov_b32 s95, 6144\n"
"v_exp_f32 v18, v18\n"
"v_mfma_f32_16x16x32_bf16 v[28:31], a[136:139], a[8:11], v[28:31]\n"
"buffer_load_dwordx4 a[248:251], v9, s[56:59], s95 offen offset:3072 sc1\n"
"v_mfma_f32_16x16x32_bf16 v[28:31], a[132:135], a[4:7], v[28:31]\n"
"v_exp_f32 v19, v19\n"
"v_mfma_f32_16x16x32_bf16 v[28:31], a[128:131], a[0:3], v[28:31]\n"
"v_cvt_pk_bf16_f32 v245, v18, v19\n"
"v_mfma_f32_16x16x32_bf16 v[76:79], a[208:211], v[32:35], v[76:79]\n"
"v_exp_f32 v20, v20\n"
"v_mfma_f32_16x16x32_bf16 v[208:211], a[144:147], v[32:35], v[208:211]\n"
"v_mfma_f32_16x16x32_bf16 v[208:211], a[176:179], v[36:39], v[208:211]\n"
"v_exp_f32 v21, v21\n"
"v_mfma_f32_16x16x32_bf16 v[212:215], a[180:183], v[36:39], v[212:215]\n"
"ds_read_b64_tr_b16 a[144:145], v5 offset:49152\n"
"v_cvt_pk_bf16_f32 v246, v20, v21\n"
"v_mfma_f32_16x16x32_bf16 v[212:215], a[148:151], v[32:35], v[212:215]\n"
"v_exp_f32 v22, v22\n"
"v_mfma_f32_16x16x32_bf16 v[216:219], a[152:155], v[32:35], v[216:219]\n"
"ds_read_b64_tr_b16 a[146:147], v5 offset:53248\n"
"v_mfma_f32_16x16x32_bf16 v[216:219], a[184:187], v[36:39], v[216:219]\n"
"v_exp_f32 v23, v23\n"
"v_mfma_f32_16x16x32_bf16 v[220:223], a[188:191], v[36:39], v[220:223]\n"
"ds_read_b64_tr_b16 a[176:177], v5 offset:57344\n"
"v_cvt_pk_bf16_f32 v247, v22, v23\n"
"v_mfma_f32_16x16x32_bf16 v[220:223], a[156:159], v[32:35], v[220:223]\n"
"ds_read_b64_tr_b16 a[178:179], v5 offset:61440\n"
"v_mfma_f32_16x16x32_bf16 v[16:19], a[80:83], a[16:19], v[44:47]\n"
"v_exp_f32 v24, v24\n"
"v_mfma_f32_16x16x32_bf16 v[16:19], a[84:87], a[20:23], v[16:19]\n"
"ds_read_b64_tr_b16 a[148:149], v6 offset:49152\n"
"v_mfma_f32_16x16x32_bf16 v[16:19], a[88:91], a[24:27], v[16:19]\n"
"ds_read_b64_tr_b16 a[150:151], v6 offset:53248\n"
"v_exp_f32 v25, v25\n"
"v_mfma_f32_16x16x32_bf16 v[16:19], a[92:95], a[28:31], v[16:19]\n"
"ds_read_b64_tr_b16 a[180:181], v6 offset:57344\n"
"v_cvt_pk_bf16_f32 v248, v24, v25\n"
"v_mfma_f32_16x16x32_bf16 v[20:23], a[108:111], a[28:31], v[44:47]\n"
"v_exp_f32 v26, v26\n"
"v_mfma_f32_16x16x32_bf16 v[20:23], a[104:107], a[24:27], v[20:23]\n"
"ds_read_b64_tr_b16 a[182:183], v6 offset:61440\n"
"v_mfma_f32_16x16x32_bf16 v[20:23], a[100:103], a[20:23], v[20:23]\n"
"v_exp_f32 v27, v27\n"
"v_mfma_f32_16x16x32_bf16 v[20:23], a[96:99], a[16:19], v[20:23]\n"
"ds_read_b64_tr_b16 a[152:153], v5 offset:49664\n"
"v_cvt_pk_bf16_f32 v249, v26, v27\n"
"v_mfma_f32_16x16x32_bf16 v[76:79], a[208:211], v[36:39], v[76:79]\n"
"ds_read_b64_tr_b16 a[154:155], v5 offset:53760\n"
"v_exp_f32 v28, v28\n"
"v_mfma_f32_16x16x32_bf16 v[224:227], a[192:195], v[36:39], v[224:227]\n"
"ds_read_b64_tr_b16 a[184:185], v5 offset:57856\n"
"v_mfma_f32_16x16x32_bf16 v[224:227], a[160:163], v[32:35], v[224:227]\n"
"v_exp_f32 v29, v29\n"
"v_mfma_f32_16x16x32_bf16 v[228:231], a[164:167], v[32:35], v[228:231]\n"
"ds_read_b64_tr_b16 a[186:187], v5 offset:61952\n"
"v_cvt_pk_bf16_f32 v250, v28, v29\n"
"v_mfma_f32_16x16x32_bf16 v[228:231], a[196:199], v[36:39], v[228:231]\n"
"v_exp_f32 v30, v30\n"
"v_mfma_f32_16x16x32_bf16 v[232:235], a[200:203], v[36:39], v[232:235]\n"
"ds_read_b64_tr_b16 a[156:157], v6 offset:49664\n"
"v_mfma_f32_16x16x32_bf16 v[232:235], a[168:171], v[32:35], v[232:235]\n"
"ds_read_b64_tr_b16 a[158:159], v6 offset:53760\n"
"v_exp_f32 v31, v31\n"
"v_mfma_f32_16x16x32_bf16 v[236:239], a[172:175], v[32:35], v[236:239]\n"
"ds_read_b64_tr_b16 a[188:189], v6 offset:57856\n"
"v_cvt_pk_bf16_f32 v251, v30, v31\n"
"v_mfma_f32_16x16x32_bf16 v[236:239], a[204:207], v[36:39], v[236:239]\n"
"ds_read_b64_tr_b16 a[190:191], v6 offset:61952\n"
"v_mfma_f32_16x16x32_bf16 v[24:27], a[112:115], a[16:19], v[44:47]\n"
"v_exp_f32 v16, v16\n"
"v_mfma_f32_16x16x32_bf16 v[24:27], a[116:119], a[20:23], v[24:27]\n"
"ds_read_b64_tr_b16 a[160:161], v5 offset:50176\n"
"v_mfma_f32_16x16x32_bf16 v[24:27], a[120:123], a[24:27], v[24:27]\n"
"ds_read_b64_tr_b16 a[162:163], v5 offset:54272\n"
"v_exp_f32 v17, v17\n"
"v_mfma_f32_16x16x32_bf16 v[24:27], a[124:127], a[28:31], v[24:27]\n"
"ds_read_b64_tr_b16 a[192:193], v5 offset:58368\n"
"v_cvt_pk_bf16_f32 v32, v16, v17\n"
"v_mfma_f32_16x16x32_bf16 v[28:31], a[140:143], a[28:31], v[44:47]\n"
"v_exp_f32 v18, v18\n"
"v_mfma_f32_16x16x32_bf16 v[28:31], a[136:139], a[24:27], v[28:31]\n"
"ds_read_b64_tr_b16 a[194:195], v5 offset:62464\n"
"v_mfma_f32_16x16x32_bf16 v[28:31], a[132:135], a[20:23], v[28:31]\n"
"v_exp_f32 v19, v19\n"
"v_mfma_f32_16x16x32_bf16 v[28:31], a[128:131], a[16:19], v[28:31]\n"
"ds_read_b64_tr_b16 a[164:165], v6 offset:50176\n"
"v_cvt_pk_bf16_f32 v33, v18, v19\n"
"v_mfma_f32_16x16x32_bf16 v[60:63], a[208:211], v[244:247], v[60:63]\n"
"s_waitcnt lgkmcnt(15)\n"
"ds_read_b64_tr_b16 a[166:167], v6 offset:54272\n"
"v_exp_f32 v20, v20\n"
"v_mfma_f32_16x16x32_bf16 v[80:83], a[144:147], v[244:247], v[80:83]\n"
"ds_read_b64_tr_b16 a[196:197], v6 offset:58368\n"
"v_mfma_f32_16x16x32_bf16 v[80:83], a[176:179], v[248:251], v[80:83]\n"
"s_waitcnt lgkmcnt(15)\n"
"v_exp_f32 v21, v21\n"
"v_mfma_f32_16x16x32_bf16 v[84:87], a[180:183], v[248:251], v[84:87]\n"
"ds_read_b64_tr_b16 a[198:199], v6 offset:62464\n"
"v_cvt_pk_bf16_f32 v34, v20, v21\n"
"v_mfma_f32_16x16x32_bf16 v[84:87], a[148:151], v[244:247], v[84:87]\n"
"s_waitcnt lgkmcnt(12)\n"
"v_exp_f32 v22, v22\n"
"v_mfma_f32_16x16x32_bf16 v[88:91], a[152:155], v[244:247], v[88:91]\n"
"ds_read_b64_tr_b16 a[168:169], v5 offset:50688\n"
"v_mfma_f32_16x16x32_bf16 v[88:91], a[184:187], v[248:251], v[88:91]\n"
"s_waitcnt lgkmcnt(9)\n"
"v_exp_f32 v23, v23\n"
"v_mfma_f32_16x16x32_bf16 v[92:95], a[188:191], v[248:251], v[92:95]\n"
"ds_read_b64_tr_b16 a[170:171], v5 offset:54784\n"
"v_cvt_pk_bf16_f32 v35, v22, v23\n"
"v_mfma_f32_16x16x32_bf16 v[92:95], a[156:159], v[244:247], v[92:95]\n"
"ds_read_b64_tr_b16 a[200:201], v5 offset:58880\n"
"v_mfma_f32_16x16x32_bf16 v[16:19], a[80:83], a[32:35], v[48:51]\n"
"v_exp_f32 v24, v24\n"
"v_mfma_f32_16x16x32_bf16 v[16:19], a[84:87], a[36:39], v[16:19]\n"
"ds_read_b64_tr_b16 a[202:203], v5 offset:62976\n"
"v_mfma_f32_16x16x32_bf16 v[16:19], a[88:91], a[40:43], v[16:19]\n"
"v_exp_f32 v25, v25\n"
"v_mfma_f32_16x16x32_bf16 v[16:19], a[92:95], a[44:47], v[16:19]\n"
"ds_read_b64_tr_b16 a[172:173], v6 offset:50688\n"
"v_cvt_pk_bf16_f32 v36, v24, v25\n"
"v_mfma_f32_16x16x32_bf16 v[20:23], a[108:111], a[44:47], v[48:51]\n"
"v_exp_f32 v26, v26\n"
"v_mfma_f32_16x16x32_bf16 v[20:23], a[104:107], a[40:43], v[20:23]\n"
"ds_read_b64_tr_b16 a[174:175], v6 offset:54784\n"
"v_mfma_f32_16x16x32_bf16 v[20:23], a[100:103], a[36:39], v[20:23]\n"
"v_exp_f32 v27, v27\n"
"v_mfma_f32_16x16x32_bf16 v[20:23], a[96:99], a[32:35], v[20:23]\n"
"ds_read_b64_tr_b16 a[204:205], v6 offset:58880\n"
"v_cvt_pk_bf16_f32 v37, v26, v27\n"
"v_mfma_f32_16x16x32_bf16 v[60:63], a[208:211], v[248:251], v[60:63]\n"
"s_waitcnt lgkmcnt(11)\n"
"v_exp_f32 v28, v28\n"
"v_mfma_f32_16x16x32_bf16 v[96:99], a[192:195], v[248:251], v[96:99]\n"
"ds_read_b64_tr_b16 a[206:207], v6 offset:62976\n"
"v_mfma_f32_16x16x32_bf16 v[96:99], a[160:163], v[244:247], v[96:99]\n"
"s_waitcnt lgkmcnt(8)\n"
"v_exp_f32 v29, v29\n"
"v_mfma_f32_16x16x32_bf16 v[100:103], a[164:167], v[244:247], v[100:103]\n"
"v_cvt_pk_bf16_f32 v38, v28, v29\n"
"v_mfma_f32_16x16x32_bf16 v[100:103], a[196:199], v[248:251], v[100:103]\n"
"s_waitcnt lgkmcnt(4)\n"
"s_mov_b32 s95, 10240\n"
"v_exp_f32 v30, v30\n"
"v_mfma_f32_16x16x32_bf16 v[104:107], a[200:203], v[248:251], v[104:107]\n"
"buffer_load_dwordx4 a[252:255], v9, s[56:59], s95 offen offset:0 sc1\n"
"v_mfma_f32_16x16x32_bf16 v[104:107], a[168:171], v[244:247], v[104:107]\n"
"s_waitcnt lgkmcnt(0)\n"
"v_exp_f32 v31, v31\n"
"v_mfma_f32_16x16x32_bf16 v[108:111], a[172:175], v[244:247], v[108:111]\n"
"v_cvt_pk_bf16_f32 v39, v30, v31\n"
"v_mfma_f32_16x16x32_bf16 v[108:111], a[204:207], v[248:251], v[108:111]\n"
"v_mfma_f32_16x16x32_bf16 v[24:27], a[112:115], a[32:35], v[48:51]\n"
"s_waitcnt vmcnt(7) lgkmcnt(0)\n"
"s_barrier\n"
"s_mov_b32 s96, 0\n"
"v_exp_f32 v16, v16\n"
"v_mfma_f32_16x16x32_bf16 v[24:27], a[116:119], a[36:39], v[24:27]\n"
"v_rcp_f32 v44, v60\n"
"v_cmp_nle_f32 vcc, v60, v11\n"
"v_mul_f32 v80, v80, v44\n"
"v_mfma_f32_16x16x32_bf16 v[24:27], a[120:123], a[40:43], v[24:27]\n"
"s_or_b64 s[98:99], s[98:99], vcc\n"
"v_exp_f32 v17, v17\n"
"v_mfma_f32_16x16x32_bf16 v[24:27], a[124:127], a[44:47], v[24:27]\n"
"v_cvt_pk_bf16_f32 v244, v16, v17\n"
"v_mul_f32 v81, v81, v44\n"
"v_mul_f32 v82, v82, v44\n"
"v_mul_f32 v83, v83, v44\n"
"v_mfma_f32_16x16x32_bf16 v[28:31], a[140:143], a[44:47], v[48:51]\n"
"v_exp_f32 v18, v18\n"
"v_mfma_f32_16x16x32_bf16 v[28:31], a[136:139], a[40:43], v[28:31]\n"
"v_mul_f32 v84, v84, v44\n"
"v_mul_f32 v85, v85, v44\n"
"v_mul_f32 v86, v86, v44\n"
"v_mfma_f32_16x16x32_bf16 v[28:31], a[132:135], a[36:39], v[28:31]\n"
"v_exp_f32 v19, v19\n"
"v_mfma_f32_16x16x32_bf16 v[28:31], a[128:131], a[32:35], v[28:31]\n"
"v_cvt_pk_bf16_f32 v245, v18, v19\n"
"v_mul_f32 v87, v87, v44\n"
"v_cvt_pk_bf16_f32 v240, v80, v81\n"
"v_cvt_pk_bf16_f32 v241, v82, v83\n"
"v_mfma_f32_16x16x32_bf16 v[64:67], a[208:211], v[32:35], v[64:67]\n"
"v_exp_f32 v20, v20\n"
"v_mfma_f32_16x16x32_bf16 v[112:115], a[144:147], v[32:35], v[112:115]\n"
"v_cvt_pk_bf16_f32 v242, v84, v85\n"
"v_cvt_pk_bf16_f32 v243, v86, v87\n"
"v_mul_f32 v88, v88, v44\n"
"v_mfma_f32_16x16x32_bf16 v[112:115], a[176:179], v[36:39], v[112:115]\n"
"v_exp_f32 v21, v21\n"
"v_mfma_f32_16x16x32_bf16 v[116:119], a[180:183], v[36:39], v[116:119]\n"
"buffer_store_dwordx4 v[240:243], v9, s[52:55], s96 offen offset:0 sc1\n"
"v_cvt_pk_bf16_f32 v246, v20, v21\n"
"v_mul_f32 v89, v89, v44\n"
"v_mul_f32 v90, v90, v44\n"
"v_mfma_f32_16x16x32_bf16 v[116:119], a[148:151], v[32:35], v[116:119]\n"
"v_exp_f32 v22, v22\n"
"v_mfma_f32_16x16x32_bf16 v[120:123], a[152:155], v[32:35], v[120:123]\n"
"v_mul_f32 v91, v91, v44\n"
"v_mul_f32 v92, v92, v44\n"
"v_mul_f32 v93, v93, v44\n"
"v_mfma_f32_16x16x32_bf16 v[120:123], a[184:187], v[36:39], v[120:123]\n"
"v_exp_f32 v23, v23\n"
"v_mfma_f32_16x16x32_bf16 v[124:127], a[188:191], v[36:39], v[124:127]\n"
"v_cvt_pk_bf16_f32 v247, v22, v23\n"
"v_mul_f32 v94, v94, v44\n"
"v_mul_f32 v95, v95, v44\n"
"v_cvt_pk_bf16_f32 v252, v88, v89\n"
"v_mfma_f32_16x16x32_bf16 v[124:127], a[156:159], v[32:35], v[124:127]\n"
"v_cvt_pk_bf16_f32 v253, v90, v91\n"
"v_cvt_pk_bf16_f32 v254, v92, v93\n"
"v_cvt_pk_bf16_f32 v255, v94, v95\n"
"v_mfma_f32_16x16x32_bf16 v[16:19], a[80:83], a[48:51], v[52:55]\n"
"v_exp_f32 v24, v24\n"
"v_mfma_f32_16x16x32_bf16 v[16:19], a[84:87], a[52:55], v[16:19]\n"
"v_mul_f32 v96, v96, v44\n"
"v_mul_f32 v97, v97, v44\n"
"v_mul_f32 v98, v98, v44\n"
"v_mfma_f32_16x16x32_bf16 v[16:19], a[88:91], a[56:59], v[16:19]\n"
"v_exp_f32 v25, v25\n"
"v_mfma_f32_16x16x32_bf16 v[16:19], a[92:95], a[60:63], v[16:19]\n"
"buffer_store_dwordx4 v[252:255], v9, s[52:55], s96 offen offset:1024 sc1\n"
"v_cvt_pk_bf16_f32 v248, v24, v25\n"
"v_mul_f32 v99, v99, v44\n"
"v_mul_f32 v100, v100, v44\n"
"v_mfma_f32_16x16x32_bf16 v[20:23], a[108:111], a[60:63], v[52:55]\n"
"v_exp_f32 v26, v26\n"
"v_mfma_f32_16x16x32_bf16 v[20:23], a[104:107], a[56:59], v[20:23]\n"
"v_mul_f32 v101, v101, v44\n"
"v_mul_f32 v102, v102, v44\n"
"v_mul_f32 v103, v103, v44\n"
"v_mfma_f32_16x16x32_bf16 v[20:23], a[100:103], a[52:55], v[20:23]\n"
"v_exp_f32 v27, v27\n"
"v_mfma_f32_16x16x32_bf16 v[20:23], a[96:99], a[48:51], v[20:23]\n"
"v_cvt_pk_bf16_f32 v249, v26, v27\n"
"v_cvt_pk_bf16_f32 v40, v96, v97\n"
"v_cvt_pk_bf16_f32 v41, v98, v99\n"
"v_cvt_pk_bf16_f32 v42, v100, v101\n"
"v_mfma_f32_16x16x32_bf16 v[64:67], a[208:211], v[36:39], v[64:67]\n"
"v_exp_f32 v28, v28\n"
"v_mfma_f32_16x16x32_bf16 v[128:131], a[192:195], v[36:39], v[128:131]\n"
"v_cvt_pk_bf16_f32 v43, v102, v103\n"
"v_mul_f32 v104, v104, v44\n"
"v_mul_f32 v105, v105, v44\n"
"v_mfma_f32_16x16x32_bf16 v[128:131], a[160:163], v[32:35], v[128:131]\n"
"v_exp_f32 v29, v29\n"
"v_mfma_f32_16x16x32_bf16 v[132:135], a[164:167], v[32:35], v[132:135]\n"
"buffer_store_dwordx4 v[40:43], v9, s[52:55], s96 offen offset:2048 sc1\n"
"v_cvt_pk_bf16_f32 v250, v28, v29\n"
"v_mul_f32 v106, v106, v44\n"
"v_mul_f32 v107, v107, v44\n"
"v_mfma_f32_16x16x32_bf16 v[132:135], a[196:199], v[36:39], v[132:135]\n"
"v_exp_f32 v30, v30\n"
"v_mfma_f32_16x16x32_bf16 v[136:139], a[200:203], v[36:39], v[136:139]\n"
"v_mul_f32 v108, v108, v44\n"
"v_mul_f32 v109, v109, v44\n"
"v_mul_f32 v110, v110, v44\n"
"v_mfma_f32_16x16x32_bf16 v[136:139], a[168:171], v[32:35], v[136:139]\n"
"v_exp_f32 v31, v31\n"
"v_mfma_f32_16x16x32_bf16 v[140:143], a[172:175], v[32:35], v[140:143]\n"
"v_cvt_pk_bf16_f32 v251, v30, v31\n"
"v_mul_f32 v111, v111, v44\n"
"v_cvt_pk_bf16_f32 v240, v104, v105\n"
"v_cvt_pk_bf16_f32 v241, v106, v107\n"
"v_mfma_f32_16x16x32_bf16 v[140:143], a[204:207], v[36:39], v[140:143]\n"
"v_cvt_pk_bf16_f32 v242, v108, v109\n"
"v_cvt_pk_bf16_f32 v243, v110, v111\n"
"v_mfma_f32_16x16x32_bf16 v[24:27], a[112:115], a[48:51], v[52:55]\n"
"s_mov_b32 s97, 131072\n"
"v_exp_f32 v16, v16\n"
"v_mfma_f32_16x16x32_bf16 v[24:27], a[116:119], a[52:55], v[24:27]\n"
"buffer_store_dwordx4 v[240:243], v9, s[52:55], s96 offen offset:3072 sc1\n"
"v_rcp_f32 v46, v64\n"
"v_cmp_nle_f32 vcc, v64, v11\n"
"v_mul_f32 v112, v112, v46\n"
"v_mfma_f32_16x16x32_bf16 v[24:27], a[120:123], a[56:59], v[24:27]\n"
"s_or_b64 s[98:99], s[98:99], vcc\n"
"v_exp_f32 v17, v17\n"
"v_mfma_f32_16x16x32_bf16 v[24:27], a[124:127], a[60:63], v[24:27]\n"
"v_cvt_pk_bf16_f32 v32, v16, v17\n"
"v_mul_f32 v113, v113, v46\n"
"v_mul_f32 v114, v114, v46\n"
"v_mfma_f32_16x16x32_bf16 v[28:31], a[140:143], a[60:63], v[52:55]\n"
"v_exp_f32 v18, v18\n"
"v_mfma_f32_16x16x32_bf16 v[28:31], a[136:139], a[56:59], v[28:31]\n"
"v_mul_f32 v115, v115, v46\n"
"v_mul_f32 v116, v116, v46\n"
"v_mul_f32 v117, v117, v46\n"
"v_mul_f32 v118, v118, v46\n"
"v_mfma_f32_16x16x32_bf16 v[28:31], a[132:135], a[52:55], v[28:31]\n"
"v_exp_f32 v19, v19\n"
"v_mfma_f32_16x16x32_bf16 v[28:31], a[128:131], a[48:51], v[28:31]\n"
"v_cvt_pk_bf16_f32 v33, v18, v19\n"
"v_mul_f32 v119, v119, v46\n"
"v_cvt_pk_bf16_f32 v252, v112, v113\n"
"v_mfma_f32_16x16x32_bf16 v[68:71], a[208:211], v[244:247], v[68:71]\n"
"v_exp_f32 v20, v20\n"
"v_mfma_f32_16x16x32_bf16 v[144:147], a[144:147], v[244:247], v[144:147]\n"
"v_cvt_pk_bf16_f32 v253, v114, v115\n"
"v_cvt_pk_bf16_f32 v254, v116, v117\n"
"v_cvt_pk_bf16_f32 v255, v118, v119\n"
"v_mul_f32 v120, v120, v46\n"
"v_mfma_f32_16x16x32_bf16 v[144:147], a[176:179], v[248:251], v[144:147]\n"
"v_exp_f32 v21, v21\n"
"v_mfma_f32_16x16x32_bf16 v[148:151], a[180:183], v[248:251], v[148:151]\n"
"buffer_store_dwordx4 v[252:255], v9, s[52:55], s97 offen offset:0 sc1\n"
"v_cvt_pk_bf16_f32 v34, v20, v21\n"
"v_mul_f32 v121, v121, v46\n"
"v_mfma_f32_16x16x32_bf16 v[148:151], a[148:151], v[244:247], v[148:151]\n"
"v_exp_f32 v22, v22\n"
"v_mfma_f32_16x16x32_bf16 v[152:155], a[152:155], v[244:247], v[152:155]\n"
"v_mul_f32 v122, v122, v46\n"
"v_mul_f32 v123, v123, v46\n"
"v_mul_f32 v124, v124, v46\n"
"v_mul_f32 v125, v125, v46\n"
"v_mfma_f32_16x16x32_bf16 v[152:155], a[184:187], v[248:251], v[152:155]\n"
"v_exp_f32 v23, v23\n"
"v_mfma_f32_16x16x32_bf16 v[156:159], a[188:191], v[248:251], v[156:159]\n"
"v_cvt_pk_bf16_f32 v35, v22, v23\n"
"v_mul_f32 v126, v126, v46\n"
"v_mul_f32 v127, v127, v46\n"
"v_mfma_f32_16x16x32_bf16 v[156:159], a[156:159], v[244:247], v[156:159]\n"
"v_cvt_pk_bf16_f32 v40, v120, v121\n"
"v_cvt_pk_bf16_f32 v41, v122, v123\n"
"v_cvt_pk_bf16_f32 v42, v124, v125\n"
"v_cvt_pk_bf16_f32 v43, v126, v127\n"
"v_mfma_f32_16x16x32_bf16 v[16:19], a[80:83], a[64:67], v[56:59]\n"
"v_exp_f32 v24, v24\n"
"v_mfma_f32_16x16x32_bf16 v[16:19], a[84:87], a[68:71], v[16:19]\n"
"v_mul_f32 v128, v128, v46\n"
"v_mul_f32 v129, v129, v46\n"
"v_mul_f32 v130, v130, v46\n"
"v_mfma_f32_16x16x32_bf16 v[16:19], a[88:91], a[72:75], v[16:19]\n"
"v_exp_f32 v25, v25\n"
"v_mfma_f32_16x16x32_bf16 v[16:19], a[92:95], a[76:79], v[16:19]\n"
"buffer_store_dwordx4 v[40:43], v9, s[52:55], s97 offen offset:1024 sc1\n"
"v_cvt_pk_bf16_f32 v36, v24, v25\n"
"v_mul_f32 v131, v131, v46\n"
"v_mul_f32 v132, v132, v46\n"
"v_mfma_f32_16x16x32_bf16 v[20:23], a[108:111], a[76:79], v[56:59]\n"
"v_exp_f32 v26, v26\n"
"v_mfma_f32_16x16x32_bf16 v[20:23], a[104:107], a[72:75], v[20:23]\n"
"v_mul_f32 v133, v133, v46\n"
"v_mul_f32 v134, v134, v46\n"
"v_mul_f32 v135, v135, v46\n"
"v_mfma_f32_16x16x32_bf16 v[20:23], a[100:103], a[68:71], v[20:23]\n"
"v_exp_f32 v27, v27\n"
"v_mfma_f32_16x16x32_bf16 v[20:23], a[96:99], a[64:67], v[20:23]\n"
"v_cvt_pk_bf16_f32 v37, v26, v27\n"
"v_cvt_pk_bf16_f32 v240, v128, v129\n"
"v_cvt_pk_bf16_f32 v241, v130, v131\n"
"v_cvt_pk_bf16_f32 v242, v132, v133\n"
"v_mfma_f32_16x16x32_bf16 v[68:71], a[208:211], v[248:251], v[68:71]\n"
"v_exp_f32 v28, v28\n"
"v_mfma_f32_16x16x32_bf16 v[160:163], a[192:195], v[248:251], v[160:163]\n"
"v_cvt_pk_bf16_f32 v243, v134, v135\n"
"v_mul_f32 v136, v136, v46\n"
"v_mul_f32 v137, v137, v46\n"
"v_mfma_f32_16x16x32_bf16 v[160:163], a[160:163], v[244:247], v[160:163]\n"
"v_exp_f32 v29, v29\n"
"v_mfma_f32_16x16x32_bf16 v[164:167], a[164:167], v[244:247], v[164:167]\n"
"buffer_store_dwordx4 v[240:243], v9, s[52:55], s97 offen offset:2048 sc1\n"
"v_cvt_pk_bf16_f32 v38, v28, v29\n"
"v_mul_f32 v138, v138, v46\n"
"v_mul_f32 v139, v139, v46\n"
"v_mfma_f32_16x16x32_bf16 v[164:167], a[196:199], v[248:251], v[164:167]\n"
"v_exp_f32 v30, v30\n"
"v_mfma_f32_16x16x32_bf16 v[168:171], a[200:203], v[248:251], v[168:171]\n"
"v_mul_f32 v140, v140, v46\n"
"v_mul_f32 v141, v141, v46\n"
"v_mul_f32 v142, v142, v46\n"
"v_mfma_f32_16x16x32_bf16 v[168:171], a[168:171], v[244:247], v[168:171]\n"
"v_exp_f32 v31, v31\n"
"v_mfma_f32_16x16x32_bf16 v[172:175], a[172:175], v[244:247], v[172:175]\n"
"v_cvt_pk_bf16_f32 v39, v30, v31\n"
"v_mul_f32 v143, v143, v46\n"
"v_cvt_pk_bf16_f32 v252, v136, v137\n"
"v_cvt_pk_bf16_f32 v253, v138, v139\n"
"v_mfma_f32_16x16x32_bf16 v[172:175], a[204:207], v[248:251], v[172:175]\n"
"v_cvt_pk_bf16_f32 v254, v140, v141\n"
"v_cvt_pk_bf16_f32 v255, v142, v143\n"
"v_mfma_f32_16x16x32_bf16 v[24:27], a[112:115], a[64:67], v[56:59]\n"
"s_mov_b32 s96, 262144\n"
"v_exp_f32 v16, v16\n"
"v_mfma_f32_16x16x32_bf16 v[24:27], a[116:119], a[68:71], v[24:27]\n"
"buffer_store_dwordx4 v[252:255], v9, s[52:55], s97 offen offset:3072 sc1\n"
"v_rcp_f32 v44, v68\n"
"v_cmp_nle_f32 vcc, v68, v11\n"
"v_mul_f32 v144, v144, v44\n"
"v_mfma_f32_16x16x32_bf16 v[24:27], a[120:123], a[72:75], v[24:27]\n"
"s_or_b64 s[98:99], s[98:99], vcc\n"
"v_exp_f32 v17, v17\n"
"v_mfma_f32_16x16x32_bf16 v[24:27], a[124:127], a[76:79], v[24:27]\n"
"v_cvt_pk_bf16_f32 v244, v16, v17\n"
"v_mul_f32 v145, v145, v44\n"
"v_mul_f32 v146, v146, v44\n"
"v_mfma_f32_16x16x32_bf16 v[28:31], a[140:143], a[76:79], v[56:59]\n"
"v_exp_f32 v18, v18\n"
"v_mfma_f32_16x16x32_bf16 v[28:31], a[136:139], a[72:75], v[28:31]\n"
"v_mul_f32 v147, v147, v44\n"
"v_mul_f32 v148, v148, v44\n"
"v_mul_f32 v149, v149, v44\n"
"v_mul_f32 v150, v150, v44\n"
"v_mfma_f32_16x16x32_bf16 v[28:31], a[132:135], a[68:71], v[28:31]\n"
"v_exp_f32 v19, v19\n"
"v_mfma_f32_16x16x32_bf16 v[28:31], a[128:131], a[64:67], v[28:31]\n"
"v_cvt_pk_bf16_f32 v245, v18, v19\n"
"v_mul_f32 v151, v151, v44\n"
"v_cvt_pk_bf16_f32 v40, v144, v145\n"
"v_mfma_f32_16x16x32_bf16 v[72:75], a[208:211], v[32:35], v[72:75]\n"
"v_exp_f32 v20, v20\n"
"v_mfma_f32_16x16x32_bf16 v[176:179], a[144:147], v[32:35], v[176:179]\n"
"v_cvt_pk_bf16_f32 v41, v146, v147\n"
"v_cvt_pk_bf16_f32 v42, v148, v149\n"
"v_cvt_pk_bf16_f32 v43, v150, v151\n"
"v_mul_f32 v152, v152, v44\n"
"v_mfma_f32_16x16x32_bf16 v[176:179], a[176:179], v[36:39], v[176:179]\n"
"v_exp_f32 v21, v21\n"
"v_mfma_f32_16x16x32_bf16 v[180:183], a[180:183], v[36:39], v[180:183]\n"
"buffer_store_dwordx4 v[40:43], v9, s[52:55], s96 offen offset:0 sc1\n"
"v_cvt_pk_bf16_f32 v246, v20, v21\n"
"v_mul_f32 v153, v153, v44\n"
"v_mfma_f32_16x16x32_bf16 v[180:183], a[148:151], v[32:35], v[180:183]\n"
"v_exp_f32 v22, v22\n"
"v_mfma_f32_16x16x32_bf16 v[184:187], a[152:155], v[32:35], v[184:187]\n"
"v_mul_f32 v154, v154, v44\n"
"v_mul_f32 v155, v155, v44\n"
"v_mul_f32 v156, v156, v44\n"
"v_mul_f32 v157, v157, v44\n"
"v_mfma_f32_16x16x32_bf16 v[184:187], a[184:187], v[36:39], v[184:187]\n"
"v_exp_f32 v23, v23\n"
"v_mfma_f32_16x16x32_bf16 v[188:191], a[188:191], v[36:39], v[188:191]\n"
"v_cvt_pk_bf16_f32 v247, v22, v23\n"
"v_mul_f32 v158, v158, v44\n"
"v_mul_f32 v159, v159, v44\n"
"v_mfma_f32_16x16x32_bf16 v[188:191], a[156:159], v[32:35], v[188:191]\n"
"v_cvt_pk_bf16_f32 v240, v152, v153\n"
"v_cvt_pk_bf16_f32 v241, v154, v155\n"
"v_cvt_pk_bf16_f32 v242, v156, v157\n"
"v_cvt_pk_bf16_f32 v243, v158, v159\n"
"s_nop 1\n"
"v_mfma_f32_16x16x32_bf16 v[72:75], a[208:211], v[36:39], v[72:75]\n"
"v_exp_f32 v24, v24\n"
"v_mfma_f32_16x16x32_bf16 v[192:195], a[192:195], v[36:39], v[192:195]\n"
"v_mul_f32 v160, v160, v44\n"
"v_mul_f32 v161, v161, v44\n"
"v_mul_f32 v162, v162, v44\n"
"v_exp_f32 v25, v25\n"
"v_mfma_f32_16x16x32_bf16 v[192:195], a[160:163], v[32:35], v[192:195]\n"
"buffer_store_dwordx4 v[240:243], v9, s[52:55], s96 offen offset:1024 sc1\n"
"v_cvt_pk_bf16_f32 v248, v24, v25\n"
"v_mul_f32 v163, v163, v44\n"
"v_mul_f32 v164, v164, v44\n"
"v_exp_f32 v26, v26\n"
"v_mfma_f32_16x16x32_bf16 v[196:199], a[164:167], v[32:35], v[196:199]\n"
"v_mul_f32 v165, v165, v44\n"
"v_mul_f32 v166, v166, v44\n"
"v_mul_f32 v167, v167, v44\n"
"v_exp_f32 v27, v27\n"
"v_mfma_f32_16x16x32_bf16 v[196:199], a[196:199], v[36:39], v[196:199]\n"
"v_cvt_pk_bf16_f32 v249, v26, v27\n"
"v_cvt_pk_bf16_f32 v252, v160, v161\n"
"v_cvt_pk_bf16_f32 v253, v162, v163\n"
"v_cvt_pk_bf16_f32 v254, v164, v165\n"
"v_exp_f32 v28, v28\n"
"v_mfma_f32_16x16x32_bf16 v[200:203], a[200:203], v[36:39], v[200:203]\n"
"v_cvt_pk_bf16_f32 v255, v166, v167\n"
"v_mul_f32 v168, v168, v44\n"
"v_mul_f32 v169, v169, v44\n"
"v_exp_f32 v29, v29\n"
"v_mfma_f32_16x16x32_bf16 v[200:203], a[168:171], v[32:35], v[200:203]\n"
"buffer_store_dwordx4 v[252:255], v9, s[52:55], s96 offen offset:2048 sc1\n"
"v_cvt_pk_bf16_f32 v250, v28, v29\n"
"v_mul_f32 v170, v170, v44\n"
"v_mul_f32 v171, v171, v44\n"
"v_exp_f32 v30, v30\n"
"v_mfma_f32_16x16x32_bf16 v[204:207], a[172:175], v[32:35], v[204:207]\n"
"v_mul_f32 v172, v172, v44\n"
"v_mul_f32 v173, v173, v44\n"
"v_mul_f32 v174, v174, v44\n"
"v_exp_f32 v31, v31\n"
"v_mfma_f32_16x16x32_bf16 v[204:207], a[204:207], v[36:39], v[204:207]\n"
"v_cvt_pk_bf16_f32 v251, v30, v31\n"
"v_mul_f32 v175, v175, v44\n"
"v_cvt_pk_bf16_f32 v40, v168, v169\n"
"v_cvt_pk_bf16_f32 v41, v170, v171\n"
"v_cvt_pk_bf16_f32 v42, v172, v173\n"
"v_cvt_pk_bf16_f32 v43, v174, v175\n"
"s_nop 1\n"
"v_mfma_f32_16x16x32_bf16 v[76:79], a[208:211], v[244:247], v[76:79]\n"
"s_mov_b32 s97, 393216\n"
"v_mfma_f32_16x16x32_bf16 v[208:211], a[144:147], v[244:247], v[208:211]\n"
"buffer_store_dwordx4 v[40:43], v9, s[52:55], s96 offen offset:3072 sc1\n"
"v_rcp_f32 v46, v72\n"
"v_cmp_nle_f32 vcc, v72, v11\n"
"s_or_b64 s[98:99], s[98:99], vcc\n"
"v_mfma_f32_16x16x32_bf16 v[208:211], a[176:179], v[248:251], v[208:211]\n"
"v_mul_f32 v176, v176, v46\n"
"v_mul_f32 v177, v177, v46\n"
"v_mul_f32 v178, v178, v46\n"
"v_mfma_f32_16x16x32_bf16 v[212:215], a[180:183], v[248:251], v[212:215]\n"
"v_mul_f32 v179, v179, v46\n"
"v_mul_f32 v180, v180, v46\n"
"v_mul_f32 v181, v181, v46\n"
"v_mfma_f32_16x16x32_bf16 v[212:215], a[148:151], v[244:247], v[212:215]\n"
"v_mul_f32 v182, v182, v46\n"
"v_mul_f32 v183, v183, v46\n"
"v_cvt_pk_bf16_f32 v240, v176, v177\n"
"v_mfma_f32_16x16x32_bf16 v[216:219], a[152:155], v[244:247], v[216:219]\n"
"v_cvt_pk_bf16_f32 v241, v178, v179\n"
"v_cvt_pk_bf16_f32 v242, v180, v181\n"
"v_cvt_pk_bf16_f32 v243, v182, v183\n"
"v_mul_f32 v184, v184, v46\n"
"v_mfma_f32_16x16x32_bf16 v[216:219], a[184:187], v[248:251], v[216:219]\n"
"buffer_store_dwordx4 v[240:243], v9, s[52:55], s97 offen offset:0 sc1\n"
"v_mul_f32 v185, v185, v46\n"
"v_mul_f32 v186, v186, v46\n"
"v_mfma_f32_16x16x32_bf16 v[220:223], a[188:191], v[248:251], v[220:223]\n"
"v_mul_f32 v187, v187, v46\n"
"v_mul_f32 v188, v188, v46\n"
"v_mul_f32 v189, v189, v46\n"
"v_mfma_f32_16x16x32_bf16 v[220:223], a[156:159], v[244:247], v[220:223]\n"
"v_mul_f32 v190, v190, v46\n"
"v_mul_f32 v191, v191, v46\n"
"v_cvt_pk_bf16_f32 v252, v184, v185\n"
"v_cvt_pk_bf16_f32 v253, v186, v187\n"
"v_cvt_pk_bf16_f32 v254, v188, v189\n"
"v_cvt_pk_bf16_f32 v255, v190, v191\n"
"s_nop 1\n"
"v_mfma_f32_16x16x32_bf16 v[76:79], a[208:211], v[248:251], v[76:79]\n"
"v_mfma_f32_16x16x32_bf16 v[224:227], a[192:195], v[248:251], v[224:227]\n"
"v_mul_f32 v192, v192, v46\n"
"v_mul_f32 v193, v193, v46\n"
"v_mul_f32 v194, v194, v46\n"
"v_mfma_f32_16x16x32_bf16 v[224:227], a[160:163], v[244:247], v[224:227]\n"
"buffer_store_dwordx4 v[252:255], v9, s[52:55], s97 offen offset:1024 sc1\n"
"v_mul_f32 v195, v195, v46\n"
"v_mul_f32 v196, v196, v46\n"
"v_mfma_f32_16x16x32_bf16 v[228:231], a[164:167], v[244:247], v[228:231]\n"
"v_mul_f32 v197, v197, v46\n"
"v_mul_f32 v198, v198, v46\n"
"v_mul_f32 v199, v199, v46\n"
"v_mfma_f32_16x16x32_bf16 v[228:231], a[196:199], v[248:251], v[228:231]\n"
"v_cvt_pk_bf16_f32 v40, v192, v193\n"
"v_cvt_pk_bf16_f32 v41, v194, v195\n"
"v_cvt_pk_bf16_f32 v42, v196, v197\n"
"v_mfma_f32_16x16x32_bf16 v[232:235], a[200:203], v[248:251], v[232:235]\n"
"v_cvt_pk_bf16_f32 v43, v198, v199\n"
"v_mul_f32 v200, v200, v46\n"
"v_mul_f32 v201, v201, v46\n"
"v_mfma_f32_16x16x32_bf16 v[232:235], a[168:171], v[244:247], v[232:235]\n"
"buffer_store_dwordx4 v[40:43], v9, s[52:55], s97 offen offset:2048 sc1\n"
"v_mul_f32 v202, v202, v46\n"
"v_mul_f32 v203, v203, v46\n"
"v_mfma_f32_16x16x32_bf16 v[236:239], a[172:175], v[244:247], v[236:239]\n"
"v_mul_f32 v204, v204, v46\n"
"v_mul_f32 v205, v205, v46\n"
"v_mul_f32 v206, v206, v46\n"
"v_mfma_f32_16x16x32_bf16 v[236:239], a[204:207], v[248:251], v[236:239]\n"
"v_mul_f32 v207, v207, v46\n"
"v_cvt_pk_bf16_f32 v240, v200, v201\n"
"v_cvt_pk_bf16_f32 v241, v202, v203\n"
"v_cvt_pk_bf16_f32 v242, v204, v205\n"
"v_cvt_pk_bf16_f32 v243, v206, v207\n"
"buffer_store_dwordx4 v[240:243], v9, s[52:55], s97 offen offset:3072 sc1\n"
"s_nop 6\n"
".p2align 3\n"
"s_mov_b32 s96, 524288\n"
"v_rcp_f32 v44, v76\n"
"v_cmp_nle_f32 vcc, v76, v11\n"
"v_mov_b32 v45, v44\n"
"s_or_b64 s[98:99], s[98:99], vcc\n"
"v_pk_mul_f32 v[208:209], v[208:209], v[44:45]\n"
"v_pk_mul_f32 v[210:211], v[210:211], v[44:45]\n"
"v_pk_mul_f32 v[212:213], v[212:213], v[44:45]\n"
"v_pk_mul_f32 v[214:215], v[214:215], v[44:45]\n"
"v_cvt_pk_bf16_f32 v252, v208, v209\n"
"v_cvt_pk_bf16_f32 v253, v210, v211\n"
"v_cvt_pk_bf16_f32 v254, v212, v213\n"
"v_cvt_pk_bf16_f32 v255, v214, v215\n"
"buffer_store_dwordx4 v[252:255], v9, s[52:55], s96 offen offset:0 sc1\n"
"v_pk_mul_f32 v[216:217], v[216:217], v[44:45]\n"
"v_pk_mul_f32 v[218:219], v[218:219], v[44:45]\n"
"v_pk_mul_f32 v[220:221], v[220:221], v[44:45]\n"
"v_pk_mul_f32 v[222:223], v[222:223], v[44:45]\n"
"v_cvt_pk_bf16_f32 v40, v216, v217\n"
"v_cvt_pk_bf16_f32 v41, v218, v219\n"
"v_cvt_pk_bf16_f32 v42, v220, v221\n"
"v_cvt_pk_bf16_f32 v43, v222, v223\n"
"buffer_store_dwordx4 v[40:43], v9, s[52:55], s96 offen offset:1024 sc1\n"
"v_pk_mul_f32 v[224:225], v[224:225], v[44:45]\n"
"v_pk_mul_f32 v[226:227], v[226:227], v[44:45]\n"
"v_pk_mul_f32 v[228:229], v[228:229], v[44:45]\n"
"v_pk_mul_f32 v[230:231], v[230:231], v[44:45]\n"
"v_cvt_pk_bf16_f32 v240, v224, v225\n"
"v_cvt_pk_bf16_f32 v241, v226, v227\n"
"v_cvt_pk_bf16_f32 v242, v228, v229\n"
"v_cvt_pk_bf16_f32 v243, v230, v231\n"
"buffer_store_dwordx4 v[240:243], v9, s[52:55], s96 offen offset:2048 sc1\n"
"v_pk_mul_f32 v[232:233], v[232:233], v[44:45]\n"
"v_pk_mul_f32 v[234:235], v[234:235], v[44:45]\n"
"v_pk_mul_f32 v[236:237], v[236:237], v[44:45]\n"
"v_pk_mul_f32 v[238:239], v[238:239], v[44:45]\n"
"v_cvt_pk_bf16_f32 v252, v232, v233\n"
"v_cvt_pk_bf16_f32 v253, v234, v235\n"
"v_cvt_pk_bf16_f32 v254, v236, v237\n"
"v_cvt_pk_bf16_f32 v255, v238, v239\n"
"buffer_store_dwordx4 v[252:255], v9, s[52:55], s96 offen offset:3072 sc1\n"
"s_cmp_lg_u64 s[98:99], 0\n"
"s_cbranch_scc1 Lfallback_%=\n"
"Lfbdone_%=:\n"
"s_cmp_ge_u32 s24, 30\n"
"s_cbranch_scc1 Lidle_%=\n"
"s_mov_b32 s95, 30720\n"
"buffer_load_dwordx4 a[168:171], v9, s[56:59], s95 offen offset:3072 sc1\n"
"s_mov_b32 s95, 34816\n"
"buffer_load_dwordx4 a[172:175], v9, s[56:59], s95 offen offset:0 sc1\n"
"buffer_load_dwordx4 a[176:179], v9, s[56:59], s95 offen offset:1024 sc1\n"
"s_mov_b32 s95, 36864\n"
"buffer_load_dwordx4 a[180:183], v9, s[56:59], s95 offen offset:0 sc1\n"
"buffer_load_dwordx4 a[184:187], v9, s[56:59], s95 offen offset:1024 sc1\n"
"buffer_load_dwordx4 a[188:191], v9, s[56:59], s95 offen offset:2048 sc1\n"
"buffer_load_dwordx4 a[192:195], v9, s[56:59], s95 offen offset:3072 sc1\n"
"s_mov_b32 s95, 40960\n"
"buffer_load_dwordx4 a[196:199], v9, s[56:59], s95 offen offset:0 sc1\n"
"buffer_load_dwordx4 a[200:203], v9, s[56:59], s95 offen offset:1024 sc1\n"
"s_mov_b32 s95, 43008\n"
"buffer_load_dwordx4 a[204:207], v9, s[56:59], s95 offen offset:0 sc1\n"
"buffer_load_dwordx4 a[208:211], v9, s[56:59], s95 offen offset:1024 sc1\n"
"buffer_load_dwordx4 a[0:3], v9, s[56:59], s95 offen offset:2048 sc1\n"
"buffer_load_dwordx4 a[4:7], v9, s[56:59], s95 offen offset:3072 sc1\n"
"s_mov_b32 s95, 47104\n"
"buffer_load_dwordx4 a[8:11], v9, s[56:59], s95 offen offset:0 sc1\n"
"s_bfe_u32 s87, s25, 0x10002\n"
"s_lshl_b32 s87, s87, 19\n"
"s_xor_b32 s85, s87, 0x80000\n"
"v_add_u32 v3, s83, v9\n"
"v_add_u32 v4, 0x10000, v3\n"
"v_add_u32 v5, s94, v9\n"
"s_add_u32 s96, %[wid], 1\n"
"s_and_b32 s96, s96, 3\n"
"s_lshl_b32 s96, s96, 17\n"
"v_add_u32 v1, s96, v9\n"
"s_add_u32 s96, %[wid], 2\n"
"s_and_b32 s96, s96, 3\n"
"s_lshl_b32 s96, s96, 17\n"
"v_add_u32 v2, s96, v9\n"
"s_add_u32 s96, %[wid], 3\n"
"s_and_b32 s96, s96, 3\n"
"s_lshl_b32 s96, s96, 17\n"
"v_add_u32 v12, s96, v9\n"
"s_add_u32 s96, %[wid], 4\n"
"s_and_b32 s96, s96, 3\n"
"s_lshl_b32 s96, s96, 17\n"
"v_add_u32 v13, s96, v9\n"
"v_and_b32 v6, 3, v0\n"
"v_lshlrev_b32 v6, 2, v6\n"
"v_bfe_u32 v8, v0, 2, 2\n"
"v_min_u32 v8, s18, v8\n"
"v_lshl_add_u32 v6, v8, 8, v6\n"
"v_add_u32 v6, s35, v6\n"
"v_mov_b32 v7, s34\n"
"v_mov_b32 v10, 1\n"
"s_waitcnt vmcnt(14)\n"
"buffer_store_dword v10, v7, s[64:67], 0 offen sc1\n"
"s_mov_b32 s95, 47104\n"
"buffer_load_dwordx4 a[12:15], v9, s[56:59], s95 offen offset:1024 sc1\n"
"s_mov_b32 s95, 49152\n"
"buffer_load_dwordx4 a[16:19], v9, s[56:59], s95 offen offset:0 sc1\n"
"buffer_load_dwordx4 a[20:23], v9, s[56:59], s95 offen offset:1024 sc1\n"
"buffer_load_dwordx4 a[24:27], v9, s[56:59], s95 offen offset:2048 sc1\n"
"buffer_load_dwordx4 a[28:31], v9, s[56:59], s95 offen offset:3072 sc1\n"
"s_mov_b32 s95, 53248\n"
"buffer_load_dwordx4 a[32:35], v9, s[56:59], s95 offen offset:0 sc1\n"
"buffer_load_dwordx4 a[36:39], v9, s[56:59], s95 offen offset:1024 sc1\n"
"s_mov_b32 s95, 55296\n"
"buffer_load_dwordx4 a[40:43], v9, s[56:59], s95 offen offset:0 sc1\n"
"buffer_load_dwordx4 a[44:47], v9, s[56:59], s95 offen offset:1024 sc1\n"
"buffer_load_dwordx4 a[48:51], v9, s[56:59], s95 offen offset:2048 sc1\n"
"buffer_load_dwordx4 a[52:55], v9, s[56:59], s95 offen offset:3072 sc1\n"
"s_mov_b32 s95, 59392\n"
"buffer_load_dwordx4 a[56:59], v9, s[56:59], s95 offen offset:0 sc1\n"
"buffer_load_dwordx4 a[60:63], v9, s[56:59], s95 offen offset:1024 sc1\n"
"s_mov_b32 s95, 61440\n"
"buffer_load_dwordx4 a[64:67], v9, s[56:59], s95 offen offset:0 sc1\n"
"buffer_load_dwordx4 a[68:71], v9, s[56:59], s95 offen offset:1024 sc1\n"
"buffer_load_dwordx4 a[72:75], v9, s[56:59], s95 offen offset:2048 sc1\n"
"buffer_load_dwordx4 a[76:79], v9, s[56:59], s95 offen offset:3072 sc1\n"
"s_mov_b32 s95, 65536\n"
"buffer_load_dwordx4 v[192:195], v9, s[56:59], s95 offen offset:0 sc1\n"
"buffer_load_dwordx4 v[196:199], v9, s[56:59], s95 offen offset:1024 sc1\n"
"s_mov_b32 s95, 67584\n"
"buffer_load_dwordx4 v[200:203], v9, s[56:59], s95 offen offset:0 sc1\n"
"buffer_load_dwordx4 v[204:207], v9, s[56:59], s95 offen offset:1024 sc1\n"
"buffer_load_dwordx4 v[208:211], v9, s[56:59], s95 offen offset:2048 sc1\n"
"buffer_load_dwordx4 v[212:215], v9, s[56:59], s95 offen offset:3072 sc1\n"
"s_mov_b32 s95, 71680\n"
"buffer_load_dwordx4 v[216:219], v9, s[56:59], s95 offen offset:0 sc1\n"
"buffer_load_dwordx4 v[220:223], v9, s[56:59], s95 offen offset:1024 sc1\n"
"s_mov_b32 s95, 73728\n"
"buffer_load_dwordx4 v[224:227], v9, s[56:59], s95 offen offset:0 sc1\n"
"buffer_load_dwordx4 v[228:231], v9, s[56:59], s95 offen offset:1024 sc1\n"
"buffer_load_dwordx4 v[232:235], v9, s[56:59], s95 offen offset:2048 sc1\n"
"buffer_load_dwordx4 v[236:239], v9, s[56:59], s95 offen offset:3072 sc1\n"
"s_mov_b32 s95, 77824\n"
"buffer_load_dwordx4 v[240:243], v9, s[56:59], s95 offen offset:0 sc1\n"
"buffer_load_dwordx4 v[244:247], v9, s[56:59], s95 offen offset:1024 sc1\n"
"buffer_load_dword v8, v6, s[64:67], 0 offen sc1\n"
"ds_read_b128 a[80:83], v5 offset:0\n"
"ds_read_b128 a[84:87], v5 offset:1024\n"
"ds_read_b128 a[88:91], v5 offset:2048\n"
"ds_read_b128 a[92:95], v5 offset:3072\n"
"ds_read_b128 a[96:99], v5 offset:4096\n"
"ds_read_b128 a[100:103], v5 offset:5120\n"
"ds_read_b128 a[104:107], v5 offset:6144\n"
"ds_read_b128 a[108:111], v5 offset:7168\n"
"ds_read_b128 a[112:115], v3 offset:0\n"
"ds_read_b128 a[116:119], v3 offset:1024\n"
"ds_read_b128 a[120:123], v3 offset:2048\n"
"ds_read_b128 a[124:127], v3 offset:3072\n"
"ds_read_b128 a[128:131], v4 offset:0\n"
"ds_read_b128 a[132:135], v4 offset:1024\n"
"ds_read_b128 a[136:139], v4 offset:2048\n"
"ds_read_b128 a[140:143], v4 offset:3072\n"
"ds_read_b128 a[144:147], v3 offset:16384\n"
"ds_read_b128 a[148:151], v3 offset:17408\n"
"ds_read_b128 a[152:155], v3 offset:18432\n"
"ds_read_b128 a[156:159], v3 offset:19456\n"
"ds_read_b128 a[160:163], v4 offset:16384\n"
"ds_read_b128 a[164:167], v4 offset:17408\n"
"s_mov_b64 s[52:53], s[22:23]\n"
"s_add_u32 s52, s52, s27\n"
"s_addc_u32 s53, s53, 0\n"
"s_and_b32 s53, s53, 0xffff\n"
"s_mov_b32 s54, 933888\n"
"s_mov_b32 s55, 0x20000\n"
"s_mov_b32 s88, 0x40000\n"
"Lop_%=:\n"
"s_waitcnt vmcnt(0)\n"
"v_cmp_ne_u32 vcc, 1, v8\n"
"s_cbranch_vccz Look_%=\n"
"s_sleep 2\n"
"buffer_load_dword v8, v6, s[64:67], 0 offen sc1\n"
"s_sub_u32 s88, s88, 1\n"
"s_cmp_lg_u32 s88, 0\n"
"s_cbranch_scc1 Lop_%=\n"
"s_waitcnt vmcnt(0)\n"
"Look_%=:\n"
"s_add_u32 s86, s85, 0\n"
"buffer_load_dwordx4 v[112:115], v1, s[52:55], s86 offen offset:0 sc1\n"
"buffer_load_dwordx4 v[116:119], v1, s[52:55], s86 offen offset:1024 sc1\n"
"buffer_load_dwordx4 v[120:123], v1, s[52:55], s86 offen offset:2048 sc1\n"
"buffer_load_dwordx4 v[124:127], v1, s[52:55], s86 offen offset:3072 sc1\n"
"s_add_u32 s86, s85, 0\n"
"buffer_load_dwordx4 v[128:131], v2, s[52:55], s86 offen offset:0 sc1\n"
"buffer_load_dwordx4 v[132:135], v2, s[52:55], s86 offen offset:1024 sc1\n"
"buffer_load_dwordx4 v[136:139], v2, s[52:55], s86 offen offset:2048 sc1\n"
"buffer_load_dwordx4 v[140:143], v2, s[52:55], s86 offen offset:3072 sc1\n"
"s_add_u32 s86, s85, 0\n"
"buffer_load_dwordx4 v[144:147], v12, s[52:55], s86 offen offset:0 sc1\n"
"buffer_load_dwordx4 v[148:151], v12, s[52:55], s86 offen offset:1024 sc1\n"
"buffer_load_dwordx4 v[152:155], v12, s[52:55], s86 offen offset:2048 sc1\n"
"buffer_load_dwordx4 v[156:159], v12, s[52:55], s86 offen offset:3072 sc1\n"
"s_add_u32 s86, s85, 0\n"
"buffer_load_dwordx4 v[160:163], v13, s[52:55], s86 offen offset:0 sc1\n"
"buffer_load_dwordx4 v[164:167], v13, s[52:55], s86 offen offset:1024 sc1\n"
"buffer_load_dwordx4 v[168:171], v13, s[52:55], s86 offen offset:2048 sc1\n"
"buffer_load_dwordx4 v[172:175], v13, s[52:55], s86 offen offset:3072 sc1\n"
"s_add_u32 s86, s85, 4096\n"
"buffer_load_dwordx4 v[176:179], v1, s[52:55], s86 offen offset:0 sc1\n"
"buffer_load_dwordx4 v[180:183], v1, s[52:55], s86 offen offset:1024 sc1\n"
"buffer_load_dwordx4 v[184:187], v1, s[52:55], s86 offen offset:2048 sc1\n"
"buffer_load_dwordx4 v[188:191], v1, s[52:55], s86 offen offset:3072 sc1\n"
"s_waitcnt vmcnt(16)\n"
"v_mfma_f32_16x16x32_bf16 v[16:19], a[212:215], v[112:115], 0\n"
"s_mov_b32 s95, 86016\n"
"s_add_u32 m0, s83, 98304\n"
"v_mfma_f32_16x16x32_bf16 v[16:19], a[236:239], v[116:119], v[16:19]\n"
"s_waitcnt lgkmcnt(15)\n"
"buffer_load_dwordx4 v9, s[56:59], s95 offen offset:0 sc1 lds\n"
"v_mfma_f32_16x16x32_bf16 v[16:19], a[84:87], v[120:123], v[16:19]\n"
"s_waitcnt lgkmcnt(9)\n"
"v_mfma_f32_16x16x32_bf16 v[16:19], a[108:111], v[124:127], v[16:19]\n"
"s_mov_b32 s95, 86016\n"
"v_mfma_f32_16x16x32_bf16 v[20:23], a[112:115], v[124:127], 0\n"
"buffer_load_dwordx4 v9, s[56:59], s95 offen offset:1024 sc1 lds\n"
"v_mfma_f32_16x16x32_bf16 v[20:23], a[88:91], v[120:123], v[20:23]\n"
"s_sub_u32 s96, %[wid], 1\n"
"s_and_b32 s96, s96, 3\n"
"s_lshl_b32 s96, s96, 12\n"
"v_mfma_f32_16x16x32_bf16 v[20:23], a[240:243], v[116:119], v[20:23]\n"
"v_add_u32 v6, s96, v9\n"
"v_mfma_f32_16x16x32_bf16 v[20:23], a[216:219], v[112:115], v[20:23]\n"
"s_mov_b32 s95, 86016\n"
"v_mfma_f32_16x16x32_bf16 v[24:27], a[220:223], v[112:115], 0\n"
"buffer_load_dwordx4 v9, s[56:59], s95 offen offset:2048 sc1 lds\n"
"v_mfma_f32_16x16x32_bf16 v[24:27], a[244:247], v[116:119], v[24:27]\n"
"s_sub_u32 s96, %[wid], 2\n"
"s_and_b32 s96, s96, 3\n"
"s_lshl_b32 s96, s96, 12\n"
"v_mfma_f32_16x16x32_bf16 v[24:27], a[92:95], v[120:123], v[24:27]\n"
"v_add_u32 v7, s96, v9\n"
"v_mfma_f32_16x16x32_bf16 v[24:27], a[116:119], v[124:127], v[24:27]\n"
"s_mov_b32 s95, 86016\n"
"v_mfma_f32_16x16x32_bf16 v[28:31], a[120:123], v[124:127], 0\n"
"buffer_load_dwordx4 v9, s[56:59], s95 offen offset:3072 sc1 lds\n"
"v_mfma_f32_16x16x32_bf16 v[28:31], a[96:99], v[120:123], v[28:31]\n"
"s_sub_u32 s96, %[wid], 3\n"
"s_and_b32 s96, s96, 3\n"
"s_lshl_b32 s96, s96, 12\n"
"v_mfma_f32_16x16x32_bf16 v[28:31], a[248:251], v[116:119], v[28:31]\n"
"s_mov_b32 s95, 90112\n"
"s_add_u32 m0, s83, 49152\n"
"v_add_u32 v8, s96, v9\n"
"v_mfma_f32_16x16x32_bf16 v[28:31], a[224:227], v[112:115], v[28:31]\n"
"buffer_load_dwordx4 v9, s[56:59], s95 offen offset:0 sc1 lds\n"
"v_add_u32 v10, 0x10000, v8\n"
"v_mfma_f32_16x16x32_bf16 v[32:35], a[228:231], v[112:115], 0\n"
"v_add_u32 v14, 4096, v9\n"
"v_mfma_f32_16x16x32_bf16 v[32:35], a[252:255], v[116:119], v[32:35]\n"
"v_mov_b32 v248, 1\n"
"v_mfma_f32_16x16x32_bf16 v[32:35], a[100:103], v[120:123], v[32:35]\n"
"s_mov_b32 s95, 90112\n"
"v_mfma_f32_16x16x32_bf16 v[32:35], a[124:127], v[124:127], v[32:35]\n"
"buffer_load_dwordx4 v9, s[56:59], s95 offen offset:1024 sc1 lds\n"
"v_mfma_f32_16x16x32_bf16 v[36:39], a[128:131], v[124:127], 0\n"
"s_xor_b32 s97, s25, 4\n"
"s_mul_i32 s96, s97, 6144\n"
"s_add_u32 s96, s96, 38797312\n"
"v_mfma_f32_16x16x32_bf16 v[36:39], a[104:107], v[120:123], v[36:39]\n"
"s_mov_b64 s[36:37], s[22:23]\n"
"s_add_u32 s36, s36, s96\n"
"s_addc_u32 s37, s37, 0\n"
"v_mfma_f32_16x16x32_bf16 v[36:39], a[80:83], v[116:119], v[36:39]\n"
"v_mfma_f32_16x16x32_bf16 v[36:39], a[232:235], v[112:115], v[36:39]\n"
"s_waitcnt vmcnt(18)\n"
"v_mfma_f32_16x16x32_bf16 v[40:43], a[212:215], v[128:131], 0\n"
"s_add_u32 s86, s85, 4096\n"
"v_mfma_f32_16x16x32_bf16 v[40:43], a[236:239], v[132:135], v[40:43]\n"
"buffer_load_dwordx4 v[112:115], v2, s[52:55], s86 offen offset:0 sc1\n"
"v_mfma_f32_16x16x32_bf16 v[40:43], a[84:87], v[136:139], v[40:43]\n"
"v_mfma_f32_16x16x32_bf16 v[40:43], a[108:111], v[140:143], v[40:43]\n"
"s_and_b32 s37, s37, 0xffff\n"
"s_mov_b32 s38, 6144\n"
"s_mov_b32 s39, 0x20000\n"
"v_mfma_f32_16x16x32_bf16 v[44:47], a[112:115], v[140:143], 0\n"
"buffer_load_dwordx4 v[116:119], v2, s[52:55], s86 offen offset:1024 sc1\n"
"s_lshl_b32 s97, s97, 2\n"
"s_add_u32 s97, s97, 12288\n"
"v_mfma_f32_16x16x32_bf16 v[44:47], a[88:91], v[136:139], v[44:47]\n"
"v_mov_b32 v249, s97\n"
"v_mfma_f32_16x16x32_bf16 v[44:47], a[240:243], v[132:135], v[44:47]\n"
"v_xor_b32 v250, 16, v249\n"
"v_mfma_f32_16x16x32_bf16 v[44:47], a[216:219], v[128:131], v[44:47]\n"
"buffer_load_dwordx4 v[120:123], v2, s[52:55], s86 offen offset:2048 sc1\n"
"s_mul_i32 s96, s25, 6144\n"
"s_add_u32 s96, s96, 38797312\n"
"v_mfma_f32_16x16x32_bf16 v[48:51], a[220:223], v[128:131], 0\n"
"s_mov_b64 s[40:41], s[22:23]\n"
"s_add_u32 s40, s40, s96\n"
"s_addc_u32 s41, s41, 0\n"
"v_mfma_f32_16x16x32_bf16 v[48:51], a[244:247], v[132:135], v[48:51]\n"
"s_and_b32 s41, s41, 0xffff\n"
"s_mov_b32 s42, 6144\n"
"s_mov_b32 s43, 0x20000\n"
"v_mfma_f32_16x16x32_bf16 v[48:51], a[92:95], v[136:139], v[48:51]\n"
"buffer_load_dwordx4 v[124:127], v2, s[52:55], s86 offen offset:3072 sc1\n"
"v_mfma_f32_16x16x32_bf16 v[48:51], a[116:119], v[140:143], v[48:51]\n"
"v_mfma_f32_16x16x32_bf16 v[52:55], a[120:123], v[140:143], 0\n"
"s_mov_b32 s95, 90112\n"
"v_mfma_f32_16x16x32_bf16 v[52:55], a[96:99], v[136:139], v[52:55]\n"
"buffer_load_dwordx4 v9, s[56:59], s95 offen offset:2048 sc1 lds\n"
"v_mfma_f32_16x16x32_bf16 v[52:55], a[248:251], v[132:135], v[52:55]\n"
"v_mfma_f32_16x16x32_bf16 v[52:55], a[224:227], v[128:131], v[52:55]\n"
"s_mov_b32 s95, 90112\n"
"v_mfma_f32_16x16x32_bf16 v[56:59], a[228:231], v[128:131], 0\n"
"buffer_load_dwordx4 v9, s[56:59], s95 offen offset:3072 sc1 lds\n"
"v_mfma_f32_16x16x32_bf16 v[56:59], a[252:255], v[132:135], v[56:59]\n"
"v_mfma_f32_16x16x32_bf16 v[56:59], a[100:103], v[136:139], v[56:59]\n"
"s_mov_b32 s95, 94208\n"
"s_add_u32 m0, s83, 114688\n"
"v_mfma_f32_16x16x32_bf16 v[56:59], a[124:127], v[140:143], v[56:59]\n"
"buffer_load_dwordx4 v9, s[56:59], s95 offen offset:0 sc1 lds\n"
"v_mfma_f32_16x16x32_bf16 v[60:63], a[128:131], v[140:143], 0\n"
"v_mfma_f32_16x16x32_bf16 v[60:63], a[104:107], v[136:139], v[60:63]\n"
"v_mfma_f32_16x16x32_bf16 v[60:63], a[80:83], v[132:135], v[60:63]\n"
"v_mfma_f32_16x16x32_bf16 v[60:63], a[232:235], v[128:131], v[60:63]\n"
"s_waitcnt vmcnt(21)\n"
"v_mfma_f32_16x16x32_bf16 v[64:67], a[212:215], v[144:147], 0\n"
"s_add_u32 s86, s85, 4096\n"
"v_mfma_f32_16x16x32_bf16 v[64:67], a[236:239], v[148:151], v[64:67]\n"
"buffer_load_dwordx4 v[128:131], v12, s[52:55], s86 offen offset:0 sc1\n"
"v_mfma_f32_16x16x32_bf16 v[64:67], a[84:87], v[152:155], v[64:67]\n"
"v_mfma_f32_16x16x32_bf16 v[64:67], a[108:111], v[156:159], v[64:67]\n"
"v_mfma_f32_16x16x32_bf16 v[68:71], a[112:115], v[156:159], 0\n"
"buffer_load_dwordx4 v[132:135], v12, s[52:55], s86 offen offset:1024 sc1\n"
"v_mfma_f32_16x16x32_bf16 v[68:71], a[88:91], v[152:155], v[68:71]\n"
"v_mfma_f32_16x16x32_bf16 v[68:71], a[240:243], v[148:151], v[68:71]\n"
"v_mfma_f32_16x16x32_bf16 v[68:71], a[216:219], v[144:147], v[68:71]\n"
"buffer_load_dwordx4 v[136:139], v12, s[52:55], s86 offen offset:2048 sc1\n"
"v_mfma_f32_16x16x32_bf16 v[72:75], a[220:223], v[144:147], 0\n"
"v_mfma_f32_16x16x32_bf16 v[72:75], a[244:247], v[148:151], v[72:75]\n"
"v_mfma_f32_16x16x32_bf16 v[72:75], a[92:95], v[152:155], v[72:75]\n"
"buffer_load_dwordx4 v[140:143], v12, s[52:55], s86 offen offset:3072 sc1\n"
"v_mfma_f32_16x16x32_bf16 v[72:75], a[116:119], v[156:159], v[72:75]\n"
"v_mfma_f32_16x16x32_bf16 v[76:79], a[120:123], v[156:159], 0\n"
"s_mov_b32 s95, 94208\n"
"v_mfma_f32_16x16x32_bf16 v[76:79], a[96:99], v[152:155], v[76:79]\n"
"buffer_load_dwordx4 v9, s[56:59], s95 offen offset:1024 sc1 lds\n"
"v_mfma_f32_16x16x32_bf16 v[76:79], a[248:251], v[148:151], v[76:79]\n"
"v_mfma_f32_16x16x32_bf16 v[76:79], a[224:227], v[144:147], v[76:79]\n"
"s_mov_b32 s95, 94208\n"
"v_mfma_f32_16x16x32_bf16 v[80:83], a[228:231], v[144:147], 0\n"
"buffer_load_dwordx4 v9, s[56:59], s95 offen offset:2048 sc1 lds\n"
"v_mfma_f32_16x16x32_bf16 v[80:83], a[252:255], v[148:151], v[80:83]\n"
"v_mfma_f32_16x16x32_bf16 v[80:83], a[100:103], v[152:155], v[80:83]\n"
"s_mov_b32 s95, 94208\n"
"v_mfma_f32_16x16x32_bf16 v[80:83], a[124:127], v[156:159], v[80:83]\n"
"buffer_load_dwordx4 v9, s[56:59], s95 offen offset:3072 sc1 lds\n"
"v_mfma_f32_16x16x32_bf16 v[84:87], a[128:131], v[156:159], 0\n"
"v_mfma_f32_16x16x32_bf16 v[84:87], a[104:107], v[152:155], v[84:87]\n"
"v_mfma_f32_16x16x32_bf16 v[84:87], a[80:83], v[148:151], v[84:87]\n"
"v_mfma_f32_16x16x32_bf16 v[84:87], a[232:235], v[144:147], v[84:87]\n"
"s_waitcnt vmcnt(24)\n"
"v_mfma_f32_16x16x32_bf16 v[88:91], a[212:215], v[160:163], 0\n"
"s_add_u32 s86, s85, 4096\n"
"v_mfma_f32_16x16x32_bf16 v[88:91], a[236:239], v[164:167], v[88:91]\n"
"buffer_load_dwordx4 v[144:147], v13, s[52:55], s86 offen offset:0 sc1\n"
"v_mfma_f32_16x16x32_bf16 v[88:91], a[84:87], v[168:171], v[88:91]\n"
"v_mfma_f32_16x16x32_bf16 v[88:91], a[108:111], v[172:175], v[88:91]\n"
"v_mfma_f32_16x16x32_bf16 v[92:95], a[112:115], v[172:175], 0\n"
"v_mfma_f32_16x16x32_bf16 v[92:95], a[88:91], v[168:171], v[92:95]\n"
"v_mfma_f32_16x16x32_bf16 v[92:95], a[240:243], v[164:167], v[92:95]\n"
"buffer_load_dwordx4 v[148:151], v13, s[52:55], s86 offen offset:1024 sc1\n"
"v_mfma_f32_16x16x32_bf16 v[92:95], a[216:219], v[160:163], v[92:95]\n"
"v_mfma_f32_16x16x32_bf16 v[96:99], a[220:223], v[160:163], 0\n"
"v_mfma_f32_16x16x32_bf16 v[96:99], a[244:247], v[164:167], v[96:99]\n"
"v_mfma_f32_16x16x32_bf16 v[96:99], a[92:95], v[168:171], v[96:99]\n"
"v_mfma_f32_16x16x32_bf16 v[96:99], a[116:119], v[172:175], v[96:99]\n"
"v_mfma_f32_16x16x32_bf16 v[100:103], a[120:123], v[172:175], 0\n"
"buffer_load_dwordx4 v[152:155], v13, s[52:55], s86 offen offset:2048 sc1\n"
"v_mfma_f32_16x16x32_bf16 v[100:103], a[96:99], v[168:171], v[100:103]\n"
"v_mfma_f32_16x16x32_bf16 v[100:103], a[248:251], v[164:167], v[100:103]\n"
"v_mfma_f32_16x16x32_bf16 v[100:103], a[224:227], v[160:163], v[100:103]\n"
"v_mfma_f32_16x16x32_bf16 v[104:107], a[228:231], v[160:163], 0\n"
"v_mfma_f32_16x16x32_bf16 v[104:107], a[252:255], v[164:167], v[104:107]\n"
"buffer_load_dwordx4 v[156:159], v13, s[52:55], s86 offen offset:3072 sc1\n"
"v_mfma_f32_16x16x32_bf16 v[104:107], a[100:103], v[168:171], v[104:107]\n"
"v_mfma_f32_16x16x32_bf16 v[104:107], a[124:127], v[172:175], v[104:107]\n"
"v_mfma_f32_16x16x32_bf16 v[108:111], a[128:131], v[172:175], 0\n"
"v_mfma_f32_16x16x32_bf16 v[108:111], a[104:107], v[168:171], v[108:111]\n"
"v_mfma_f32_16x16x32_bf16 v[108:111], a[80:83], v[164:167], v[108:111]\n"
"v_mfma_f32_16x16x32_bf16 v[108:111], a[232:235], v[160:163], v[108:111]\n"
"s_waitcnt lgkmcnt(3)\n"
"s_waitcnt vmcnt(24)\n"
"v_mfma_f32_16x16x32_bf16 v[16:19], a[132:135], v[176:179], v[16:19]\n"
"s_waitcnt lgkmcnt(0)\n"
"s_add_u32 s86, s85, 8192\n"
"v_mfma_f32_16x16x32_bf16 v[16:19], a[156:159], v[180:183], v[16:19]\n"
"buffer_load_dwordx4 v[160:163], v1, s[52:55], s86 offen offset:0 sc1\n"
"v_mfma_f32_16x16x32_bf16 v[16:19], a[180:183], v[184:187], v[16:19]\n"
"v_mfma_f32_16x16x32_bf16 v[16:19], a[204:207], v[188:191], v[16:19]\n"
"v_mfma_f32_16x16x32_bf16 v[20:23], a[208:211], v[188:191], v[20:23]\n"
"v_mfma_f32_16x16x32_bf16 v[20:23], a[184:187], v[184:187], v[20:23]\n"
"v_mfma_f32_16x16x32_bf16 v[20:23], a[160:163], v[180:183], v[20:23]\n"
"buffer_load_dwordx4 v[164:167], v1, s[52:55], s86 offen offset:1024 sc1\n"
"v_mfma_f32_16x16x32_bf16 v[20:23], a[136:139], v[176:179], v[20:23]\n"
"v_mfma_f32_16x16x32_bf16 v[24:27], a[140:143], v[176:179], v[24:27]\n"
"v_mfma_f32_16x16x32_bf16 v[24:27], a[164:167], v[180:183], v[24:27]\n"
"v_mfma_f32_16x16x32_bf16 v[24:27], a[188:191], v[184:187], v[24:27]\n"
"v_mfma_f32_16x16x32_bf16 v[24:27], a[0:3], v[188:191], v[24:27]\n"
"v_mfma_f32_16x16x32_bf16 v[28:31], a[4:7], v[188:191], v[28:31]\n"
"buffer_load_dwordx4 v[168:171], v1, s[52:55], s86 offen offset:2048 sc1\n"
"v_mfma_f32_16x16x32_bf16 v[28:31], a[192:195], v[184:187], v[28:31]\n"
"v_mfma_f32_16x16x32_bf16 v[28:31], a[168:171], v[180:183], v[28:31]\n"
"v_mfma_f32_16x16x32_bf16 v[28:31], a[144:147], v[176:179], v[28:31]\n"
"v_mfma_f32_16x16x32_bf16 v[32:35], a[148:151], v[176:179], v[32:35]\n"
"v_mfma_f32_16x16x32_bf16 v[32:35], a[172:175], v[180:183], v[32:35]\n"
"buffer_load_dwordx4 v[172:175], v1, s[52:55], s86 offen offset:3072 sc1\n"
"v_mfma_f32_16x16x32_bf16 v[32:35], a[196:199], v[184:187], v[32:35]\n"
"v_mfma_f32_16x16x32_bf16 v[32:35], a[8:11], v[188:191], v[32:35]\n"
"v_mfma_f32_16x16x32_bf16 v[36:39], a[12:15], v[188:191], v[36:39]\n"
"v_mfma_f32_16x16x32_bf16 v[36:39], a[200:203], v[184:187], v[36:39]\n"
"v_mfma_f32_16x16x32_bf16 v[36:39], a[176:179], v[180:183], v[36:39]\n"
"v_mfma_f32_16x16x32_bf16 v[36:39], a[152:155], v[176:179], v[36:39]\n"
"s_waitcnt vmcnt(18)\n"
"v_mfma_f32_16x16x32_bf16 v[40:43], a[132:135], v[112:115], v[40:43]\n"
"s_add_u32 s86, s85, 8192\n"
"v_mfma_f32_16x16x32_bf16 v[40:43], a[156:159], v[116:119], v[40:43]\n"
"buffer_load_dwordx4 v[176:179], v2, s[52:55], s86 offen offset:0 sc1\n"
"v_mfma_f32_16x16x32_bf16 v[40:43], a[180:183], v[120:123], v[40:43]\n"
"v_mfma_f32_16x16x32_bf16 v[40:43], a[204:207], v[124:127], v[40:43]\n"
"v_mfma_f32_16x16x32_bf16 v[44:47], a[208:211], v[124:127], v[44:47]\n"
"v_mfma_f32_16x16x32_bf16 v[44:47], a[184:187], v[120:123], v[44:47]\n"
"v_mfma_f32_16x16x32_bf16 v[44:47], a[160:163], v[116:119], v[44:47]\n"
"buffer_load_dwordx4 v[180:183], v2, s[52:55], s86 offen offset:1024 sc1\n"
"v_mfma_f32_16x16x32_bf16 v[44:47], a[136:139], v[112:115], v[44:47]\n"
"v_mfma_f32_16x16x32_bf16 v[48:51], a[140:143], v[112:115], v[48:51]\n"
"v_mfma_f32_16x16x32_bf16 v[48:51], a[164:167], v[116:119], v[48:51]\n"
"v_mfma_f32_16x16x32_bf16 v[48:51], a[188:191], v[120:123], v[48:51]\n"
"v_mfma_f32_16x16x32_bf16 v[48:51], a[0:3], v[124:127], v[48:51]\n"
"v_mfma_f32_16x16x32_bf16 v[52:55], a[4:7], v[124:127], v[52:55]\n"
"buffer_load_dwordx4 v[184:187], v2, s[52:55], s86 offen offset:2048 sc1\n"
"v_mfma_f32_16x16x32_bf16 v[52:55], a[192:195], v[120:123], v[52:55]\n"
"v_mfma_f32_16x16x32_bf16 v[52:55], a[168:171], v[116:119], v[52:55]\n"
"v_mfma_f32_16x16x32_bf16 v[52:55], a[144:147], v[112:115], v[52:55]\n"
"v_mfma_f32_16x16x32_bf16 v[56:59], a[148:151], v[112:115], v[56:59]\n"
"v_mfma_f32_16x16x32_bf16 v[56:59], a[172:175], v[116:119], v[56:59]\n"
"buffer_load_dwordx4 v[188:191], v2, s[52:55], s86 offen offset:3072 sc1\n"
"v_mfma_f32_16x16x32_bf16 v[56:59], a[196:199], v[120:123], v[56:59]\n"
"v_mfma_f32_16x16x32_bf16 v[56:59], a[8:11], v[124:127], v[56:59]\n"
"v_mfma_f32_16x16x32_bf16 v[60:63], a[12:15], v[124:127], v[60:63]\n"
"v_mfma_f32_16x16x32_bf16 v[60:63], a[200:203], v[120:123], v[60:63]\n"
"v_mfma_f32_16x16x32_bf16 v[60:63], a[176:179], v[116:119], v[60:63]\n"
"v_mfma_f32_16x16x32_bf16 v[60:63], a[152:155], v[112:115], v[60:63]\n"
"s_waitcnt vmcnt(12)\n"
"v_mfma_f32_16x16x32_bf16 v[64:67], a[132:135], v[128:131], v[64:67]\n"
"s_add_u32 s86, s85, 8192\n"
"v_mfma_f32_16x16x32_bf16 v[64:67], a[156:159], v[132:135], v[64:67]\n"
"buffer_load_dwordx4 v[112:115], v12, s[52:55], s86 offen offset:0 sc1\n"
"v_mfma_f32_16x16x32_bf16 v[64:67], a[180:183], v[136:139], v[64:67]\n"
"ds_read_b128 a[212:215], v4 offset:18432\n"
"v_mfma_f32_16x16x32_bf16 v[64:67], a[204:207], v[140:143], v[64:67]\n"
"v_mfma_f32_16x16x32_bf16 v[68:71], a[208:211], v[140:143], v[68:71]\n"
"ds_read_b128 a[216:219], v4 offset:19456\n"
"v_mfma_f32_16x16x32_bf16 v[68:71], a[184:187], v[136:139], v[68:71]\n"
"v_mfma_f32_16x16x32_bf16 v[68:71], a[160:163], v[132:135], v[68:71]\n"
"buffer_load_dwordx4 v[116:119], v12, s[52:55], s86 offen offset:1024 sc1\n"
"v_mfma_f32_16x16x32_bf16 v[68:71], a[136:139], v[128:131], v[68:71]\n"
"ds_read_b128 a[220:223], v3 offset:32768\n"
"v_mfma_f32_16x16x32_bf16 v[72:75], a[140:143], v[128:131], v[72:75]\n"
"v_mfma_f32_16x16x32_bf16 v[72:75], a[164:167], v[132:135], v[72:75]\n"
"ds_read_b128 a[224:227], v3 offset:33792\n"
"v_mfma_f32_16x16x32_bf16 v[72:75], a[188:191], v[136:139], v[72:75]\n"
"v_mfma_f32_16x16x32_bf16 v[72:75], a[0:3], v[140:143], v[72:75]\n"
"ds_read_b128 a[228:231], v3 offset:34816\n"
"v_mfma_f32_16x16x32_bf16 v[76:79], a[4:7], v[140:143], v[76:79]\n"
"buffer_load_dwordx4 v[120:123], v12, s[52:55], s86 offen offset:2048 sc1\n"
"v_mfma_f32_16x16x32_bf16 v[76:79], a[192:195], v[136:139], v[76:79]\n"
"ds_read_b128 a[232:235], v3 offset:35840\n"
"v_mfma_f32_16x16x32_bf16 v[76:79], a[168:171], v[132:135], v[76:79]\n"
"v_mfma_f32_16x16x32_bf16 v[76:79], a[144:147], v[128:131], v[76:79]\n"
"ds_read_b128 a[236:239], v4 offset:32768\n"
"v_mfma_f32_16x16x32_bf16 v[80:83], a[148:151], v[128:131], v[80:83]\n"
"v_mfma_f32_16x16x32_bf16 v[80:83], a[172:175], v[132:135], v[80:83]\n"
"buffer_load_dwordx4 v[124:127], v12, s[52:55], s86 offen offset:3072 sc1\n"
"v_mfma_f32_16x16x32_bf16 v[80:83], a[196:199], v[136:139], v[80:83]\n"
"ds_read_b128 a[240:243], v4 offset:33792\n"
"v_mfma_f32_16x16x32_bf16 v[80:83], a[8:11], v[140:143], v[80:83]\n"
"v_mfma_f32_16x16x32_bf16 v[84:87], a[12:15], v[140:143], v[84:87]\n"
"ds_read_b128 a[244:247], v4 offset:34816\n"
"v_mfma_f32_16x16x32_bf16 v[84:87], a[200:203], v[136:139], v[84:87]\n"
"v_mfma_f32_16x16x32_bf16 v[84:87], a[176:179], v[132:135], v[84:87]\n"
"v_mfma_f32_16x16x32_bf16 v[84:87], a[152:155], v[128:131], v[84:87]\n"
"s_waitcnt vmcnt(12)\n"
"v_mfma_f32_16x16x32_bf16 v[88:91], a[132:135], v[144:147], v[88:91]\n"
"s_add_u32 s86, s85, 8192\n"
"v_mfma_f32_16x16x32_bf16 v[88:91], a[156:159], v[148:151], v[88:91]\n"
"buffer_load_dwordx4 v[128:131], v13, s[52:55], s86 offen offset:0 sc1\n"
"v_mfma_f32_16x16x32_bf16 v[88:91], a[180:183], v[152:155], v[88:91]\n"
"ds_read_b128 a[248:251], v4 offset:35840\n"
"v_mfma_f32_16x16x32_bf16 v[88:91], a[204:207], v[156:159], v[88:91]\n"
"v_mfma_f32_16x16x32_bf16 v[92:95], a[208:211], v[156:159], v[92:95]\n"
"ds_read_b128 a[252:255], v3 offset:49152\n"
"v_mfma_f32_16x16x32_bf16 v[92:95], a[184:187], v[152:155], v[92:95]\n"
"v_mfma_f32_16x16x32_bf16 v[92:95], a[160:163], v[148:151], v[92:95]\n"
"buffer_load_dwordx4 v[132:135], v13, s[52:55], s86 offen offset:1024 sc1\n"
"v_mfma_f32_16x16x32_bf16 v[92:95], a[136:139], v[144:147], v[92:95]\n"
"ds_read_b128 a[80:83], v3 offset:50176\n"
"v_mfma_f32_16x16x32_bf16 v[96:99], a[140:143], v[144:147], v[96:99]\n"
"v_mfma_f32_16x16x32_bf16 v[96:99], a[164:167], v[148:151], v[96:99]\n"
"ds_read_b128 a[84:87], v3 offset:51200\n"
"v_mfma_f32_16x16x32_bf16 v[96:99], a[188:191], v[152:155], v[96:99]\n"
"v_mfma_f32_16x16x32_bf16 v[96:99], a[0:3], v[156:159], v[96:99]\n"
"ds_read_b128 a[88:91], v3 offset:52224\n"
"v_mfma_f32_16x16x32_bf16 v[100:103], a[4:7], v[156:159], v[100:103]\n"
"buffer_load_dwordx4 v[136:139], v13, s[52:55], s86 offen offset:2048 sc1\n"
"v_mfma_f32_16x16x32_bf16 v[100:103], a[192:195], v[152:155], v[100:103]\n"
"ds_read_b128 a[92:95], v4 offset:49152\n"
"v_mfma_f32_16x16x32_bf16 v[100:103], a[168:171], v[148:151], v[100:103]\n"
"v_mfma_f32_16x16x32_bf16 v[100:103], a[144:147], v[144:147], v[100:103]\n"
"ds_read_b128 a[96:99], v4 offset:50176\n"
"v_mfma_f32_16x16x32_bf16 v[104:107], a[148:151], v[144:147], v[104:107]\n"
"v_mfma_f32_16x16x32_bf16 v[104:107], a[172:175], v[148:151], v[104:107]\n"
"buffer_load_dwordx4 v[140:143], v13, s[52:55], s86 offen offset:3072 sc1\n"
"v_mfma_f32_16x16x32_bf16 v[104:107], a[196:199], v[152:155], v[104:107]\n"
"ds_read_b128 a[100:103], v4 offset:51200\n"
"v_mfma_f32_16x16x32_bf16 v[104:107], a[8:11], v[156:159], v[104:107]\n"
"v_mfma_f32_16x16x32_bf16 v[108:111], a[12:15], v[156:159], v[108:111]\n"
"ds_read_b128 a[104:107], v4 offset:52224\n"
"v_mfma_f32_16x16x32_bf16 v[108:111], a[200:203], v[152:155], v[108:111]\n"
"v_mfma_f32_16x16x32_bf16 v[108:111], a[176:179], v[148:151], v[108:111]\n"
"v_mfma_f32_16x16x32_bf16 v[108:111], a[152:155], v[144:147], v[108:111]\n"
"s_waitcnt vmcnt(12) lgkmcnt(0)\n"
"v_mfma_f32_16x16x32_bf16 v[16:19], a[16:19], v[160:163], v[16:19]\n"
"s_add_u32 s86, s85, 12288\n"
"v_mfma_f32_16x16x32_bf16 v[16:19], a[40:43], v[164:167], v[16:19]\n"
"buffer_load_dwordx4 v[144:147], v1, s[52:55], s86 offen offset:0 sc1\n"
"v_mfma_f32_16x16x32_bf16 v[16:19], a[64:67], v[168:171], v[16:19]\n"
"v_mfma_f32_16x16x32_bf16 v[16:19], v[200:203], v[172:175], v[16:19]\n"
"v_mfma_f32_16x16x32_bf16 v[20:23], v[204:207], v[172:175], v[20:23]\n"
"buffer_load_dwordx4 v[148:151], v1, s[52:55], s86 offen offset:1024 sc1\n"
"v_mfma_f32_16x16x32_bf16 v[20:23], a[68:71], v[168:171], v[20:23]\n"
"v_mfma_f32_16x16x32_bf16 v[20:23], a[44:47], v[164:167], v[20:23]\n"
"v_mfma_f32_16x16x32_bf16 v[20:23], a[20:23], v[160:163], v[20:23]\n"
"buffer_load_dwordx4 v[152:155], v1, s[52:55], s86 offen offset:2048 sc1\n"
"v_mfma_f32_16x16x32_bf16 v[24:27], a[24:27], v[160:163], v[24:27]\n"
"v_mfma_f32_16x16x32_bf16 v[24:27], a[48:51], v[164:167], v[24:27]\n"
"v_mfma_f32_16x16x32_bf16 v[24:27], a[72:75], v[168:171], v[24:27]\n"
"buffer_load_dwordx4 v[156:159], v1, s[52:55], s86 offen offset:3072 sc1\n"
"v_mfma_f32_16x16x32_bf16 v[24:27], v[208:211], v[172:175], v[24:27]\n"
"v_mfma_f32_16x16x32_bf16 v[28:31], v[212:215], v[172:175], v[28:31]\n"
"s_mov_b32 s95, 0\n"
"s_add_u32 m0, s83, 67584\n"
"v_mfma_f32_16x16x32_bf16 v[28:31], a[76:79], v[168:171], v[28:31]\n"
"buffer_load_dwordx4 v9, s[56:59], s95 offen sc1 lds\n"
"v_mfma_f32_16x16x32_bf16 v[28:31], a[52:55], v[164:167], v[28:31]\n"
"v_mfma_f32_16x16x32_bf16 v[28:31], a[28:31], v[160:163], v[28:31]\n"
"s_mov_b32 s95, 1024\n"
"s_add_u32 m0, s83, 68608\n"
"v_mfma_f32_16x16x32_bf16 v[32:35], a[32:35], v[160:163], v[32:35]\n"
"buffer_load_dwordx4 v9, s[56:59], s95 offen sc1 lds\n"
"v_mfma_f32_16x16x32_bf16 v[32:35], a[56:59], v[164:167], v[32:35]\n"
"v_mfma_f32_16x16x32_bf16 v[32:35], v[192:195], v[168:171], v[32:35]\n"
"s_mov_b32 s95, 2048\n"
"s_add_u32 m0, s83, 81920\n"
"v_mfma_f32_16x16x32_bf16 v[32:35], v[216:219], v[172:175], v[32:35]\n"
"buffer_load_dwordx4 v9, s[56:59], s95 offen sc1 lds\n"
"v_mfma_f32_16x16x32_bf16 v[36:39], v[220:223], v[172:175], v[36:39]\n"
"v_mfma_f32_16x16x32_bf16 v[36:39], v[196:199], v[168:171], v[36:39]\n"
"v_mfma_f32_16x16x32_bf16 v[36:39], a[60:63], v[164:167], v[36:39]\n"
"v_mfma_f32_16x16x32_bf16 v[36:39], a[36:39], v[160:163], v[36:39]\n"
"s_waitcnt vmcnt(15)\n"
"v_mfma_f32_16x16x32_bf16 v[40:43], a[16:19], v[176:179], v[40:43]\n"
"s_add_u32 s86, s85, 12288\n"
"v_mfma_f32_16x16x32_bf16 v[40:43], a[40:43], v[180:183], v[40:43]\n"
"buffer_load_dwordx4 v[160:163], v2, s[52:55], s86 offen offset:0 sc1\n"
"v_mfma_f32_16x16x32_bf16 v[40:43], a[64:67], v[184:187], v[40:43]\n"
"v_mfma_f32_16x16x32_bf16 v[40:43], v[200:203], v[188:191], v[40:43]\n"
"v_mfma_f32_16x16x32_bf16 v[44:47], v[204:207], v[188:191], v[44:47]\n"
"buffer_load_dwordx4 v[164:167], v2, s[52:55], s86 offen offset:1024 sc1\n"
"v_mfma_f32_16x16x32_bf16 v[44:47], a[68:71], v[184:187], v[44:47]\n"
"v_mfma_f32_16x16x32_bf16 v[44:47], a[44:47], v[180:183], v[44:47]\n"
"v_mfma_f32_16x16x32_bf16 v[44:47], a[20:23], v[176:179], v[44:47]\n"
"buffer_load_dwordx4 v[168:171], v2, s[52:55], s86 offen offset:2048 sc1\n"
"v_mfma_f32_16x16x32_bf16 v[48:51], a[24:27], v[176:179], v[48:51]\n"
"v_mfma_f32_16x16x32_bf16 v[48:51], a[48:51], v[180:183], v[48:51]\n"
"v_mfma_f32_16x16x32_bf16 v[48:51], a[72:75], v[184:187], v[48:51]\n"
"buffer_load_dwordx4 v[172:175], v2, s[52:55], s86 offen offset:3072 sc1\n"
"v_mfma_f32_16x16x32_bf16 v[48:51], v[208:211], v[188:191], v[48:51]\n"
"v_mfma_f32_16x16x32_bf16 v[52:55], v[212:215], v[188:191], v[52:55]\n"
"s_mov_b32 s95, 3072\n"
"s_add_u32 m0, s83, 82944\n"
"v_mfma_f32_16x16x32_bf16 v[52:55], a[76:79], v[184:187], v[52:55]\n"
"buffer_load_dwordx4 v9, s[56:59], s95 offen sc1 lds\n"
"v_mfma_f32_16x16x32_bf16 v[52:55], a[52:55], v[180:183], v[52:55]\n"
"v_mfma_f32_16x16x32_bf16 v[52:55], a[28:31], v[176:179], v[52:55]\n"
"s_mov_b32 s95, 4096\n"
"s_add_u32 m0, s83, 83968\n"
"v_mfma_f32_16x16x32_bf16 v[56:59], a[32:35], v[176:179], v[56:59]\n"
"buffer_load_dwordx4 v9, s[56:59], s95 offen sc1 lds\n"
"v_mfma_f32_16x16x32_bf16 v[56:59], a[56:59], v[180:183], v[56:59]\n"
"v_mfma_f32_16x16x32_bf16 v[56:59], v[192:195], v[184:187], v[56:59]\n"
"s_mov_b32 s95, 5120\n"
"s_add_u32 m0, s83, 84992\n"
"v_mfma_f32_16x16x32_bf16 v[56:59], v[216:219], v[188:191], v[56:59]\n"
"buffer_load_dwordx4 v9, s[56:59], s95 offen sc1 lds\n"
"v_mfma_f32_16x16x32_bf16 v[60:63], v[220:223], v[188:191], v[60:63]\n"
"v_mfma_f32_16x16x32_bf16 v[60:63], v[196:199], v[184:187], v[60:63]\n"
"v_mfma_f32_16x16x32_bf16 v[60:63], a[60:63], v[180:183], v[60:63]\n"
"v_mfma_f32_16x16x32_bf16 v[60:63], a[36:39], v[176:179], v[60:63]\n"
"s_waitcnt vmcnt(18)\n"
"v_mfma_f32_16x16x32_bf16 v[64:67], a[16:19], v[112:115], v[64:67]\n"
"s_add_u32 s86, s85, 12288\n"
"v_mfma_f32_16x16x32_bf16 v[64:67], a[40:43], v[116:119], v[64:67]\n"
"buffer_load_dwordx4 v[176:179], v12, s[52:55], s86 offen offset:0 sc1\n"
"v_mfma_f32_16x16x32_bf16 v[64:67], a[64:67], v[120:123], v[64:67]\n"
"v_mfma_f32_16x16x32_bf16 v[64:67], v[200:203], v[124:127], v[64:67]\n"
"v_mfma_f32_16x16x32_bf16 v[68:71], v[204:207], v[124:127], v[68:71]\n"
"buffer_load_dwordx4 v[180:183], v12, s[52:55], s86 offen offset:1024 sc1\n"
"v_mfma_f32_16x16x32_bf16 v[68:71], a[68:71], v[120:123], v[68:71]\n"
"v_mfma_f32_16x16x32_bf16 v[68:71], a[44:47], v[116:119], v[68:71]\n"
"v_mfma_f32_16x16x32_bf16 v[68:71], a[20:23], v[112:115], v[68:71]\n"
"v_mfma_f32_16x16x32_bf16 v[72:75], a[24:27], v[112:115], v[72:75]\n"
"buffer_load_dwordx4 v[184:187], v12, s[52:55], s86 offen offset:2048 sc1\n"
"v_mfma_f32_16x16x32_bf16 v[72:75], a[48:51], v[116:119], v[72:75]\n"
"v_mfma_f32_16x16x32_bf16 v[72:75], a[72:75], v[120:123], v[72:75]\n"
"v_mfma_f32_16x16x32_bf16 v[72:75], v[208:211], v[124:127], v[72:75]\n"
"v_mfma_f32_16x16x32_bf16 v[76:79], v[212:215], v[124:127], v[76:79]\n"
"buffer_load_dwordx4 v[188:191], v12, s[52:55], s86 offen offset:3072 sc1\n"
"v_mfma_f32_16x16x32_bf16 v[76:79], a[76:79], v[120:123], v[76:79]\n"
"v_mfma_f32_16x16x32_bf16 v[76:79], a[52:55], v[116:119], v[76:79]\n"
"s_mov_b32 s95, 6144\n"
"s_add_u32 m0, s83, 98304\n"
"v_mfma_f32_16x16x32_bf16 v[76:79], a[28:31], v[112:115], v[76:79]\n"
"buffer_load_dwordx4 v9, s[56:59], s95 offen sc1 lds\n"
"v_mfma_f32_16x16x32_bf16 v[80:83], a[32:35], v[112:115], v[80:83]\n"
"v_mfma_f32_16x16x32_bf16 v[80:83], a[56:59], v[116:119], v[80:83]\n"
"v_mfma_f32_16x16x32_bf16 v[80:83], v[192:195], v[120:123], v[80:83]\n"
"s_mov_b32 s95, 7168\n"
"s_add_u32 m0, s83, 99328\n"
"v_mfma_f32_16x16x32_bf16 v[80:83], v[216:219], v[124:127], v[80:83]\n"
"buffer_load_dwordx4 v9, s[56:59], s95 offen sc1 lds\n"
"v_mfma_f32_16x16x32_bf16 v[84:87], v[220:223], v[124:127], v[84:87]\n"
"v_mfma_f32_16x16x32_bf16 v[84:87], v[196:199], v[120:123], v[84:87]\n"
"v_mfma_f32_16x16x32_bf16 v[84:87], a[60:63], v[116:119], v[84:87]\n"
"v_mfma_f32_16x16x32_bf16 v[84:87], a[36:39], v[112:115], v[84:87]\n"
"s_waitcnt vmcnt(20)\n"
"v_mfma_f32_16x16x32_bf16 v[88:91], a[16:19], v[128:131], v[88:91]\n"
"s_add_u32 s86, s85, 12288\n"
"v_mfma_f32_16x16x32_bf16 v[88:91], a[40:43], v[132:135], v[88:91]\n"
"buffer_load_dwordx4 v[112:115], v13, s[52:55], s86 offen offset:0 sc1\n"
"v_mfma_f32_16x16x32_bf16 v[88:91], a[64:67], v[136:139], v[88:91]\n"
"v_mfma_f32_16x16x32_bf16 v[88:91], v[200:203], v[140:143], v[88:91]\n"
"v_mfma_f32_16x16x32_bf16 v[92:95], v[204:207], v[140:143], v[92:95]\n"
"buffer_load_dwordx4 v[116:119], v13, s[52:55], s86 offen offset:1024 sc1\n"
"v_mfma_f32_16x16x32_bf16 v[92:95], a[68:71], v[136:139], v[92:95]\n"
"v_mfma_f32_16x16x32_bf16 v[92:95], a[44:47], v[132:135], v[92:95]\n"
"v_mfma_f32_16x16x32_bf16 v[92:95], a[20:23], v[128:131], v[92:95]\n"
"v_mfma_f32_16x16x32_bf16 v[96:99], a[24:27], v[128:131], v[96:99]\n"
"buffer_load_dwordx4 v[120:123], v13, s[52:55], s86 offen offset:2048 sc1\n"
"v_mfma_f32_16x16x32_bf16 v[96:99], a[48:51], v[132:135], v[96:99]\n"
"v_mfma_f32_16x16x32_bf16 v[96:99], a[72:75], v[136:139], v[96:99]\n"
"v_mfma_f32_16x16x32_bf16 v[96:99], v[208:211], v[140:143], v[96:99]\n"
"v_mfma_f32_16x16x32_bf16 v[100:103], v[212:215], v[140:143], v[100:103]\n"
"buffer_load_dwordx4 v[124:127], v13, s[52:55], s86 offen offset:3072 sc1\n"
"v_mfma_f32_16x16x32_bf16 v[100:103], a[76:79], v[136:139], v[100:103]\n"
"v_mfma_f32_16x16x32_bf16 v[100:103], a[52:55], v[132:135], v[100:103]\n"
"s_mov_b32 s95, 8192\n"
"s_add_u32 m0, s83, 100352\n"
"v_mfma_f32_16x16x32_bf16 v[100:103], a[28:31], v[128:131], v[100:103]\n"
"buffer_load_dwordx4 v9, s[56:59], s95 offen sc1 lds\n"
"v_mfma_f32_16x16x32_bf16 v[104:107], a[32:35], v[128:131], v[104:107]\n"
"v_mfma_f32_16x16x32_bf16 v[104:107], a[56:59], v[132:135], v[104:107]\n"
"v_mfma_f32_16x16x32_bf16 v[104:107], v[192:195], v[136:139], v[104:107]\n"
"s_mov_b32 s95, 9216\n"
"s_add_u32 m0, s83, 101376\n"
"v_mfma_f32_16x16x32_bf16 v[104:107], v[216:219], v[140:143], v[104:107]\n"
"buffer_load_dwordx4 v9, s[56:59], s95 offen sc1 lds\n"
"v_mfma_f32_16x16x32_bf16 v[108:111], v[220:223], v[140:143], v[108:111]\n"
"v_mfma_f32_16x16x32_bf16 v[108:111], v[196:199], v[136:139], v[108:111]\n"
"v_mfma_f32_16x16x32_bf16 v[108:111], a[60:63], v[132:135], v[108:111]\n"
"v_mfma_f32_16x16x32_bf16 v[108:111], a[36:39], v[128:131], v[108:111]\n"
"s_waitcnt vmcnt(22)\n"
"v_mfma_f32_16x16x32_bf16 v[16:19], v[224:227], v[144:147], v[16:19]\n"
"s_add_u32 s86, s87, 12288\n"
"v_mfma_f32_16x16x32_bf16 v[16:19], a[212:215], v[148:151], v[16:19]\n"
"buffer_load_dwordx4 v[128:131], v1, s[52:55], s86 offen offset:0 sc1\n"
"v_mfma_f32_16x16x32_bf16 v[16:19], a[236:239], v[152:155], v[16:19]\n"
"v_mfma_f32_16x16x32_bf16 v[16:19], a[84:87], v[156:159], v[16:19]\n"
"v_mfma_f32_16x16x32_bf16 v[20:23], a[88:91], v[156:159], v[20:23]\n"
"buffer_load_dwordx4 v[132:135], v1, s[52:55], s86 offen offset:1024 sc1\n"
"v_mfma_f32_16x16x32_bf16 v[20:23], a[240:243], v[152:155], v[20:23]\n"
"v_mfma_f32_16x16x32_bf16 v[20:23], a[216:219], v[148:151], v[20:23]\n"
"v_mfma_f32_16x16x32_bf16 v[20:23], v[228:231], v[144:147], v[20:23]\n"
"v_mfma_f32_16x16x32_bf16 v[24:27], v[232:235], v[144:147], v[24:27]\n"
"buffer_load_dwordx4 v[136:139], v1, s[52:55], s86 offen offset:2048 sc1\n"
"v_mfma_f32_16x16x32_bf16 v[24:27], a[220:223], v[148:151], v[24:27]\n"
"v_mfma_f32_16x16x32_bf16 v[24:27], a[244:247], v[152:155], v[24:27]\n"
"v_mfma_f32_16x16x32_bf16 v[24:27], a[92:95], v[156:159], v[24:27]\n"
"v_mfma_f32_16x16x32_bf16 v[28:31], a[96:99], v[156:159], v[28:31]\n"
"buffer_load_dwordx4 v[140:143], v1, s[52:55], s86 offen offset:3072 sc1\n"
"v_mfma_f32_16x16x32_bf16 v[28:31], a[248:251], v[152:155], v[28:31]\n"
"ds_write_b128 v3, v[16:19] offset:0\n"
"v_mfma_f32_16x16x32_bf16 v[28:31], a[224:227], v[148:151], v[28:31]\n"
"s_mov_b32 s95, 10240\n"
"s_add_u32 m0, s83, 114688\n"
"v_mfma_f32_16x16x32_bf16 v[28:31], v[236:239], v[144:147], v[28:31]\n"
"buffer_load_dwordx4 v9, s[56:59], s95 offen sc1 lds\n"
"v_mfma_f32_16x16x32_bf16 v[32:35], v[240:243], v[144:147], v[32:35]\n"
"ds_write_b128 v3, v[20:23] offset:1024\n"
"v_mfma_f32_16x16x32_bf16 v[32:35], a[228:231], v[148:151], v[32:35]\n"
"v_mfma_f32_16x16x32_bf16 v[32:35], a[252:255], v[152:155], v[32:35]\n"
"s_mov_b32 s95, 11264\n"
"s_add_u32 m0, s83, 115712\n"
"v_mfma_f32_16x16x32_bf16 v[32:35], a[100:103], v[156:159], v[32:35]\n"
"buffer_load_dwordx4 v9, s[56:59], s95 offen sc1 lds\n"
"v_mfma_f32_16x16x32_bf16 v[36:39], a[104:107], v[156:159], v[36:39]\n"
"ds_write_b128 v3, v[24:27] offset:2048\n"
"v_mfma_f32_16x16x32_bf16 v[36:39], a[80:83], v[152:155], v[36:39]\n"
"v_mfma_f32_16x16x32_bf16 v[36:39], a[232:235], v[148:151], v[36:39]\n"
"v_mfma_f32_16x16x32_bf16 v[36:39], v[244:247], v[144:147], v[36:39]\n"
"s_waitcnt vmcnt(21)\n"
"v_mfma_f32_16x16x32_bf16 v[40:43], v[224:227], v[160:163], v[40:43]\n"
"s_add_u32 s86, s87, 12288\n"
"ds_write_b128 v3, v[28:31] offset:3072\n"
"v_mfma_f32_16x16x32_bf16 v[40:43], a[212:215], v[164:167], v[40:43]\n"
"buffer_load_dwordx4 v[144:147], v2, s[52:55], s86 offen offset:0 sc1\n"
"v_mfma_f32_16x16x32_bf16 v[40:43], a[236:239], v[168:171], v[40:43]\n"
"v_mfma_f32_16x16x32_bf16 v[40:43], a[84:87], v[172:175], v[40:43]\n"
"v_mfma_f32_16x16x32_bf16 v[44:47], a[88:91], v[172:175], v[44:47]\n"
"buffer_load_dwordx4 v[148:151], v2, s[52:55], s86 offen offset:1024 sc1\n"
"v_mfma_f32_16x16x32_bf16 v[44:47], a[240:243], v[168:171], v[44:47]\n"
"ds_write_b128 v3, v[32:35] offset:16384\n"
"v_mfma_f32_16x16x32_bf16 v[44:47], a[216:219], v[164:167], v[44:47]\n"
"v_mfma_f32_16x16x32_bf16 v[44:47], v[228:231], v[160:163], v[44:47]\n"
"v_mfma_f32_16x16x32_bf16 v[48:51], v[232:235], v[160:163], v[48:51]\n"
"buffer_load_dwordx4 v[152:155], v2, s[52:55], s86 offen offset:2048 sc1\n"
"v_mfma_f32_16x16x32_bf16 v[48:51], a[220:223], v[164:167], v[48:51]\n"
"ds_write_b128 v3, v[36:39] offset:17408\n"
"v_mfma_f32_16x16x32_bf16 v[48:51], a[244:247], v[168:171], v[48:51]\n"
"v_mfma_f32_16x16x32_bf16 v[48:51], a[92:95], v[172:175], v[48:51]\n"
"v_mfma_f32_16x16x32_bf16 v[52:55], a[96:99], v[172:175], v[52:55]\n"
"buffer_load_dwordx4 v[156:159], v2, s[52:55], s86 offen offset:3072 sc1\n"
"v_mfma_f32_16x16x32_bf16 v[52:55], a[248:251], v[168:171], v[52:55]\n"
"ds_write_b128 v3, v[40:43] offset:18432\n"
"v_mfma_f32_16x16x32_bf16 v[52:55], a[224:227], v[164:167], v[52:55]\n"
"s_mov_b32 s95, 12288\n"
"s_add_u32 m0, s83, 116736\n"
"v_mfma_f32_16x16x32_bf16 v[52:55], v[236:239], v[160:163], v[52:55]\n"
"buffer_load_dwordx4 v9, s[56:59], s95 offen sc1 lds\n"
"v_mfma_f32_16x16x32_bf16 v[56:59], v[240:243], v[160:163], v[56:59]\n"
"ds_write_b128 v3, v[44:47] offset:19456\n"
"v_mfma_f32_16x16x32_bf16 v[56:59], a[228:231], v[164:167], v[56:59]\n"
"v_mfma_f32_16x16x32_bf16 v[56:59], a[252:255], v[168:171], v[56:59]\n"
"s_mov_b32 s95, 13312\n"
"s_add_u32 m0, s83, 117760\n"
"v_mfma_f32_16x16x32_bf16 v[56:59], a[100:103], v[172:175], v[56:59]\n"
"buffer_load_dwordx4 v9, s[56:59], s95 offen sc1 lds\n"
"v_mfma_f32_16x16x32_bf16 v[60:63], a[104:107], v[172:175], v[60:63]\n"
"ds_write_b128 v3, v[48:51] offset:32768\n"
"v_mfma_f32_16x16x32_bf16 v[60:63], a[80:83], v[168:171], v[60:63]\n"
"v_mfma_f32_16x16x32_bf16 v[60:63], a[232:235], v[164:167], v[60:63]\n"
"v_mfma_f32_16x16x32_bf16 v[60:63], v[244:247], v[160:163], v[60:63]\n"
"s_waitcnt vmcnt(20)\n"
"v_mfma_f32_16x16x32_bf16 v[64:67], v[224:227], v[176:179], v[64:67]\n"
"s_add_u32 s86, s87, 12288\n"
"ds_write_b128 v3, v[52:55] offset:33792\n"
"v_mfma_f32_16x16x32_bf16 v[64:67], a[212:215], v[180:183], v[64:67]\n"
"buffer_load_dwordx4 v[160:163], v12, s[52:55], s86 offen offset:0 sc1\n"
"v_mfma_f32_16x16x32_bf16 v[64:67], a[236:239], v[184:187], v[64:67]\n"
"v_mfma_f32_16x16x32_bf16 v[64:67], a[84:87], v[188:191], v[64:67]\n"
"v_mfma_f32_16x16x32_bf16 v[68:71], a[88:91], v[188:191], v[68:71]\n"
"buffer_load_dwordx4 v[164:167], v12, s[52:55], s86 offen offset:1024 sc1\n"
"v_mfma_f32_16x16x32_bf16 v[68:71], a[240:243], v[184:187], v[68:71]\n"
"ds_write_b128 v3, v[56:59] offset:34816\n"
"v_mfma_f32_16x16x32_bf16 v[68:71], a[216:219], v[180:183], v[68:71]\n"
"v_mfma_f32_16x16x32_bf16 v[68:71], v[228:231], v[176:179], v[68:71]\n"
"v_mfma_f32_16x16x32_bf16 v[72:75], v[232:235], v[176:179], v[72:75]\n"
"buffer_load_dwordx4 v[168:171], v12, s[52:55], s86 offen offset:2048 sc1\n"
"v_mfma_f32_16x16x32_bf16 v[72:75], a[220:223], v[180:183], v[72:75]\n"
"ds_write_b128 v3, v[60:63] offset:35840\n"
"v_mfma_f32_16x16x32_bf16 v[72:75], a[244:247], v[184:187], v[72:75]\n"
"v_mfma_f32_16x16x32_bf16 v[72:75], a[92:95], v[188:191], v[72:75]\n"
"v_mfma_f32_16x16x32_bf16 v[76:79], a[96:99], v[188:191], v[76:79]\n"
"buffer_load_dwordx4 v[172:175], v12, s[52:55], s86 offen offset:3072 sc1\n"
"v_mfma_f32_16x16x32_bf16 v[76:79], a[248:251], v[184:187], v[76:79]\n"
"ds_write_b128 v3, v[64:67] offset:49152\n"
"v_mfma_f32_16x16x32_bf16 v[76:79], a[224:227], v[180:183], v[76:79]\n"
"s_mov_b32 s95, 14336\n"
"s_add_u32 m0, s94, 0\n"
"v_mfma_f32_16x16x32_bf16 v[76:79], v[236:239], v[176:179], v[76:79]\n"
"buffer_load_dwordx4 v9, s[56:59], s95 offen sc1 lds\n"
"v_mfma_f32_16x16x32_bf16 v[80:83], v[240:243], v[176:179], v[80:83]\n"
"ds_write_b128 v3, v[68:71] offset:50176\n"
"v_mfma_f32_16x16x32_bf16 v[80:83], a[228:231], v[180:183], v[80:83]\n"
"v_mfma_f32_16x16x32_bf16 v[80:83], a[252:255], v[184:187], v[80:83]\n"
"s_mov_b32 s95, 15360\n"
"s_add_u32 m0, s94, 1024\n"
"v_mfma_f32_16x16x32_bf16 v[80:83], a[100:103], v[188:191], v[80:83]\n"
"buffer_load_dwordx4 v9, s[56:59], s95 offen sc1 lds\n"
"v_mfma_f32_16x16x32_bf16 v[84:87], a[104:107], v[188:191], v[84:87]\n"
"ds_write_b128 v3, v[72:75] offset:51200\n"
"v_mfma_f32_16x16x32_bf16 v[84:87], a[80:83], v[184:187], v[84:87]\n"
"v_mfma_f32_16x16x32_bf16 v[84:87], a[232:235], v[180:183], v[84:87]\n"
"v_mfma_f32_16x16x32_bf16 v[84:87], v[244:247], v[176:179], v[84:87]\n"
"s_waitcnt vmcnt(20)\n"
"v_mfma_f32_16x16x32_bf16 v[88:91], v[224:227], v[112:115], v[88:91]\n"
"s_add_u32 s86, s87, 12288\n"
"ds_write_b128 v3, v[76:79] offset:52224\n"
"v_mfma_f32_16x16x32_bf16 v[88:91], a[212:215], v[116:119], v[88:91]\n"
"buffer_load_dwordx4 v[176:179], v13, s[52:55], s86 offen offset:0 sc1\n"
"v_mfma_f32_16x16x32_bf16 v[88:91], a[236:239], v[120:123], v[88:91]\n"
"v_mfma_f32_16x16x32_bf16 v[88:91], a[84:87], v[124:127], v[88:91]\n"
"v_mfma_f32_16x16x32_bf16 v[92:95], a[88:91], v[124:127], v[92:95]\n"
"buffer_load_dwordx4 v[180:183], v13, s[52:55], s86 offen offset:1024 sc1\n"
"v_mfma_f32_16x16x32_bf16 v[92:95], a[240:243], v[120:123], v[92:95]\n"
"ds_write_b128 v4, v[80:83] offset:0\n"
"v_mfma_f32_16x16x32_bf16 v[92:95], a[216:219], v[116:119], v[92:95]\n"
"v_mfma_f32_16x16x32_bf16 v[92:95], v[228:231], v[112:115], v[92:95]\n"
"v_mfma_f32_16x16x32_bf16 v[96:99], v[232:235], v[112:115], v[96:99]\n"
"buffer_load_dwordx4 v[184:187], v13, s[52:55], s86 offen offset:2048 sc1\n"
"v_mfma_f32_16x16x32_bf16 v[96:99], a[220:223], v[116:119], v[96:99]\n"
"ds_write_b128 v4, v[84:87] offset:1024\n"
"v_mfma_f32_16x16x32_bf16 v[96:99], a[244:247], v[120:123], v[96:99]\n"
"v_mfma_f32_16x16x32_bf16 v[96:99], a[92:95], v[124:127], v[96:99]\n"
"v_mfma_f32_16x16x32_bf16 v[100:103], a[96:99], v[124:127], v[100:103]\n"
"buffer_load_dwordx4 v[188:191], v13, s[52:55], s86 offen offset:3072 sc1\n"
"v_mfma_f32_16x16x32_bf16 v[100:103], a[248:251], v[120:123], v[100:103]\n"
"v_mfma_f32_16x16x32_bf16 v[100:103], a[224:227], v[116:119], v[100:103]\n"
"s_mov_b32 s95, 16384\n"
"s_add_u32 m0, s94, 2048\n"
"v_mfma_f32_16x16x32_bf16 v[100:103], v[236:239], v[112:115], v[100:103]\n"
"buffer_load_dwordx4 v9, s[56:59], s95 offen sc1 lds\n"
"v_mfma_f32_16x16x32_bf16 v[104:107], v[240:243], v[112:115], v[104:107]\n"
"v_mfma_f32_16x16x32_bf16 v[104:107], a[228:231], v[116:119], v[104:107]\n"
"v_mfma_f32_16x16x32_bf16 v[104:107], a[252:255], v[120:123], v[104:107]\n"
"s_mov_b32 s95, 17408\n"
"s_add_u32 m0, s94, 3072\n"
"v_mfma_f32_16x16x32_bf16 v[104:107], a[100:103], v[124:127], v[104:107]\n"
"buffer_load_dwordx4 v9, s[56:59], s95 offen sc1 lds\n"
"v_mfma_f32_16x16x32_bf16 v[108:111], a[104:107], v[124:127], v[108:111]\n"
"v_mfma_f32_16x16x32_bf16 v[108:111], a[80:83], v[120:123], v[108:111]\n"
"v_mfma_f32_16x16x32_bf16 v[108:111], a[232:235], v[116:119], v[108:111]\n"
"v_mfma_f32_16x16x32_bf16 v[108:111], v[244:247], v[112:115], v[108:111]\n"
"s_waitcnt lgkmcnt(0)\n"
"s_barrier\n"
"ds_read_b128 v[16:19], v6 offset:0\n"
"ds_read_b128 v[20:23], v6 offset:1024\n"
"ds_read_b128 v[24:27], v6 offset:2048\n"
"ds_read_b128 v[28:31], v6 offset:3072\n"
"ds_read_b128 v[32:35], v6 offset:16384\n"
"ds_read_b128 v[36:39], v6 offset:17408\n"
"ds_read_b128 v[40:43], v7 offset:18432\n"
"ds_read_b128 v[44:47], v7 offset:19456\n"
"ds_read_b128 v[48:51], v7 offset:32768\n"
"ds_read_b128 v[52:55], v7 offset:33792\n"
"ds_read_b128 v[56:59], v7 offset:34816\n"
"ds_read_b128 v[60:63], v7 offset:35840\n"
"ds_read_b128 v[64:67], v8 offset:49152\n"
"ds_read_b128 v[68:71], v8 offset:50176\n"
"ds_read_b128 v[72:75], v8 offset:51200\n"
"ds_read_b128 v[76:79], v8 offset:52224\n"
"ds_read_b128 v[80:83], v10 offset:0\n"
"ds_read_b128 v[84:87], v10 offset:1024\n"
"s_waitcnt lgkmcnt(12)\n"
"v_pk_add_f32 v[88:89], v[88:89], v[16:17]\n"
"v_pk_add_f32 v[90:91], v[90:91], v[18:19]\n"
"v_pk_add_f32 v[92:93], v[92:93], v[20:21]\n"
"v_pk_add_f32 v[94:95], v[94:95], v[22:23]\n"
"v_pk_add_f32 v[96:97], v[96:97], v[24:25]\n"
"v_pk_add_f32 v[98:99], v[98:99], v[26:27]\n"
"v_pk_add_f32 v[100:101], v[100:101], v[28:29]\n"
"v_pk_add_f32 v[102:103], v[102:103], v[30:31]\n"
"v_pk_add_f32 v[104:105], v[104:105], v[32:33]\n"
"v_pk_add_f32 v[106:107], v[106:107], v[34:35]\n"
"v_pk_add_f32 v[108:109], v[108:109], v[36:37]\n"
"v_pk_add_f32 v[110:111], v[110:111], v[38:39]\n"
"s_waitcnt lgkmcnt(6)\n"
"v_pk_add_f32 v[88:89], v[88:89], v[40:41]\n"
"v_pk_add_f32 v[90:91], v[90:91], v[42:43]\n"
"v_pk_add_f32 v[92:93], v[92:93], v[44:45]\n"
"v_pk_add_f32 v[94:95], v[94:95], v[46:47]\n"
"v_pk_add_f32 v[96:97], v[96:97], v[48:49]\n"
"v_pk_add_f32 v[98:99], v[98:99], v[50:51]\n"
"v_pk_add_f32 v[100:101], v[100:101], v[52:53]\n"
"v_pk_add_f32 v[102:103], v[102:103], v[54:55]\n"
"v_pk_add_f32 v[104:105], v[104:105], v[56:57]\n"
"v_pk_add_f32 v[106:107], v[106:107], v[58:59]\n"
"v_pk_add_f32 v[108:109], v[108:109], v[60:61]\n"
"v_pk_add_f32 v[110:111], v[110:111], v[62:63]\n"
"s_waitcnt lgkmcnt(0)\n"
"v_pk_add_f32 v[88:89], v[88:89], v[64:65]\n"
"v_pk_add_f32 v[90:91], v[90:91], v[66:67]\n"
"v_pk_add_f32 v[92:93], v[92:93], v[68:69]\n"
"v_pk_add_f32 v[94:95], v[94:95], v[70:71]\n"
"v_pk_add_f32 v[96:97], v[96:97], v[72:73]\n"
"v_pk_add_f32 v[98:99], v[98:99], v[74:75]\n"
"v_pk_add_f32 v[100:101], v[100:101], v[76:77]\n"
"v_pk_add_f32 v[102:103], v[102:103], v[78:79]\n"
"v_pk_add_f32 v[104:105], v[104:105], v[80:81]\n"
"v_pk_add_f32 v[106:107], v[106:107], v[82:83]\n"
"v_pk_add_f32 v[108:109], v[108:109], v[84:85]\n"
"v_pk_add_f32 v[110:111], v[110:111], v[86:87]\n"
"s_waitcnt vmcnt(20)\n"
"v_mfma_f32_16x16x32_bf16 v[16:19], v[224:227], v[128:131], 0\n"
"s_add_u32 s86, s87, 8192\n"
"v_mfma_f32_16x16x32_bf16 v[16:19], a[212:215], v[132:135], v[16:19]\n"
"buffer_load_dwordx4 v[112:115], v1, s[52:55], s86 offen offset:0 sc1\n"
"v_mfma_f32_16x16x32_bf16 v[16:19], a[236:239], v[136:139], v[16:19]\n"
"v_mfma_f32_16x16x32_bf16 v[16:19], a[84:87], v[140:143], v[16:19]\n"
"buffer_load_dwordx4 v[116:119], v1, s[52:55], s86 offen offset:1024 sc1\n"
"v_mfma_f32_16x16x32_bf16 v[20:23], a[88:91], v[140:143], 0\n"
"v_mfma_f32_16x16x32_bf16 v[20:23], a[240:243], v[136:139], v[20:23]\n"
"buffer_load_dwordx4 v[120:123], v1, s[52:55], s86 offen offset:2048 sc1\n"
"v_mfma_f32_16x16x32_bf16 v[20:23], a[216:219], v[132:135], v[20:23]\n"
"v_mfma_f32_16x16x32_bf16 v[20:23], v[228:231], v[128:131], v[20:23]\n"
"buffer_load_dwordx4 v[124:127], v1, s[52:55], s86 offen offset:3072 sc1\n"
"v_mfma_f32_16x16x32_bf16 v[24:27], v[232:235], v[128:131], 0\n"
"v_mfma_f32_16x16x32_bf16 v[24:27], a[220:223], v[132:135], v[24:27]\n"
"buffer_store_dwordx4 v[88:91], v9, s[36:39], 0 offen offset:0 sc1\n"
"v_mfma_f32_16x16x32_bf16 v[24:27], a[244:247], v[136:139], v[24:27]\n"
"v_mfma_f32_16x16x32_bf16 v[24:27], a[92:95], v[140:143], v[24:27]\n"
"v_mfma_f32_16x16x32_bf16 v[28:31], a[96:99], v[140:143], 0\n"
"buffer_store_dwordx4 v[92:95], v9, s[36:39], 0 offen offset:1024 sc1\n"
"v_mfma_f32_16x16x32_bf16 v[28:31], a[248:251], v[136:139], v[28:31]\n"
"v_mfma_f32_16x16x32_bf16 v[28:31], a[224:227], v[132:135], v[28:31]\n"
"buffer_store_dwordx4 v[96:99], v9, s[36:39], 0 offen offset:2048 sc1\n"
"v_mfma_f32_16x16x32_bf16 v[28:31], v[236:239], v[128:131], v[28:31]\n"
"v_mfma_f32_16x16x32_bf16 v[32:35], v[240:243], v[128:131], 0\n"
"buffer_store_dwordx4 v[100:103], v9, s[36:39], 0 offen offset:3072 sc1\n"
"v_mfma_f32_16x16x32_bf16 v[32:35], a[228:231], v[132:135], v[32:35]\n"
"v_mfma_f32_16x16x32_bf16 v[32:35], a[252:255], v[136:139], v[32:35]\n"
"buffer_store_dwordx4 v[104:107], v14, s[36:39], 0 offen offset:0 sc1\n"
"v_mfma_f32_16x16x32_bf16 v[32:35], a[100:103], v[140:143], v[32:35]\n"
"v_mfma_f32_16x16x32_bf16 v[36:39], a[104:107], v[140:143], 0\n"
"buffer_store_dwordx4 v[108:111], v14, s[36:39], 0 offen offset:1024 sc1\n"
"v_mfma_f32_16x16x32_bf16 v[36:39], a[80:83], v[136:139], v[36:39]\n"
"v_mfma_f32_16x16x32_bf16 v[36:39], a[232:235], v[132:135], v[36:39]\n"
"v_mfma_f32_16x16x32_bf16 v[36:39], v[244:247], v[128:131], v[36:39]\n"
"s_waitcnt vmcnt(24)\n"
"v_mfma_f32_16x16x32_bf16 v[40:43], v[224:227], v[144:147], 0\n"
"s_add_u32 s86, s87, 8192\n"
"v_mfma_f32_16x16x32_bf16 v[40:43], a[212:215], v[148:151], v[40:43]\n"
"buffer_load_dwordx4 v[128:131], v2, s[52:55], s86 offen offset:0 sc1\n"
"v_mfma_f32_16x16x32_bf16 v[40:43], a[236:239], v[152:155], v[40:43]\n"
"v_mfma_f32_16x16x32_bf16 v[40:43], a[84:87], v[156:159], v[40:43]\n"
"v_mfma_f32_16x16x32_bf16 v[44:47], a[88:91], v[156:159], 0\n"
"v_mfma_f32_16x16x32_bf16 v[44:47], a[240:243], v[152:155], v[44:47]\n"
"v_mfma_f32_16x16x32_bf16 v[44:47], a[216:219], v[148:151], v[44:47]\n"
"buffer_load_dwordx4 v[132:135], v2, s[52:55], s86 offen offset:1024 sc1\n"
"v_mfma_f32_16x16x32_bf16 v[44:47], v[228:231], v[144:147], v[44:47]\n"
"v_mfma_f32_16x16x32_bf16 v[48:51], v[232:235], v[144:147], 0\n"
"v_mfma_f32_16x16x32_bf16 v[48:51], a[220:223], v[148:151], v[48:51]\n"
"v_mfma_f32_16x16x32_bf16 v[48:51], a[244:247], v[152:155], v[48:51]\n"
"v_mfma_f32_16x16x32_bf16 v[48:51], a[92:95], v[156:159], v[48:51]\n"
"v_mfma_f32_16x16x32_bf16 v[52:55], a[96:99], v[156:159], 0\n"
"buffer_load_dwordx4 v[136:139], v2, s[52:55], s86 offen offset:2048 sc1\n"
"v_mfma_f32_16x16x32_bf16 v[52:55], a[248:251], v[152:155], v[52:55]\n"
"v_mfma_f32_16x16x32_bf16 v[52:55], a[224:227], v[148:151], v[52:55]\n"
"v_mfma_f32_16x16x32_bf16 v[52:55], v[236:239], v[144:147], v[52:55]\n"
"v_mfma_f32_16x16x32_bf16 v[56:59], v[240:243], v[144:147], 0\n"
"v_mfma_f32_16x16x32_bf16 v[56:59], a[228:231], v[148:151], v[56:59]\n"
"buffer_load_dwordx4 v[140:143], v2, s[52:55], s86 offen offset:3072 sc1\n"
"v_mfma_f32_16x16x32_bf16 v[56:59], a[252:255], v[152:155], v[56:59]\n"
"v_mfma_f32_16x16x32_bf16 v[56:59], a[100:103], v[156:159], v[56:59]\n"
"v_mfma_f32_16x16x32_bf16 v[60:63], a[104:107], v[156:159], 0\n"
"v_mfma_f32_16x16x32_bf16 v[60:63], a[80:83], v[152:155], v[60:63]\n"
"v_mfma_f32_16x16x32_bf16 v[60:63], a[232:235], v[148:151], v[60:63]\n"
"v_mfma_f32_16x16x32_bf16 v[60:63], v[244:247], v[144:147], v[60:63]\n"
"s_waitcnt vmcnt(22)\n"
"v_mfma_f32_16x16x32_bf16 v[64:67], v[224:227], v[160:163], 0\n"
"s_add_u32 s86, s87, 8192\n"
"v_mfma_f32_16x16x32_bf16 v[64:67], a[212:215], v[164:167], v[64:67]\n"
"buffer_load_dwordx4 v[144:147], v12, s[52:55], s86 offen offset:0 sc1\n"
"v_mfma_f32_16x16x32_bf16 v[64:67], a[236:239], v[168:171], v[64:67]\n"
"v_mfma_f32_16x16x32_bf16 v[64:67], a[84:87], v[172:175], v[64:67]\n"
"v_mfma_f32_16x16x32_bf16 v[68:71], a[88:91], v[172:175], 0\n"
"v_mfma_f32_16x16x32_bf16 v[68:71], a[240:243], v[168:171], v[68:71]\n"
"v_mfma_f32_16x16x32_bf16 v[68:71], a[216:219], v[164:167], v[68:71]\n"
"buffer_load_dwordx4 v[148:151], v12, s[52:55], s86 offen offset:1024 sc1\n"
"v_mfma_f32_16x16x32_bf16 v[68:71], v[228:231], v[160:163], v[68:71]\n"
"v_mfma_f32_16x16x32_bf16 v[72:75], v[232:235], v[160:163], 0\n"
"v_mfma_f32_16x16x32_bf16 v[72:75], a[220:223], v[164:167], v[72:75]\n"
"v_mfma_f32_16x16x32_bf16 v[72:75], a[244:247], v[168:171], v[72:75]\n"
"v_mfma_f32_16x16x32_bf16 v[72:75], a[92:95], v[172:175], v[72:75]\n"
"v_mfma_f32_16x16x32_bf16 v[76:79], a[96:99], v[172:175], 0\n"
"buffer_load_dwordx4 v[152:155], v12, s[52:55], s86 offen offset:2048 sc1\n"
"v_mfma_f32_16x16x32_bf16 v[76:79], a[248:251], v[168:171], v[76:79]\n"
"v_mfma_f32_16x16x32_bf16 v[76:79], a[224:227], v[164:167], v[76:79]\n"
"v_mfma_f32_16x16x32_bf16 v[76:79], v[236:239], v[160:163], v[76:79]\n"
"v_mfma_f32_16x16x32_bf16 v[80:83], v[240:243], v[160:163], 0\n"
"v_mfma_f32_16x16x32_bf16 v[80:83], a[228:231], v[164:167], v[80:83]\n"
"buffer_load_dwordx4 v[156:159], v12, s[52:55], s86 offen offset:3072 sc1\n"
"v_mfma_f32_16x16x32_bf16 v[80:83], a[252:255], v[168:171], v[80:83]\n"
"v_mfma_f32_16x16x32_bf16 v[80:83], a[100:103], v[172:175], v[80:83]\n"
"v_mfma_f32_16x16x32_bf16 v[84:87], a[104:107], v[172:175], 0\n"
"v_mfma_f32_16x16x32_bf16 v[84:87], a[80:83], v[168:171], v[84:87]\n"
"v_mfma_f32_16x16x32_bf16 v[84:87], a[232:235], v[164:167], v[84:87]\n"
"v_mfma_f32_16x16x32_bf16 v[84:87], v[244:247], v[160:163], v[84:87]\n"
"s_waitcnt vmcnt(20)\n"
"v_mfma_f32_16x16x32_bf16 v[88:91], v[224:227], v[176:179], 0\n"
"s_add_u32 s86, s87, 8192\n"
"v_mfma_f32_16x16x32_bf16 v[88:91], a[212:215], v[180:183], v[88:91]\n"
"buffer_load_dwordx4 v[160:163], v13, s[52:55], s86 offen offset:0 sc1\n"
"v_mfma_f32_16x16x32_bf16 v[88:91], a[236:239], v[184:187], v[88:91]\n"
"v_mfma_f32_16x16x32_bf16 v[88:91], a[84:87], v[188:191], v[88:91]\n"
"v_mfma_f32_16x16x32_bf16 v[92:95], a[88:91], v[188:191], 0\n"
"v_mfma_f32_16x16x32_bf16 v[92:95], a[240:243], v[184:187], v[92:95]\n"
"v_mfma_f32_16x16x32_bf16 v[92:95], a[216:219], v[180:183], v[92:95]\n"
"buffer_load_dwordx4 v[164:167], v13, s[52:55], s86 offen offset:1024 sc1\n"
"v_mfma_f32_16x16x32_bf16 v[92:95], v[228:231], v[176:179], v[92:95]\n"
"v_mfma_f32_16x16x32_bf16 v[96:99], v[232:235], v[176:179], 0\n"
"v_mfma_f32_16x16x32_bf16 v[96:99], a[220:223], v[180:183], v[96:99]\n"
"v_mfma_f32_16x16x32_bf16 v[96:99], a[244:247], v[184:187], v[96:99]\n"
"v_mfma_f32_16x16x32_bf16 v[96:99], a[92:95], v[188:191], v[96:99]\n"
"v_mfma_f32_16x16x32_bf16 v[100:103], a[96:99], v[188:191], 0\n"
"buffer_load_dwordx4 v[168:171], v13, s[52:55], s86 offen offset:2048 sc1\n"
"v_mfma_f32_16x16x32_bf16 v[100:103], a[248:251], v[184:187], v[100:103]\n"
"v_mfma_f32_16x16x32_bf16 v[100:103], a[224:227], v[180:183], v[100:103]\n"
"v_mfma_f32_16x16x32_bf16 v[100:103], v[236:239], v[176:179], v[100:103]\n"
"v_mfma_f32_16x16x32_bf16 v[104:107], v[240:243], v[176:179], 0\n"
"v_mfma_f32_16x16x32_bf16 v[104:107], a[228:231], v[180:183], v[104:107]\n"
"buffer_load_dwordx4 v[172:175], v13, s[52:55], s86 offen offset:3072 sc1\n"
"v_mfma_f32_16x16x32_bf16 v[104:107], a[252:255], v[184:187], v[104:107]\n"
"v_mfma_f32_16x16x32_bf16 v[104:107], a[100:103], v[188:191], v[104:107]\n"
"v_mfma_f32_16x16x32_bf16 v[108:111], a[104:107], v[188:191], 0\n"
"v_mfma_f32_16x16x32_bf16 v[108:111], a[80:83], v[184:187], v[108:111]\n"
"v_mfma_f32_16x16x32_bf16 v[108:111], a[232:235], v[180:183], v[108:111]\n"
"v_mfma_f32_16x16x32_bf16 v[108:111], v[244:247], v[176:179], v[108:111]\n"
"s_waitcnt vmcnt(18)\n"
"v_mfma_f32_16x16x32_bf16 v[16:19], a[16:19], v[112:115], v[16:19]\n"
"s_add_u32 s86, s87, 4096\n"
"v_mfma_f32_16x16x32_bf16 v[16:19], a[40:43], v[116:119], v[16:19]\n"
"buffer_load_dwordx4 v[176:179], v1, s[52:55], s86 offen offset:0 sc1\n"
"v_mfma_f32_16x16x32_bf16 v[16:19], a[64:67], v[120:123], v[16:19]\n"
"v_mfma_f32_16x16x32_bf16 v[16:19], v[200:203], v[124:127], v[16:19]\n"
"v_add_u32 v6, 0x10000, v6\n"
"v_mfma_f32_16x16x32_bf16 v[20:23], v[204:207], v[124:127], v[20:23]\n"
"v_add_u32 v7, 0x10000, v7\n"
"v_mfma_f32_16x16x32_bf16 v[20:23], a[68:71], v[120:123], v[20:23]\n"
"v_add_u32 v8, 0x10000, v8\n"
"v_mfma_f32_16x16x32_bf16 v[20:23], a[44:47], v[116:119], v[20:23]\n"
"buffer_load_dwordx4 v[180:183], v1, s[52:55], s86 offen offset:1024 sc1\n"
"v_mfma_f32_16x16x32_bf16 v[20:23], a[20:23], v[112:115], v[20:23]\n"
"s_sub_u32 s96, %[wid], 3\n"
"s_and_b32 s96, s96, 3\n"
"s_lshl_b32 s96, s96, 13\n"
"v_mfma_f32_16x16x32_bf16 v[24:27], a[24:27], v[112:115], v[24:27]\n"
"s_add_u32 s96, s96, 131072\n"
"v_mfma_f32_16x16x32_bf16 v[24:27], a[48:51], v[116:119], v[24:27]\n"
"v_add_u32 v10, s96, v9\n"
"v_mfma_f32_16x16x32_bf16 v[24:27], a[72:75], v[120:123], v[24:27]\n"
"v_and_b32 v11, 15, v0\n"
"v_mfma_f32_16x16x32_bf16 v[24:27], v[208:211], v[124:127], v[24:27]\n"
"v_mul_u32_u24 v11, 2304, v11\n"
"v_mfma_f32_16x16x32_bf16 v[28:31], v[212:215], v[124:127], v[28:31]\n"
"buffer_load_dwordx4 v[184:187], v1, s[52:55], s86 offen offset:2048 sc1\n"
"v_bfe_u32 v251, v0, 4, 1\n"
"v_mfma_f32_16x16x32_bf16 v[28:31], a[76:79], v[120:123], v[28:31]\n"
"v_lshl_add_u32 v11, v251, 5, v11\n"
"v_mfma_f32_16x16x32_bf16 v[28:31], a[52:55], v[116:119], v[28:31]\n"
"v_lshrrev_b32 v251, 5, v0\n"
"v_mfma_f32_16x16x32_bf16 v[28:31], a[28:31], v[112:115], v[28:31]\n"
"v_lshl_add_u32 v11, v251, 4, v11\n"
"v_mfma_f32_16x16x32_bf16 v[32:35], a[32:35], v[112:115], v[32:35]\n"
"s_mov_b64 s[60:61], s[20:21]\n"
"s_and_b32 s61, s61, 0xffff\n"
"v_mfma_f32_16x16x32_bf16 v[32:35], a[56:59], v[116:119], v[32:35]\n"
"buffer_load_dwordx4 v[188:191], v1, s[52:55], s86 offen offset:3072 sc1\n"
"s_mov_b32 s62, 34752\n"
"s_mov_b32 s63, 0x20000\n"
"v_mfma_f32_16x16x32_bf16 v[32:35], v[192:195], v[120:123], v[32:35]\n"
"v_mfma_f32_16x16x32_bf16 v[32:35], v[216:219], v[124:127], v[32:35]\n"
"v_mfma_f32_16x16x32_bf16 v[36:39], v[220:223], v[124:127], v[36:39]\n"
"v_mfma_f32_16x16x32_bf16 v[36:39], v[196:199], v[120:123], v[36:39]\n"
"v_mfma_f32_16x16x32_bf16 v[36:39], a[60:63], v[116:119], v[36:39]\n"
"v_mfma_f32_16x16x32_bf16 v[36:39], a[36:39], v[112:115], v[36:39]\n"
"s_waitcnt vmcnt(12)\n"
"v_mfma_f32_16x16x32_bf16 v[40:43], a[16:19], v[128:131], v[40:43]\n"
"s_add_u32 s86, s87, 4096\n"
"v_mfma_f32_16x16x32_bf16 v[40:43], a[40:43], v[132:135], v[40:43]\n"
"buffer_load_dwordx4 v[112:115], v2, s[52:55], s86 offen offset:0 sc1\n"
"v_mfma_f32_16x16x32_bf16 v[40:43], a[64:67], v[136:139], v[40:43]\n"
"v_mfma_f32_16x16x32_bf16 v[40:43], v[200:203], v[140:143], v[40:43]\n"
"v_mfma_f32_16x16x32_bf16 v[44:47], v[204:207], v[140:143], v[44:47]\n"
"v_mfma_f32_16x16x32_bf16 v[44:47], a[68:71], v[136:139], v[44:47]\n"
"buffer_load_dwordx4 v[116:119], v2, s[52:55], s86 offen offset:1024 sc1\n"
"v_mfma_f32_16x16x32_bf16 v[44:47], a[44:47], v[132:135], v[44:47]\n"
"v_mfma_f32_16x16x32_bf16 v[44:47], a[20:23], v[128:131], v[44:47]\n"
"v_mfma_f32_16x16x32_bf16 v[48:51], a[24:27], v[128:131], v[48:51]\n"
"v_mfma_f32_16x16x32_bf16 v[48:51], a[48:51], v[132:135], v[48:51]\n"
"buffer_load_dwordx4 v[120:123], v2, s[52:55], s86 offen offset:2048 sc1\n"
"v_mfma_f32_16x16x32_bf16 v[48:51], a[72:75], v[136:139], v[48:51]\n"
"v_mfma_f32_16x16x32_bf16 v[48:51], v[208:211], v[140:143], v[48:51]\n"
"v_mfma_f32_16x16x32_bf16 v[52:55], v[212:215], v[140:143], v[52:55]\n"
"v_mfma_f32_16x16x32_bf16 v[52:55], a[76:79], v[136:139], v[52:55]\n"
"v_mfma_f32_16x16x32_bf16 v[52:55], a[52:55], v[132:135], v[52:55]\n"
"buffer_load_dwordx4 v[124:127], v2, s[52:55], s86 offen offset:3072 sc1\n"
"v_mfma_f32_16x16x32_bf16 v[52:55], a[28:31], v[128:131], v[52:55]\n"
"v_mfma_f32_16x16x32_bf16 v[56:59], a[32:35], v[128:131], v[56:59]\n"
"v_mfma_f32_16x16x32_bf16 v[56:59], a[56:59], v[132:135], v[56:59]\n"
"v_mfma_f32_16x16x32_bf16 v[56:59], v[192:195], v[136:139], v[56:59]\n"
"buffer_store_dword v248, v249, s[64:67], 0 offen sc1\n"
"v_mfma_f32_16x16x32_bf16 v[56:59], v[216:219], v[140:143], v[56:59]\n"
"v_mfma_f32_16x16x32_bf16 v[60:63], v[220:223], v[140:143], v[60:63]\n"
"v_mfma_f32_16x16x32_bf16 v[60:63], v[196:199], v[136:139], v[60:63]\n"
"v_mfma_f32_16x16x32_bf16 v[60:63], a[60:63], v[132:135], v[60:63]\n"
"v_mfma_f32_16x16x32_bf16 v[60:63], a[36:39], v[128:131], v[60:63]\n"
"s_waitcnt vmcnt(13)\n"
"v_mfma_f32_16x16x32_bf16 v[64:67], a[16:19], v[144:147], v[64:67]\n"
"s_add_u32 s86, s87, 4096\n"
"v_mfma_f32_16x16x32_bf16 v[64:67], a[40:43], v[148:151], v[64:67]\n"
"buffer_load_dwordx4 v[128:131], v12, s[52:55], s86 offen offset:0 sc1\n"
"v_mfma_f32_16x16x32_bf16 v[64:67], a[64:67], v[152:155], v[64:67]\n"
"v_mfma_f32_16x16x32_bf16 v[64:67], v[200:203], v[156:159], v[64:67]\n"
"v_mfma_f32_16x16x32_bf16 v[68:71], v[204:207], v[156:159], v[68:71]\n"
"v_mfma_f32_16x16x32_bf16 v[68:71], a[68:71], v[152:155], v[68:71]\n"
"v_mfma_f32_16x16x32_bf16 v[68:71], a[44:47], v[148:151], v[68:71]\n"
"buffer_load_dwordx4 v[132:135], v12, s[52:55], s86 offen offset:1024 sc1\n"
"v_mfma_f32_16x16x32_bf16 v[68:71], a[20:23], v[144:147], v[68:71]\n"
"v_mfma_f32_16x16x32_bf16 v[72:75], a[24:27], v[144:147], v[72:75]\n"
"v_mfma_f32_16x16x32_bf16 v[72:75], a[48:51], v[148:151], v[72:75]\n"
"v_mfma_f32_16x16x32_bf16 v[72:75], a[72:75], v[152:155], v[72:75]\n"
"v_mfma_f32_16x16x32_bf16 v[72:75], v[208:211], v[156:159], v[72:75]\n"
"v_mfma_f32_16x16x32_bf16 v[76:79], v[212:215], v[156:159], v[76:79]\n"
"buffer_load_dwordx4 v[136:139], v12, s[52:55], s86 offen offset:2048 sc1\n"
"v_mfma_f32_16x16x32_bf16 v[76:79], a[76:79], v[152:155], v[76:79]\n"
"v_mfma_f32_16x16x32_bf16 v[76:79], a[52:55], v[148:151], v[76:79]\n"
"v_mfma_f32_16x16x32_bf16 v[76:79], a[28:31], v[144:147], v[76:79]\n"
"v_mfma_f32_16x16x32_bf16 v[80:83], a[32:35], v[144:147], v[80:83]\n"
"v_mfma_f32_16x16x32_bf16 v[80:83], a[56:59], v[148:151], v[80:83]\n"
"buffer_load_dwordx4 v[140:143], v12, s[52:55], s86 offen offset:3072 sc1\n"
"v_mfma_f32_16x16x32_bf16 v[80:83], v[192:195], v[152:155], v[80:83]\n"
"v_mfma_f32_16x16x32_bf16 v[80:83], v[216:219], v[156:159], v[80:83]\n"
"v_mfma_f32_16x16x32_bf16 v[84:87], v[220:223], v[156:159], v[84:87]\n"
"v_mfma_f32_16x16x32_bf16 v[84:87], v[196:199], v[152:155], v[84:87]\n"
"v_mfma_f32_16x16x32_bf16 v[84:87], a[60:63], v[148:151], v[84:87]\n"
"v_mfma_f32_16x16x32_bf16 v[84:87], a[36:39], v[144:147], v[84:87]\n"
"s_waitcnt vmcnt(13)\n"
"v_mfma_f32_16x16x32_bf16 v[88:91], a[16:19], v[160:163], v[88:91]\n"
"s_add_u32 s86, s87, 4096\n"
"v_mfma_f32_16x16x32_bf16 v[88:91], a[40:43], v[164:167], v[88:91]\n"
"buffer_load_dwordx4 v[144:147], v13, s[52:55], s86 offen offset:0 sc1\n"
"v_mfma_f32_16x16x32_bf16 v[88:91], a[64:67], v[168:171], v[88:91]\n"
"v_mfma_f32_16x16x32_bf16 v[88:91], v[200:203], v[172:175], v[88:91]\n"
"v_mfma_f32_16x16x32_bf16 v[92:95], v[204:207], v[172:175], v[92:95]\n"
"v_mfma_f32_16x16x32_bf16 v[92:95], a[68:71], v[168:171], v[92:95]\n"
"v_mfma_f32_16x16x32_bf16 v[92:95], a[44:47], v[164:167], v[92:95]\n"
"buffer_load_dwordx4 v[148:151], v13, s[52:55], s86 offen offset:1024 sc1\n"
"v_mfma_f32_16x16x32_bf16 v[92:95], a[20:23], v[160:163], v[92:95]\n"
"v_mfma_f32_16x16x32_bf16 v[96:99], a[24:27], v[160:163], v[96:99]\n"
"v_mfma_f32_16x16x32_bf16 v[96:99], a[48:51], v[164:167], v[96:99]\n"
"v_mfma_f32_16x16x32_bf16 v[96:99], a[72:75], v[168:171], v[96:99]\n"
"v_mfma_f32_16x16x32_bf16 v[96:99], v[208:211], v[172:175], v[96:99]\n"
"v_mfma_f32_16x16x32_bf16 v[100:103], v[212:215], v[172:175], v[100:103]\n"
"buffer_load_dwordx4 v[152:155], v13, s[52:55], s86 offen offset:2048 sc1\n"
"v_mfma_f32_16x16x32_bf16 v[100:103], a[76:79], v[168:171], v[100:103]\n"
"v_mfma_f32_16x16x32_bf16 v[100:103], a[52:55], v[164:167], v[100:103]\n"
"v_mfma_f32_16x16x32_bf16 v[100:103], a[28:31], v[160:163], v[100:103]\n"
"v_mfma_f32_16x16x32_bf16 v[104:107], a[32:35], v[160:163], v[104:107]\n"
"v_mfma_f32_16x16x32_bf16 v[104:107], a[56:59], v[164:167], v[104:107]\n"
"buffer_load_dwordx4 v[156:159], v13, s[52:55], s86 offen offset:3072 sc1\n"
"v_mfma_f32_16x16x32_bf16 v[104:107], v[192:195], v[168:171], v[104:107]\n"
"v_mfma_f32_16x16x32_bf16 v[104:107], v[216:219], v[172:175], v[104:107]\n"
"v_mfma_f32_16x16x32_bf16 v[108:111], v[220:223], v[172:175], v[108:111]\n"
"v_mfma_f32_16x16x32_bf16 v[108:111], v[196:199], v[168:171], v[108:111]\n"
"v_mfma_f32_16x16x32_bf16 v[108:111], a[60:63], v[164:167], v[108:111]\n"
"v_mfma_f32_16x16x32_bf16 v[108:111], a[36:39], v[160:163], v[108:111]\n"
"s_waitcnt vmcnt(13)\n"
"v_mfma_f32_16x16x32_bf16 v[16:19], a[132:135], v[176:179], v[16:19]\n"
"s_add_u32 s86, s87, 0\n"
"v_mfma_f32_16x16x32_bf16 v[16:19], a[156:159], v[180:183], v[16:19]\n"
"buffer_load_dwordx4 v[172:175], v1, s[52:55], s86 offen offset:3072 sc1\n"
"v_mfma_f32_16x16x32_bf16 v[16:19], a[180:183], v[184:187], v[16:19]\n"
"ds_read_b128 a[212:215], v4 offset:2048\n"
"v_mfma_f32_16x16x32_bf16 v[16:19], a[204:207], v[188:191], v[16:19]\n"
"v_mfma_f32_16x16x32_bf16 v[20:23], a[208:211], v[188:191], v[20:23]\n"
"ds_read_b128 a[216:219], v4 offset:3072\n"
"v_mfma_f32_16x16x32_bf16 v[20:23], a[184:187], v[184:187], v[20:23]\n"
"v_mfma_f32_16x16x32_bf16 v[20:23], a[160:163], v[180:183], v[20:23]\n"
"buffer_load_dwordx4 v[168:171], v1, s[52:55], s86 offen offset:2048 sc1\n"
"v_mfma_f32_16x16x32_bf16 v[20:23], a[136:139], v[176:179], v[20:23]\n"
"ds_read_b128 a[220:223], v4 offset:16384\n"
"v_mfma_f32_16x16x32_bf16 v[24:27], a[140:143], v[176:179], v[24:27]\n"
"v_mfma_f32_16x16x32_bf16 v[24:27], a[164:167], v[180:183], v[24:27]\n"
"ds_read_b128 a[224:227], v4 offset:17408\n"
"v_mfma_f32_16x16x32_bf16 v[24:27], a[188:191], v[184:187], v[24:27]\n"
"v_mfma_f32_16x16x32_bf16 v[24:27], a[0:3], v[188:191], v[24:27]\n"
"ds_read_b128 a[228:231], v4 offset:18432\n"
"v_mfma_f32_16x16x32_bf16 v[28:31], a[4:7], v[188:191], v[28:31]\n"
"buffer_load_dwordx4 v[164:167], v1, s[52:55], s86 offen offset:1024 sc1\n"
"v_mfma_f32_16x16x32_bf16 v[28:31], a[192:195], v[184:187], v[28:31]\n"
"v_mfma_f32_16x16x32_bf16 v[28:31], a[168:171], v[180:183], v[28:31]\n"
"v_mfma_f32_16x16x32_bf16 v[28:31], a[144:147], v[176:179], v[28:31]\n"
"v_mfma_f32_16x16x32_bf16 v[32:35], a[148:151], v[176:179], v[32:35]\n"
"v_mfma_f32_16x16x32_bf16 v[32:35], a[172:175], v[180:183], v[32:35]\n"
"buffer_load_dwordx4 v[160:163], v1, s[52:55], s86 offen offset:0 sc1\n"
"v_mfma_f32_16x16x32_bf16 v[32:35], a[196:199], v[184:187], v[32:35]\n"
"v_mfma_f32_16x16x32_bf16 v[32:35], a[8:11], v[188:191], v[32:35]\n"
"v_mfma_f32_16x16x32_bf16 v[36:39], a[12:15], v[188:191], v[36:39]\n"
"v_mfma_f32_16x16x32_bf16 v[36:39], a[200:203], v[184:187], v[36:39]\n"
"v_mfma_f32_16x16x32_bf16 v[36:39], a[176:179], v[180:183], v[36:39]\n"
"v_mfma_f32_16x16x32_bf16 v[36:39], a[152:155], v[176:179], v[36:39]\n"
"s_waitcnt vmcnt(13)\n"
"v_mfma_f32_16x16x32_bf16 v[40:43], a[132:135], v[112:115], v[40:43]\n"
"s_add_u32 s86, s87, 0\n"
"v_mfma_f32_16x16x32_bf16 v[40:43], a[156:159], v[116:119], v[40:43]\n"
"buffer_load_dwordx4 v[188:191], v2, s[52:55], s86 offen offset:3072 sc1\n"
"v_mfma_f32_16x16x32_bf16 v[40:43], a[180:183], v[120:123], v[40:43]\n"
"ds_read_b128 a[232:235], v4 offset:19456\n"
"v_mfma_f32_16x16x32_bf16 v[40:43], a[204:207], v[124:127], v[40:43]\n"
"v_mfma_f32_16x16x32_bf16 v[44:47], a[208:211], v[124:127], v[44:47]\n"
"ds_read_b128 a[236:239], v4 offset:32768\n"
"v_mfma_f32_16x16x32_bf16 v[44:47], a[184:187], v[120:123], v[44:47]\n"
"buffer_load_dwordx4 v[184:187], v2, s[52:55], s86 offen offset:2048 sc1\n"
"v_mfma_f32_16x16x32_bf16 v[44:47], a[160:163], v[116:119], v[44:47]\n"
"ds_read_b128 a[240:243], v4 offset:33792\n"
"v_mfma_f32_16x16x32_bf16 v[44:47], a[136:139], v[112:115], v[44:47]\n"
"v_mfma_f32_16x16x32_bf16 v[48:51], a[140:143], v[112:115], v[48:51]\n"
"ds_read_b128 a[244:247], v4 offset:34816\n"
"v_mfma_f32_16x16x32_bf16 v[48:51], a[164:167], v[116:119], v[48:51]\n"
"buffer_load_dwordx4 v[180:183], v2, s[52:55], s86 offen offset:1024 sc1\n"
"v_mfma_f32_16x16x32_bf16 v[48:51], a[188:191], v[120:123], v[48:51]\n"
"ds_read_b128 a[248:251], v4 offset:35840\n"
"v_mfma_f32_16x16x32_bf16 v[48:51], a[0:3], v[124:127], v[48:51]\n"
"v_mfma_f32_16x16x32_bf16 v[52:55], a[4:7], v[124:127], v[52:55]\n"
"v_mfma_f32_16x16x32_bf16 v[52:55], a[192:195], v[120:123], v[52:55]\n"
"v_mfma_f32_16x16x32_bf16 v[52:55], a[168:171], v[116:119], v[52:55]\n"
"buffer_load_dwordx4 v[176:179], v2, s[52:55], s86 offen offset:0 sc1\n"
"v_mfma_f32_16x16x32_bf16 v[52:55], a[144:147], v[112:115], v[52:55]\n"
"v_mfma_f32_16x16x32_bf16 v[56:59], a[148:151], v[112:115], v[56:59]\n"
"v_mfma_f32_16x16x32_bf16 v[56:59], a[172:175], v[116:119], v[56:59]\n"
"v_mfma_f32_16x16x32_bf16 v[56:59], a[196:199], v[120:123], v[56:59]\n"
"buffer_load_dword v251, v250, s[64:67], 0 offen sc1\n"
"v_mfma_f32_16x16x32_bf16 v[56:59], a[8:11], v[124:127], v[56:59]\n"
"v_mfma_f32_16x16x32_bf16 v[60:63], a[12:15], v[124:127], v[60:63]\n"
"v_mfma_f32_16x16x32_bf16 v[60:63], a[200:203], v[120:123], v[60:63]\n"
"v_mfma_f32_16x16x32_bf16 v[60:63], a[176:179], v[116:119], v[60:63]\n"
"v_mfma_f32_16x16x32_bf16 v[60:63], a[152:155], v[112:115], v[60:63]\n"
"s_waitcnt vmcnt(13)\n"
"v_mfma_f32_16x16x32_bf16 v[64:67], a[132:135], v[128:131], v[64:67]\n"
"s_add_u32 s86, s87, 0\n"
"v_mfma_f32_16x16x32_bf16 v[64:67], a[156:159], v[132:135], v[64:67]\n"
"buffer_load_dwordx4 v[124:127], v12, s[52:55], s86 offen offset:3072 sc1\n"
"v_mfma_f32_16x16x32_bf16 v[64:67], a[180:183], v[136:139], v[64:67]\n"
"ds_read_b128 a[252:255], v4 offset:49152\n"
"v_mfma_f32_16x16x32_bf16 v[64:67], a[204:207], v[140:143], v[64:67]\n"
"v_mfma_f32_16x16x32_bf16 v[68:71], a[208:211], v[140:143], v[68:71]\n"
"ds_read_b128 a[80:83], v4 offset:50176\n"
"v_mfma_f32_16x16x32_bf16 v[68:71], a[184:187], v[136:139], v[68:71]\n"
"v_mfma_f32_16x16x32_bf16 v[68:71], a[160:163], v[132:135], v[68:71]\n"
"buffer_load_dwordx4 v[120:123], v12, s[52:55], s86 offen offset:2048 sc1\n"
"v_mfma_f32_16x16x32_bf16 v[68:71], a[136:139], v[128:131], v[68:71]\n"
"ds_read_b128 a[84:87], v4 offset:51200\n"
"v_mfma_f32_16x16x32_bf16 v[72:75], a[140:143], v[128:131], v[72:75]\n"
"v_mfma_f32_16x16x32_bf16 v[72:75], a[164:167], v[132:135], v[72:75]\n"
"ds_read_b128 a[88:91], v4 offset:52224\n"
"v_mfma_f32_16x16x32_bf16 v[72:75], a[188:191], v[136:139], v[72:75]\n"
"v_mfma_f32_16x16x32_bf16 v[72:75], a[0:3], v[140:143], v[72:75]\n"
"v_mfma_f32_16x16x32_bf16 v[76:79], a[4:7], v[140:143], v[76:79]\n"
"buffer_load_dwordx4 v[116:119], v12, s[52:55], s86 offen offset:1024 sc1\n"
"v_mfma_f32_16x16x32_bf16 v[76:79], a[192:195], v[136:139], v[76:79]\n"
"v_mfma_f32_16x16x32_bf16 v[76:79], a[168:171], v[132:135], v[76:79]\n"
"v_mfma_f32_16x16x32_bf16 v[76:79], a[144:147], v[128:131], v[76:79]\n"
"v_mfma_f32_16x16x32_bf16 v[80:83], a[148:151], v[128:131], v[80:83]\n"
"v_mfma_f32_16x16x32_bf16 v[80:83], a[172:175], v[132:135], v[80:83]\n"
"buffer_load_dwordx4 v[112:115], v12, s[52:55], s86 offen offset:0 sc1\n"
"v_mfma_f32_16x16x32_bf16 v[80:83], a[196:199], v[136:139], v[80:83]\n"
"v_mfma_f32_16x16x32_bf16 v[80:83], a[8:11], v[140:143], v[80:83]\n"
"v_mfma_f32_16x16x32_bf16 v[84:87], a[12:15], v[140:143], v[84:87]\n"
"v_mfma_f32_16x16x32_bf16 v[84:87], a[200:203], v[136:139], v[84:87]\n"
"v_mfma_f32_16x16x32_bf16 v[84:87], a[176:179], v[132:135], v[84:87]\n"
"v_mfma_f32_16x16x32_bf16 v[84:87], a[152:155], v[128:131], v[84:87]\n"
"s_waitcnt vmcnt(13)\n"
"v_mfma_f32_16x16x32_bf16 v[88:91], a[132:135], v[144:147], v[88:91]\n"
"s_add_u32 s86, s87, 0\n"
"v_mfma_f32_16x16x32_bf16 v[88:91], a[156:159], v[148:151], v[88:91]\n"
"buffer_load_dwordx4 v[140:143], v13, s[52:55], s86 offen offset:3072 sc1\n"
"v_mfma_f32_16x16x32_bf16 v[88:91], a[180:183], v[152:155], v[88:91]\n"
"ds_read_b128 a[92:95], v5 offset:0\n"
"v_mfma_f32_16x16x32_bf16 v[88:91], a[204:207], v[156:159], v[88:91]\n"
"v_mfma_f32_16x16x32_bf16 v[92:95], a[208:211], v[156:159], v[92:95]\n"
"ds_read_b128 a[96:99], v5 offset:1024\n"
"v_mfma_f32_16x16x32_bf16 v[92:95], a[184:187], v[152:155], v[92:95]\n"
"v_mfma_f32_16x16x32_bf16 v[92:95], a[160:163], v[148:151], v[92:95]\n"
"buffer_load_dwordx4 v[136:139], v13, s[52:55], s86 offen offset:2048 sc1\n"
"v_mfma_f32_16x16x32_bf16 v[92:95], a[136:139], v[144:147], v[92:95]\n"
"ds_read_b128 a[100:103], v5 offset:2048\n"
"v_mfma_f32_16x16x32_bf16 v[96:99], a[140:143], v[144:147], v[96:99]\n"
"v_mfma_f32_16x16x32_bf16 v[96:99], a[164:167], v[148:151], v[96:99]\n"
"ds_read_b128 a[104:107], v5 offset:3072\n"
"v_mfma_f32_16x16x32_bf16 v[96:99], a[188:191], v[152:155], v[96:99]\n"
"v_mfma_f32_16x16x32_bf16 v[96:99], a[0:3], v[156:159], v[96:99]\n"
"v_mfma_f32_16x16x32_bf16 v[100:103], a[4:7], v[156:159], v[100:103]\n"
"buffer_load_dwordx4 v[132:135], v13, s[52:55], s86 offen offset:1024 sc1\n"
"v_mfma_f32_16x16x32_bf16 v[100:103], a[192:195], v[152:155], v[100:103]\n"
"v_mfma_f32_16x16x32_bf16 v[100:103], a[168:171], v[148:151], v[100:103]\n"
"v_mfma_f32_16x16x32_bf16 v[100:103], a[144:147], v[144:147], v[100:103]\n"
"v_mfma_f32_16x16x32_bf16 v[104:107], a[148:151], v[144:147], v[104:107]\n"
"v_mfma_f32_16x16x32_bf16 v[104:107], a[172:175], v[148:151], v[104:107]\n"
"buffer_load_dwordx4 v[128:131], v13, s[52:55], s86 offen offset:0 sc1\n"
"v_mfma_f32_16x16x32_bf16 v[104:107], a[196:199], v[152:155], v[104:107]\n"
"v_mfma_f32_16x16x32_bf16 v[104:107], a[8:11], v[156:159], v[104:107]\n"
"v_mfma_f32_16x16x32_bf16 v[108:111], a[12:15], v[156:159], v[108:111]\n"
"v_mfma_f32_16x16x32_bf16 v[108:111], a[200:203], v[152:155], v[108:111]\n"
"v_mfma_f32_16x16x32_bf16 v[108:111], a[176:179], v[148:151], v[108:111]\n"
"v_mfma_f32_16x16x32_bf16 v[108:111], a[152:155], v[144:147], v[108:111]\n"
"s_waitcnt vmcnt(8)\n"
"v_cmp_ne_u32 vcc, 1, v251\n"
"s_cbranch_vccnz Lpslow_%=\n"
"Lpok_%=:\n"
"v_mfma_f32_16x16x32_bf16 v[16:19], a[108:111], v[172:175], v[16:19]\n"
"s_waitcnt lgkmcnt(0)\n"
"v_mfma_f32_16x16x32_bf16 v[16:19], a[84:87], v[168:171], v[16:19]\n"
"buffer_load_dwordx4 v[224:227], v9, s[40:43], 0 offen offset:0 sc1\n"
"v_mfma_f32_16x16x32_bf16 v[16:19], a[236:239], v[164:167], v[16:19]\n"
"v_mfma_f32_16x16x32_bf16 v[16:19], a[212:215], v[160:163], v[16:19]\n"
"v_mfma_f32_16x16x32_bf16 v[20:23], a[216:219], v[160:163], v[20:23]\n"
"buffer_load_dwordx4 v[228:231], v9, s[40:43], 0 offen offset:1024 sc1\n"
"v_mfma_f32_16x16x32_bf16 v[20:23], a[240:243], v[164:167], v[20:23]\n"
"v_mfma_f32_16x16x32_bf16 v[20:23], a[88:91], v[168:171], v[20:23]\n"
"v_mfma_f32_16x16x32_bf16 v[20:23], a[112:115], v[172:175], v[20:23]\n"
"v_mfma_f32_16x16x32_bf16 v[24:27], a[116:119], v[172:175], v[24:27]\n"
"buffer_load_dwordx4 v[232:235], v9, s[40:43], 0 offen offset:2048 sc1\n"
"v_mfma_f32_16x16x32_bf16 v[24:27], a[92:95], v[168:171], v[24:27]\n"
"v_mfma_f32_16x16x32_bf16 v[24:27], a[244:247], v[164:167], v[24:27]\n"
"v_mfma_f32_16x16x32_bf16 v[24:27], a[220:223], v[160:163], v[24:27]\n"
"v_mfma_f32_16x16x32_bf16 v[28:31], a[224:227], v[160:163], v[28:31]\n"
"buffer_load_dwordx4 v[236:239], v9, s[40:43], 0 offen offset:3072 sc1\n"
"v_mfma_f32_16x16x32_bf16 v[28:31], a[248:251], v[164:167], v[28:31]\n"
"ds_write_b128 v4, v[16:19] offset:2048\n"
"v_mfma_f32_16x16x32_bf16 v[28:31], a[96:99], v[168:171], v[28:31]\n"
"v_mfma_f32_16x16x32_bf16 v[28:31], a[120:123], v[172:175], v[28:31]\n"
"buffer_load_dwordx4 v[240:243], v14, s[40:43], 0 offen offset:0 sc1\n"
"v_mfma_f32_16x16x32_bf16 v[32:35], a[124:127], v[172:175], v[32:35]\n"
"ds_write_b128 v4, v[20:23] offset:3072\n"
"v_mfma_f32_16x16x32_bf16 v[32:35], a[100:103], v[168:171], v[32:35]\n"
"v_mfma_f32_16x16x32_bf16 v[32:35], a[252:255], v[164:167], v[32:35]\n"
"v_mfma_f32_16x16x32_bf16 v[32:35], a[228:231], v[160:163], v[32:35]\n"
"buffer_load_dwordx4 v[244:247], v14, s[40:43], 0 offen offset:1024 sc1\n"
"v_mfma_f32_16x16x32_bf16 v[36:39], a[232:235], v[160:163], v[36:39]\n"
"ds_write_b128 v4, v[24:27] offset:16384\n"
"v_mfma_f32_16x16x32_bf16 v[36:39], a[80:83], v[164:167], v[36:39]\n"
"v_mfma_f32_16x16x32_bf16 v[36:39], a[104:107], v[168:171], v[36:39]\n"
"v_mfma_f32_16x16x32_bf16 v[36:39], a[128:131], v[172:175], v[36:39]\n"
"s_waitcnt vmcnt(14)\n"
"v_mfma_f32_16x16x32_bf16 v[40:43], a[108:111], v[188:191], v[40:43]\n"
"ds_write_b128 v4, v[28:31] offset:17408\n"
"v_mfma_f32_16x16x32_bf16 v[40:43], a[84:87], v[184:187], v[40:43]\n"
"v_mfma_f32_16x16x32_bf16 v[40:43], a[236:239], v[180:183], v[40:43]\n"
"v_mfma_f32_16x16x32_bf16 v[40:43], a[212:215], v[176:179], v[40:43]\n"
"v_mfma_f32_16x16x32_bf16 v[44:47], a[216:219], v[176:179], v[44:47]\n"
"ds_write_b128 v4, v[32:35] offset:18432\n"
"v_mfma_f32_16x16x32_bf16 v[44:47], a[240:243], v[180:183], v[44:47]\n"
"v_mfma_f32_16x16x32_bf16 v[44:47], a[88:91], v[184:187], v[44:47]\n"
"v_mfma_f32_16x16x32_bf16 v[44:47], a[112:115], v[188:191], v[44:47]\n"
"v_mfma_f32_16x16x32_bf16 v[48:51], a[116:119], v[188:191], v[48:51]\n"
"ds_write_b128 v4, v[36:39] offset:19456\n"
"v_mfma_f32_16x16x32_bf16 v[48:51], a[92:95], v[184:187], v[48:51]\n"
"v_mfma_f32_16x16x32_bf16 v[48:51], a[244:247], v[180:183], v[48:51]\n"
"v_mfma_f32_16x16x32_bf16 v[48:51], a[220:223], v[176:179], v[48:51]\n"
"v_mfma_f32_16x16x32_bf16 v[52:55], a[224:227], v[176:179], v[52:55]\n"
"ds_write_b128 v4, v[40:43] offset:32768\n"
"v_mfma_f32_16x16x32_bf16 v[52:55], a[248:251], v[180:183], v[52:55]\n"
"v_mfma_f32_16x16x32_bf16 v[52:55], a[96:99], v[184:187], v[52:55]\n"
"v_mfma_f32_16x16x32_bf16 v[52:55], a[120:123], v[188:191], v[52:55]\n"
"v_mfma_f32_16x16x32_bf16 v[56:59], a[124:127], v[188:191], v[56:59]\n"
"ds_write_b128 v4, v[44:47] offset:33792\n"
"v_mfma_f32_16x16x32_bf16 v[56:59], a[100:103], v[184:187], v[56:59]\n"
"v_mfma_f32_16x16x32_bf16 v[56:59], a[252:255], v[180:183], v[56:59]\n"
"v_mfma_f32_16x16x32_bf16 v[56:59], a[228:231], v[176:179], v[56:59]\n"
"v_mfma_f32_16x16x32_bf16 v[60:63], a[232:235], v[176:179], v[60:63]\n"
"ds_write_b128 v4, v[48:51] offset:34816\n"
"v_mfma_f32_16x16x32_bf16 v[60:63], a[80:83], v[180:183], v[60:63]\n"
"v_mfma_f32_16x16x32_bf16 v[60:63], a[104:107], v[184:187], v[60:63]\n"
"v_mfma_f32_16x16x32_bf16 v[60:63], a[128:131], v[188:191], v[60:63]\n"
"s_waitcnt vmcnt(10)\n"
"v_mfma_f32_16x16x32_bf16 v[64:67], a[108:111], v[124:127], v[64:67]\n"
"ds_write_b128 v4, v[52:55] offset:35840\n"
"v_mfma_f32_16x16x32_bf16 v[64:67], a[84:87], v[120:123], v[64:67]\n"
"v_mfma_f32_16x16x32_bf16 v[64:67], a[236:239], v[116:119], v[64:67]\n"
"v_mfma_f32_16x16x32_bf16 v[64:67], a[212:215], v[112:115], v[64:67]\n"
"v_mfma_f32_16x16x32_bf16 v[68:71], a[216:219], v[112:115], v[68:71]\n"
"ds_write_b128 v4, v[56:59] offset:49152\n"
"v_mfma_f32_16x16x32_bf16 v[68:71], a[240:243], v[116:119], v[68:71]\n"
"v_mfma_f32_16x16x32_bf16 v[68:71], a[88:91], v[120:123], v[68:71]\n"
"v_mfma_f32_16x16x32_bf16 v[68:71], a[112:115], v[124:127], v[68:71]\n"
"v_mfma_f32_16x16x32_bf16 v[72:75], a[116:119], v[124:127], v[72:75]\n"
"ds_write_b128 v4, v[60:63] offset:50176\n"
"v_mfma_f32_16x16x32_bf16 v[72:75], a[92:95], v[120:123], v[72:75]\n"
"v_mfma_f32_16x16x32_bf16 v[72:75], a[244:247], v[116:119], v[72:75]\n"
"v_mfma_f32_16x16x32_bf16 v[72:75], a[220:223], v[112:115], v[72:75]\n"
"v_mfma_f32_16x16x32_bf16 v[76:79], a[224:227], v[112:115], v[76:79]\n"
"ds_write_b128 v4, v[64:67] offset:51200\n"
"v_mfma_f32_16x16x32_bf16 v[76:79], a[248:251], v[116:119], v[76:79]\n"
"v_mfma_f32_16x16x32_bf16 v[76:79], a[96:99], v[120:123], v[76:79]\n"
"v_mfma_f32_16x16x32_bf16 v[76:79], a[120:123], v[124:127], v[76:79]\n"
"v_mfma_f32_16x16x32_bf16 v[80:83], a[124:127], v[124:127], v[80:83]\n"
"ds_write_b128 v4, v[68:71] offset:52224\n"
"v_mfma_f32_16x16x32_bf16 v[80:83], a[100:103], v[120:123], v[80:83]\n"
"v_mfma_f32_16x16x32_bf16 v[80:83], a[252:255], v[116:119], v[80:83]\n"
"v_mfma_f32_16x16x32_bf16 v[80:83], a[228:231], v[112:115], v[80:83]\n"
"v_mfma_f32_16x16x32_bf16 v[84:87], a[232:235], v[112:115], v[84:87]\n"
"ds_write_b128 v5, v[72:75] offset:0\n"
"v_mfma_f32_16x16x32_bf16 v[84:87], a[80:83], v[116:119], v[84:87]\n"
"v_mfma_f32_16x16x32_bf16 v[84:87], a[104:107], v[120:123], v[84:87]\n"
"v_mfma_f32_16x16x32_bf16 v[84:87], a[128:131], v[124:127], v[84:87]\n"
"s_waitcnt vmcnt(6)\n"
"v_mfma_f32_16x16x32_bf16 v[88:91], a[108:111], v[140:143], v[88:91]\n"
"ds_write_b128 v5, v[76:79] offset:1024\n"
"v_mfma_f32_16x16x32_bf16 v[88:91], a[84:87], v[136:139], v[88:91]\n"
"v_mfma_f32_16x16x32_bf16 v[88:91], a[236:239], v[132:135], v[88:91]\n"
"v_mfma_f32_16x16x32_bf16 v[88:91], a[212:215], v[128:131], v[88:91]\n"
"v_mfma_f32_16x16x32_bf16 v[92:95], a[216:219], v[128:131], v[92:95]\n"
"ds_write_b128 v5, v[80:83] offset:2048\n"
"v_mfma_f32_16x16x32_bf16 v[92:95], a[240:243], v[132:135], v[92:95]\n"
"v_mfma_f32_16x16x32_bf16 v[92:95], a[88:91], v[136:139], v[92:95]\n"
"v_mfma_f32_16x16x32_bf16 v[92:95], a[112:115], v[140:143], v[92:95]\n"
"v_mfma_f32_16x16x32_bf16 v[96:99], a[116:119], v[140:143], v[96:99]\n"
"ds_write_b128 v5, v[84:87] offset:3072\n"
"v_mfma_f32_16x16x32_bf16 v[96:99], a[92:95], v[136:139], v[96:99]\n"
"v_mfma_f32_16x16x32_bf16 v[96:99], a[244:247], v[132:135], v[96:99]\n"
"v_mfma_f32_16x16x32_bf16 v[96:99], a[220:223], v[128:131], v[96:99]\n"
"v_mfma_f32_16x16x32_bf16 v[100:103], a[224:227], v[128:131], v[100:103]\n"
"v_mfma_f32_16x16x32_bf16 v[100:103], a[248:251], v[132:135], v[100:103]\n"
"v_mfma_f32_16x16x32_bf16 v[100:103], a[96:99], v[136:139], v[100:103]\n"
"v_mfma_f32_16x16x32_bf16 v[100:103], a[120:123], v[140:143], v[100:103]\n"
"v_mfma_f32_16x16x32_bf16 v[104:107], a[124:127], v[140:143], v[104:107]\n"
"v_mfma_f32_16x16x32_bf16 v[104:107], a[100:103], v[136:139], v[104:107]\n"
"v_mfma_f32_16x16x32_bf16 v[104:107], a[252:255], v[132:135], v[104:107]\n"
"v_mfma_f32_16x16x32_bf16 v[104:107], a[228:231], v[128:131], v[104:107]\n"
"v_mfma_f32_16x16x32_bf16 v[108:111], a[232:235], v[128:131], v[108:111]\n"
"v_mfma_f32_16x16x32_bf16 v[108:111], a[80:83], v[132:135], v[108:111]\n"
"v_mfma_f32_16x16x32_bf16 v[108:111], a[104:107], v[136:139], v[108:111]\n"
"v_mfma_f32_16x16x32_bf16 v[108:111], a[128:131], v[140:143], v[108:111]\n"
"s_waitcnt lgkmcnt(0)\n"
"s_barrier\n"
"ds_read_b128 v[112:115], v6 offset:2048\n"
"ds_read_b128 v[116:119], v7 offset:32768\n"
"ds_read_b128 v[120:123], v8 offset:51200\n"
"ds_read_b128 v[124:127], v6 offset:3072\n"
"ds_read_b128 v[128:131], v7 offset:33792\n"
"ds_read_b128 v[132:135], v8 offset:52224\n"
"ds_read_b128 v[136:139], v6 offset:16384\n"
"ds_read_b128 v[140:143], v7 offset:34816\n"
"ds_read_b128 v[144:147], v10 offset:0\n"
"ds_read_b128 v[148:151], v6 offset:17408\n"
"ds_read_b128 v[152:155], v7 offset:35840\n"
"ds_read_b128 v[156:159], v10 offset:1024\n"
"ds_read_b128 v[160:163], v6 offset:18432\n"
"ds_read_b128 v[164:167], v7 offset:49152\n"
"ds_read_b128 v[168:171], v10 offset:2048\n"
"ds_read_b128 v[172:175], v6 offset:19456\n"
"ds_read_b128 v[176:179], v7 offset:50176\n"
"ds_read_b128 v[180:183], v10 offset:3072\n"
"s_waitcnt vmcnt(0)\n"
".p2align 3\n"
"v_pk_add_f32 v[88:89], v[88:89], v[224:225]\n"
"v_pk_add_f32 v[90:91], v[90:91], v[226:227]\n"
"v_pk_add_f32 v[92:93], v[92:93], v[228:229]\n"
"v_pk_add_f32 v[94:95], v[94:95], v[230:231]\n"
"v_pk_add_f32 v[96:97], v[96:97], v[232:233]\n"
"v_pk_add_f32 v[98:99], v[98:99], v[234:235]\n"
"v_pk_add_f32 v[100:101], v[100:101], v[236:237]\n"
"v_pk_add_f32 v[102:103], v[102:103], v[238:239]\n"
"v_pk_add_f32 v[104:105], v[104:105], v[240:241]\n"
"v_pk_add_f32 v[106:107], v[106:107], v[242:243]\n"
"v_pk_add_f32 v[108:109], v[108:109], v[244:245]\n"
"v_pk_add_f32 v[110:111], v[110:111], v[246:247]\n"
"s_waitcnt lgkmcnt(15)\n"
"v_pk_add_f32 v[88:89], v[88:89], v[112:113]\n"
"v_pk_add_f32 v[116:117], v[116:117], v[120:121]\n"
"v_pk_add_f32 v[90:91], v[90:91], v[114:115]\n"
"v_pk_add_f32 v[118:119], v[118:119], v[122:123]\n"
"v_pk_add_f32 v[88:89], v[88:89], v[116:117]\n"
"v_pk_add_f32 v[90:91], v[90:91], v[118:119]\n"
"v_cvt_pk_bf16_f32 v16, v88, v89\n"
"v_cvt_pk_bf16_f32 v17, v90, v91\n"
"s_waitcnt lgkmcnt(12)\n"
"v_pk_add_f32 v[92:93], v[92:93], v[124:125]\n"
"v_pk_add_f32 v[128:129], v[128:129], v[132:133]\n"
"v_pk_add_f32 v[94:95], v[94:95], v[126:127]\n"
"v_pk_add_f32 v[130:131], v[130:131], v[134:135]\n"
"v_pk_add_f32 v[92:93], v[92:93], v[128:129]\n"
"v_pk_add_f32 v[94:95], v[94:95], v[130:131]\n"
"v_cvt_pk_bf16_f32 v18, v92, v93\n"
"v_cvt_pk_bf16_f32 v19, v94, v95\n"
"s_nop 1\n"
"v_permlane16_swap_b32 v16, v18\n"
"v_permlane16_swap_b32 v17, v19\n"
"buffer_store_dwordx4 v[16:19], v11, s[60:63], 0 offen offset:0\n"
"s_waitcnt lgkmcnt(9)\n"
"v_pk_add_f32 v[96:97], v[96:97], v[136:137]\n"
"v_pk_add_f32 v[140:141], v[140:141], v[144:145]\n"
"v_pk_add_f32 v[98:99], v[98:99], v[138:139]\n"
"v_pk_add_f32 v[142:143], v[142:143], v[146:147]\n"
"v_pk_add_f32 v[96:97], v[96:97], v[140:141]\n"
"v_pk_add_f32 v[98:99], v[98:99], v[142:143]\n"
"v_cvt_pk_bf16_f32 v20, v96, v97\n"
"v_cvt_pk_bf16_f32 v21, v98, v99\n"
"s_waitcnt lgkmcnt(6)\n"
"v_pk_add_f32 v[100:101], v[100:101], v[148:149]\n"
"v_pk_add_f32 v[152:153], v[152:153], v[156:157]\n"
"v_pk_add_f32 v[102:103], v[102:103], v[150:151]\n"
"v_pk_add_f32 v[154:155], v[154:155], v[158:159]\n"
"v_pk_add_f32 v[100:101], v[100:101], v[152:153]\n"
"v_pk_add_f32 v[102:103], v[102:103], v[154:155]\n"
"v_cvt_pk_bf16_f32 v22, v100, v101\n"
"v_cvt_pk_bf16_f32 v23, v102, v103\n"
"s_nop 1\n"
"v_permlane16_swap_b32 v20, v22\n"
"v_permlane16_swap_b32 v21, v23\n"
"buffer_store_dwordx4 v[20:23], v11, s[60:63], 0 offen offset:64\n"
"s_waitcnt lgkmcnt(3)\n"
"v_pk_add_f32 v[104:105], v[104:105], v[160:161]\n"
"v_pk_add_f32 v[164:165], v[164:165], v[168:169]\n"
"v_pk_add_f32 v[106:107], v[106:107], v[162:163]\n"
"v_pk_add_f32 v[166:167], v[166:167], v[170:171]\n"
"v_pk_add_f32 v[104:105], v[104:105], v[164:165]\n"
"v_pk_add_f32 v[106:107], v[106:107], v[166:167]\n"
"v_cvt_pk_bf16_f32 v24, v104, v105\n"
"v_cvt_pk_bf16_f32 v25, v106, v107\n"
"s_waitcnt lgkmcnt(0)\n"
"v_pk_add_f32 v[108:109], v[108:109], v[172:173]\n"
"v_pk_add_f32 v[176:177], v[176:177], v[180:181]\n"
"v_pk_add_f32 v[110:111], v[110:111], v[174:175]\n"
"v_pk_add_f32 v[178:179], v[178:179], v[182:183]\n"
"v_pk_add_f32 v[108:109], v[108:109], v[176:177]\n"
"v_pk_add_f32 v[110:111], v[110:111], v[178:179]\n"
"v_cvt_pk_bf16_f32 v26, v108, v109\n"
"v_cvt_pk_bf16_f32 v27, v110, v111\n"
"s_nop 1\n"
"v_permlane16_swap_b32 v24, v26\n"
"v_permlane16_swap_b32 v25, v27\n"
"buffer_store_dwordx4 v[24:27], v11, s[60:63], 0 offen offset:128\n"
"s_endpgm\n"
"Lidle_%=:\n"
"v_mov_b32 v7, s34\n"
"v_mov_b32 v10, 1\n"
"s_waitcnt vmcnt(0)\n"
"buffer_store_dword v10, v7, s[64:67], 0 offen sc1\n"
"s_cmp_ge_u32 s25, 1020\n"
"s_cbranch_scc0 Ldone_%=\n"
"s_lshl_b32 s96, %[wid], 10\n"
"v_add_u32 v56, s96, v9\n"
"v_add_u32 v57, 16384, v56\n"
"v_add_u32 v58, 20480, v56\n"
"v_lshlrev_b32 v59, 1, v56\n"
"v_add_u32 v59, 24576, v59\n"
"s_mov_b32 s88, 0x40000\n"
"Lserve_%=:\n"
"buffer_load_dwordx4 v[44:47], v57, s[64:67], 0 offen sc1\n"
"buffer_load_dwordx4 v[40:43], v58, s[64:67], 0 offen sc1\n"
"buffer_load_dwordx4 v[48:51], v59, s[64:67], 0 offen sc1\n"
"buffer_load_dwordx4 v[52:55], v59, s[64:67], 0 offen offset:16 sc1\n"
"s_waitcnt vmcnt(0)\n"
"v_xor_b32 v40, v40, v49\n"
"v_mov_b32 v48, v44\n"
"v_xor_b32 v41, v41, v51\n"
"v_mov_b32 v50, v45\n"
"v_xor_b32 v42, v42, v53\n"
"v_mov_b32 v52, v46\n"
"v_xor_b32 v43, v43, v55\n"
"v_mov_b32 v54, v47\n"
"v_or_b32 v40, v40, v41\n"
"v_or_b32 v42, v42, v43\n"
"v_or_b32 v40, v40, v42\n"
"v_cmp_ne_u32 vcc, 0, v40\n"
"s_cbranch_vccz Ldone_%=\n"
"buffer_store_dwordx4 v[48:51], v59, s[64:67], 0 offen sc1\n"
"buffer_store_dwordx4 v[52:55], v59, s[64:67], 0 offen offset:16 sc1\n"
"s_waitcnt vmcnt(0)\n"
"s_sleep 8\n"
"s_sub_u32 s88, s88, 1\n"
"s_cmp_lg_u32 s88, 0\n"
"s_cbranch_scc1 Lserve_%=\n"
"Ldone_%=:\n"
"s_endpgm\n"
"Lwslow_%=:\n"
"s_cmp_ge_u32 s24, 30\n"
"s_cbranch_scc1 Lwok_%=\n"
"s_mov_b32 s88, 0x40000\n"
"Lwp_%=:\n"
"buffer_load_dword v12, v14, s[64:67], 0 offen sc1\n"
"s_waitcnt vmcnt(0)\n"
"v_cmp_ne_u32 vcc, 1, v12\n"
"s_cbranch_vccz Lwpgo_%=\n"
"s_sleep 2\n"
"s_sub_u32 s88, s88, 1\n"
"s_cmp_lg_u32 s88, 0\n"
"s_cbranch_scc1 Lwp_%=\n"
"Lwpgo_%=:\n"
"s_branch Lwok_%=\n"
"Lpslow_%=:\n"
"s_mov_b32 s88, 0x40000\n"
"Lpp_%=:\n"
"buffer_load_dword v251, v250, s[64:67], 0 offen sc1\n"
"s_waitcnt vmcnt(0)\n"
"v_cmp_ne_u32 vcc, 1, v251\n"
"s_cbranch_vccz Lppgo_%=\n"
"s_sleep 2\n"
"s_sub_u32 s88, s88, 1\n"
"s_cmp_lg_u32 s88, 0\n"
"s_cbranch_scc1 Lpp_%=\n"
"Lppgo_%=:\n"
"s_branch Lpok_%=\n"
"Lhslow_%=:\n"
"s_mov_b32 s8, 0x40000\n"
"Lhs_%=:\n"
"buffer_load_dwordx2 v[12:13], v7, s[64:67], 0 offen sc1\n"
"s_waitcnt vmcnt(0)\n"
"v_cmp_ne_u32 vcc, v12, v14\n"
"s_cbranch_vccz Lhok_%=\n"
"s_sleep 8\n"
"s_sub_u32 s8, s8, 1\n"
"s_cmp_lg_u32 s8, 0\n"
"s_cbranch_scc1 Lhs_%=\n"
"s_branch Lhok_%=\n"
"Lfallback_%=:\n"
"s_waitcnt vmcnt(0) lgkmcnt(0)\n"
"v_and_b32 v1, 15, v0\n"
"v_lshlrev_b32 v1, 8, v1\n"
"v_lshrrev_b32 v2, 4, v0\n"
"v_lshl_add_u32 v1, v2, 4, v1\n"
"v_add_u32 v2, 4096, v1\n"
"s_sub_u32 s96, s94, 65536\n"
"v_add_u32 v3, s96, v5\n"
"v_add_u32 v4, s96, v6\n"
"v_mov_b32 v40, 0xff800000\n"
"v_mov_b32 v44, 0xff800000\n"
"v_mov_b32 v48, 0xff800000\n"
"v_mov_b32 v52, 0xff800000\n"
"v_mov_b32 v56, 0xff800000\n"
"s_mov_b32 s85, 0\n"
"Lfb1_%=:\n"
"buffer_load_dwordx4 a[80:83], v1, s[44:47], s85 offen offset:0\n"
"buffer_load_dwordx4 a[84:87], v1, s[44:47], s85 offen offset:64\n"
"buffer_load_dwordx4 a[88:91], v1, s[44:47], s85 offen offset:128\n"
"buffer_load_dwordx4 a[92:95], v1, s[44:47], s85 offen offset:192\n"
"buffer_load_dwordx4 a[96:99], v2, s[44:47], s85 offen offset:0\n"
"buffer_load_dwordx4 a[100:103], v2, s[44:47], s85 offen offset:64\n"
"buffer_load_dwordx4 a[104:107], v2, s[44:47], s85 offen offset:128\n"
"buffer_load_dwordx4 a[108:111], v2, s[44:47], s85 offen offset:192\n"
"s_waitcnt vmcnt(0)\n"
"v_mfma_f32_16x16x32_bf16 v[16:19], a[80:83], a[0:3], 0\n"
"v_mfma_f32_16x16x32_bf16 v[16:19], a[84:87], a[4:7], v[16:19]\n"
"v_mfma_f32_16x16x32_bf16 v[16:19], a[88:91], a[8:11], v[16:19]\n"
"v_mfma_f32_16x16x32_bf16 v[16:19], a[92:95], a[12:15], v[16:19]\n"
"v_mfma_f32_16x16x32_bf16 v[20:23], a[108:111], a[12:15], 0\n"
"v_mfma_f32_16x16x32_bf16 v[20:23], a[104:107], a[8:11], v[20:23]\n"
"v_mfma_f32_16x16x32_bf16 v[20:23], a[100:103], a[4:7], v[20:23]\n"
"v_mfma_f32_16x16x32_bf16 v[20:23], a[96:99], a[0:3], v[20:23]\n"
"s_nop 7\n"
"v_max3_f32 v40, v40, v16, v17\n"
"v_max3_f32 v40, v40, v18, v19\n"
"v_max3_f32 v40, v40, v20, v21\n"
"v_max3_f32 v40, v40, v22, v23\n"
"v_mfma_f32_16x16x32_bf16 v[16:19], a[80:83], a[16:19], 0\n"
"v_mfma_f32_16x16x32_bf16 v[16:19], a[84:87], a[20:23], v[16:19]\n"
"v_mfma_f32_16x16x32_bf16 v[16:19], a[88:91], a[24:27], v[16:19]\n"
"v_mfma_f32_16x16x32_bf16 v[16:19], a[92:95], a[28:31], v[16:19]\n"
"v_mfma_f32_16x16x32_bf16 v[20:23], a[108:111], a[28:31], 0\n"
"v_mfma_f32_16x16x32_bf16 v[20:23], a[104:107], a[24:27], v[20:23]\n"
"v_mfma_f32_16x16x32_bf16 v[20:23], a[100:103], a[20:23], v[20:23]\n"
"v_mfma_f32_16x16x32_bf16 v[20:23], a[96:99], a[16:19], v[20:23]\n"
"s_nop 7\n"
"v_max3_f32 v44, v44, v16, v17\n"
"v_max3_f32 v44, v44, v18, v19\n"
"v_max3_f32 v44, v44, v20, v21\n"
"v_max3_f32 v44, v44, v22, v23\n"
"v_mfma_f32_16x16x32_bf16 v[16:19], a[80:83], a[32:35], 0\n"
"v_mfma_f32_16x16x32_bf16 v[16:19], a[84:87], a[36:39], v[16:19]\n"
"v_mfma_f32_16x16x32_bf16 v[16:19], a[88:91], a[40:43], v[16:19]\n"
"v_mfma_f32_16x16x32_bf16 v[16:19], a[92:95], a[44:47], v[16:19]\n"
"v_mfma_f32_16x16x32_bf16 v[20:23], a[108:111], a[44:47], 0\n"
"v_mfma_f32_16x16x32_bf16 v[20:23], a[104:107], a[40:43], v[20:23]\n"
"v_mfma_f32_16x16x32_bf16 v[20:23], a[100:103], a[36:39], v[20:23]\n"
"v_mfma_f32_16x16x32_bf16 v[20:23], a[96:99], a[32:35], v[20:23]\n"
"s_nop 7\n"
"v_max3_f32 v48, v48, v16, v17\n"
"v_max3_f32 v48, v48, v18, v19\n"
"v_max3_f32 v48, v48, v20, v21\n"
"v_max3_f32 v48, v48, v22, v23\n"
"v_mfma_f32_16x16x32_bf16 v[16:19], a[80:83], a[48:51], 0\n"
"v_mfma_f32_16x16x32_bf16 v[16:19], a[84:87], a[52:55], v[16:19]\n"
"v_mfma_f32_16x16x32_bf16 v[16:19], a[88:91], a[56:59], v[16:19]\n"
"v_mfma_f32_16x16x32_bf16 v[16:19], a[92:95], a[60:63], v[16:19]\n"
"v_mfma_f32_16x16x32_bf16 v[20:23], a[108:111], a[60:63], 0\n"
"v_mfma_f32_16x16x32_bf16 v[20:23], a[104:107], a[56:59], v[20:23]\n"
"v_mfma_f32_16x16x32_bf16 v[20:23], a[100:103], a[52:55], v[20:23]\n"
"v_mfma_f32_16x16x32_bf16 v[20:23], a[96:99], a[48:51], v[20:23]\n"
"s_nop 7\n"
"v_max3_f32 v52, v52, v16, v17\n"
"v_max3_f32 v52, v52, v18, v19\n"
"v_max3_f32 v52, v52, v20, v21\n"
"v_max3_f32 v52, v52, v22, v23\n"
"v_mfma_f32_16x16x32_bf16 v[16:19], a[80:83], a[64:67], 0\n"
"v_mfma_f32_16x16x32_bf16 v[16:19], a[84:87], a[68:71], v[16:19]\n"
"v_mfma_f32_16x16x32_bf16 v[16:19], a[88:91], a[72:75], v[16:19]\n"
"v_mfma_f32_16x16x32_bf16 v[16:19], a[92:95], a[76:79], v[16:19]\n"
"v_mfma_f32_16x16x32_bf16 v[20:23], a[108:111], a[76:79], 0\n"
"v_mfma_f32_16x16x32_bf16 v[20:23], a[104:107], a[72:75], v[20:23]\n"
"v_mfma_f32_16x16x32_bf16 v[20:23], a[100:103], a[68:71], v[20:23]\n"
"v_mfma_f32_16x16x32_bf16 v[20:23], a[96:99], a[64:67], v[20:23]\n"
"s_nop 7\n"
"v_max3_f32 v56, v56, v16, v17\n"
"v_max3_f32 v56, v56, v18, v19\n"
"v_max3_f32 v56, v56, v20, v21\n"
"v_max3_f32 v56, v56, v22, v23\n"
"s_add_u32 s85, s85, 8192\n"
"s_cmp_lg_u32 s85, 262144\n"
"s_cbranch_scc1 Lfb1_%=\n"
"v_mov_b32 v41, v40\n"
"s_nop 1\n"
"v_permlane16_swap_b32 v40, v41\n"
"v_max_f32 v40, v40, v41\n"
"v_mov_b32 v41, v40\n"
"s_nop 1\n"
"v_permlane32_swap_b32 v40, v41\n"
"v_max_f32 v40, v40, v41\n"
"v_sub_f32 v40, 0, v40\n"
"v_mov_b32 v41, v40\n"
"v_mov_b32 v42, v40\n"
"v_mov_b32 v43, v40\n"
"v_mov_b32 v45, v44\n"
"s_nop 1\n"
"v_permlane16_swap_b32 v44, v45\n"
"v_max_f32 v44, v44, v45\n"
"v_mov_b32 v45, v44\n"
"s_nop 1\n"
"v_permlane32_swap_b32 v44, v45\n"
"v_max_f32 v44, v44, v45\n"
"v_sub_f32 v44, 0, v44\n"
"v_mov_b32 v45, v44\n"
"v_mov_b32 v46, v44\n"
"v_mov_b32 v47, v44\n"
"v_mov_b32 v49, v48\n"
"s_nop 1\n"
"v_permlane16_swap_b32 v48, v49\n"
"v_max_f32 v48, v48, v49\n"
"v_mov_b32 v49, v48\n"
"s_nop 1\n"
"v_permlane32_swap_b32 v48, v49\n"
"v_max_f32 v48, v48, v49\n"
"v_sub_f32 v48, 0, v48\n"
"v_mov_b32 v49, v48\n"
"v_mov_b32 v50, v48\n"
"v_mov_b32 v51, v48\n"
"v_mov_b32 v53, v52\n"
"s_nop 1\n"
"v_permlane16_swap_b32 v52, v53\n"
"v_max_f32 v52, v52, v53\n"
"v_mov_b32 v53, v52\n"
"s_nop 1\n"
"v_permlane32_swap_b32 v52, v53\n"
"v_max_f32 v52, v52, v53\n"
"v_sub_f32 v52, 0, v52\n"
"v_mov_b32 v53, v52\n"
"v_mov_b32 v54, v52\n"
"v_mov_b32 v55, v52\n"
"v_mov_b32 v57, v56\n"
"s_nop 1\n"
"v_permlane16_swap_b32 v56, v57\n"
"v_max_f32 v56, v56, v57\n"
"v_mov_b32 v57, v56\n"
"s_nop 1\n"
"v_permlane32_swap_b32 v56, v57\n"
"v_max_f32 v56, v56, v57\n"
"v_sub_f32 v56, 0, v56\n"
"v_mov_b32 v57, v56\n"
"v_mov_b32 v58, v56\n"
"v_mov_b32 v59, v56\n"
"v_mov_b32 v60, 0\n"
"v_mov_b32 v61, 0\n"
"v_mov_b32 v62, 0\n"
"v_mov_b32 v63, 0\n"
"v_mov_b32 v64, 0\n"
"v_mov_b32 v65, 0\n"
"v_mov_b32 v66, 0\n"
"v_mov_b32 v67, 0\n"
"v_mov_b32 v68, 0\n"
"v_mov_b32 v69, 0\n"
"v_mov_b32 v70, 0\n"
"v_mov_b32 v71, 0\n"
"v_mov_b32 v72, 0\n"
"v_mov_b32 v73, 0\n"
"v_mov_b32 v74, 0\n"
"v_mov_b32 v75, 0\n"
"v_mov_b32 v76, 0\n"
"v_mov_b32 v77, 0\n"
"v_mov_b32 v78, 0\n"
"v_mov_b32 v79, 0\n"
"v_mov_b32 v80, 0\n"
"v_mov_b32 v81, 0\n"
"v_mov_b32 v82, 0\n"
"v_mov_b32 v83, 0\n"
"v_mov_b32 v84, 0\n"
"v_mov_b32 v85, 0\n"
"v_mov_b32 v86, 0\n"
"v_mov_b32 v87, 0\n"
"v_mov_b32 v88, 0\n"
"v_mov_b32 v89, 0\n"
"v_mov_b32 v90, 0\n"
"v_mov_b32 v91, 0\n"
"v_mov_b32 v92, 0\n"
"v_mov_b32 v93, 0\n"
"v_mov_b32 v94, 0\n"
"v_mov_b32 v95, 0\n"
"v_mov_b32 v96, 0\n"
"v_mov_b32 v97, 0\n"
"v_mov_b32 v98, 0\n"
"v_mov_b32 v99, 0\n"
"v_mov_b32 v100, 0\n"
"v_mov_b32 v101, 0\n"
"v_mov_b32 v102, 0\n"
"v_mov_b32 v103, 0\n"
"v_mov_b32 v104, 0\n"
"v_mov_b32 v105, 0\n"
"v_mov_b32 v106, 0\n"
"v_mov_b32 v107, 0\n"
"v_mov_b32 v108, 0\n"
"v_mov_b32 v109, 0\n"
"v_mov_b32 v110, 0\n"
"v_mov_b32 v111, 0\n"
"v_mov_b32 v112, 0\n"
"v_mov_b32 v113, 0\n"
"v_mov_b32 v114, 0\n"
"v_mov_b32 v115, 0\n"
"v_mov_b32 v116, 0\n"
"v_mov_b32 v117, 0\n"
"v_mov_b32 v118, 0\n"
"v_mov_b32 v119, 0\n"
"v_mov_b32 v120, 0\n"
"v_mov_b32 v121, 0\n"
"v_mov_b32 v122, 0\n"
"v_mov_b32 v123, 0\n"
"v_mov_b32 v124, 0\n"
"v_mov_b32 v125, 0\n"
"v_mov_b32 v126, 0\n"
"v_mov_b32 v127, 0\n"
"v_mov_b32 v128, 0\n"
"v_mov_b32 v129, 0\n"
"v_mov_b32 v130, 0\n"
"v_mov_b32 v131, 0\n"
"v_mov_b32 v132, 0\n"
"v_mov_b32 v133, 0\n"
"v_mov_b32 v134, 0\n"
"v_mov_b32 v135, 0\n"
"v_mov_b32 v136, 0\n"
"v_mov_b32 v137, 0\n"
"v_mov_b32 v138, 0\n"
"v_mov_b32 v139, 0\n"
"v_mov_b32 v140, 0\n"
"v_mov_b32 v141, 0\n"
"v_mov_b32 v142, 0\n"
"v_mov_b32 v143, 0\n"
"v_mov_b32 v144, 0\n"
"v_mov_b32 v145, 0\n"
"v_mov_b32 v146, 0\n"
"v_mov_b32 v147, 0\n"
"v_mov_b32 v148, 0\n"
"v_mov_b32 v149, 0\n"
"v_mov_b32 v150, 0\n"
"v_mov_b32 v151, 0\n"
"v_mov_b32 v152, 0\n"
"v_mov_b32 v153, 0\n"
"v_mov_b32 v154, 0\n"
"v_mov_b32 v155, 0\n"
"v_mov_b32 v156, 0\n"
"v_mov_b32 v157, 0\n"
"v_mov_b32 v158, 0\n"
"v_mov_b32 v159, 0\n"
"v_mov_b32 v160, 0\n"
"v_mov_b32 v161, 0\n"
"v_mov_b32 v162, 0\n"
"v_mov_b32 v163, 0\n"
"v_mov_b32 v164, 0\n"
"v_mov_b32 v165, 0\n"
"v_mov_b32 v166, 0\n"
"v_mov_b32 v167, 0\n"
"v_mov_b32 v168, 0\n"
"v_mov_b32 v169, 0\n"
"v_mov_b32 v170, 0\n"
"v_mov_b32 v171, 0\n"
"v_mov_b32 v172, 0\n"
"v_mov_b32 v173, 0\n"
"v_mov_b32 v174, 0\n"
"v_mov_b32 v175, 0\n"
"v_mov_b32 v176, 0\n"
"v_mov_b32 v177, 0\n"
"v_mov_b32 v178, 0\n"
"v_mov_b32 v179, 0\n"
"v_mov_b32 v180, 0\n"
"v_mov_b32 v181, 0\n"
"v_mov_b32 v182, 0\n"
"v_mov_b32 v183, 0\n"
"v_mov_b32 v184, 0\n"
"v_mov_b32 v185, 0\n"
"v_mov_b32 v186, 0\n"
"v_mov_b32 v187, 0\n"
"v_mov_b32 v188, 0\n"
"v_mov_b32 v189, 0\n"
"v_mov_b32 v190, 0\n"
"v_mov_b32 v191, 0\n"
"v_mov_b32 v192, 0\n"
"v_mov_b32 v193, 0\n"
"v_mov_b32 v194, 0\n"
"v_mov_b32 v195, 0\n"
"v_mov_b32 v196, 0\n"
"v_mov_b32 v197, 0\n"
"v_mov_b32 v198, 0\n"
"v_mov_b32 v199, 0\n"
"v_mov_b32 v200, 0\n"
"v_mov_b32 v201, 0\n"
"v_mov_b32 v202, 0\n"
"v_mov_b32 v203, 0\n"
"v_mov_b32 v204, 0\n"
"v_mov_b32 v205, 0\n"
"v_mov_b32 v206, 0\n"
"v_mov_b32 v207, 0\n"
"v_mov_b32 v208, 0\n"
"v_mov_b32 v209, 0\n"
"v_mov_b32 v210, 0\n"
"v_mov_b32 v211, 0\n"
"v_mov_b32 v212, 0\n"
"v_mov_b32 v213, 0\n"
"v_mov_b32 v214, 0\n"
"v_mov_b32 v215, 0\n"
"v_mov_b32 v216, 0\n"
"v_mov_b32 v217, 0\n"
"v_mov_b32 v218, 0\n"
"v_mov_b32 v219, 0\n"
"v_mov_b32 v220, 0\n"
"v_mov_b32 v221, 0\n"
"v_mov_b32 v222, 0\n"
"v_mov_b32 v223, 0\n"
"v_mov_b32 v224, 0\n"
"v_mov_b32 v225, 0\n"
"v_mov_b32 v226, 0\n"
"v_mov_b32 v227, 0\n"
"v_mov_b32 v228, 0\n"
"v_mov_b32 v229, 0\n"
"v_mov_b32 v230, 0\n"
"v_mov_b32 v231, 0\n"
"v_mov_b32 v232, 0\n"
"v_mov_b32 v233, 0\n"
"v_mov_b32 v234, 0\n"
"v_mov_b32 v235, 0\n"
"v_mov_b32 v236, 0\n"
"v_mov_b32 v237, 0\n"
"v_mov_b32 v238, 0\n"
"v_mov_b32 v239, 0\n"
"s_mov_b32 s85, 0\n"
"Lfb2_%=:\n"
"buffer_load_dwordx4 a[80:83], v1, s[44:47], s85 offen offset:0\n"
"buffer_load_dwordx4 a[84:87], v1, s[44:47], s85 offen offset:64\n"
"buffer_load_dwordx4 a[88:91], v1, s[44:47], s85 offen offset:128\n"
"buffer_load_dwordx4 a[92:95], v1, s[44:47], s85 offen offset:192\n"
"buffer_load_dwordx4 a[96:99], v2, s[44:47], s85 offen offset:0\n"
"buffer_load_dwordx4 a[100:103], v2, s[44:47], s85 offen offset:64\n"
"buffer_load_dwordx4 a[104:107], v2, s[44:47], s85 offen offset:128\n"
"buffer_load_dwordx4 a[108:111], v2, s[44:47], s85 offen offset:192\n"
"s_add_u32 s96, s85, 1024\n"
"s_add_u32 m0, s94, 0\n"
"s_nop 0\n"
"buffer_load_dwordx4 v8, s[48:51], s96 offen offset:0 lds\n"
"s_add_u32 s96, s85, 128\n"
"s_add_u32 m0, s94, 0\n"
"s_nop 0\n"
"buffer_load_dwordx4 v8, s[48:51], s96 offen offset:1024 lds\n"
"s_add_u32 s96, s85, 1024\n"
"s_add_u32 m0, s94, 0\n"
"s_nop 0\n"
"buffer_load_dwordx4 v8, s[48:51], s96 offen offset:2048 lds\n"
"s_add_u32 s96, s85, 128\n"
"s_add_u32 m0, s94, 0\n"
"s_nop 0\n"
"buffer_load_dwordx4 v8, s[48:51], s96 offen offset:3072 lds\n"
"s_add_u32 s96, s85, 5120\n"
"s_add_u32 m0, s94, 4096\n"
"s_nop 0\n"
"buffer_load_dwordx4 v8, s[48:51], s96 offen offset:0 lds\n"
"s_add_u32 s96, s85, 4224\n"
"s_add_u32 m0, s94, 4096\n"
"s_nop 0\n"
"buffer_load_dwordx4 v8, s[48:51], s96 offen offset:1024 lds\n"
"s_add_u32 s96, s85, 5120\n"
"s_add_u32 m0, s94, 4096\n"
"s_nop 0\n"
"buffer_load_dwordx4 v8, s[48:51], s96 offen offset:2048 lds\n"
"s_add_u32 s96, s85, 4224\n"
"s_add_u32 m0, s94, 4096\n"
"s_nop 0\n"
"buffer_load_dwordx4 v8, s[48:51], s96 offen offset:3072 lds\n"
"s_waitcnt vmcnt(0)\n"
"ds_read_b64_tr_b16 a[144:145], v3 offset:0\n"
"ds_read_b64_tr_b16 a[146:147], v3 offset:4096\n"
"ds_read_b64_tr_b16 a[148:149], v4 offset:0\n"
"ds_read_b64_tr_b16 a[150:151], v4 offset:4096\n"
"ds_read_b64_tr_b16 a[152:153], v3 offset:512\n"
"ds_read_b64_tr_b16 a[154:155], v3 offset:4608\n"
"ds_read_b64_tr_b16 a[156:157], v4 offset:512\n"
"ds_read_b64_tr_b16 a[158:159], v4 offset:4608\n"
"ds_read_b64_tr_b16 a[160:161], v3 offset:1024\n"
"ds_read_b64_tr_b16 a[162:163], v3 offset:5120\n"
"ds_read_b64_tr_b16 a[164:165], v4 offset:1024\n"
"ds_read_b64_tr_b16 a[166:167], v4 offset:5120\n"
"ds_read_b64_tr_b16 a[168:169], v3 offset:1536\n"
"ds_read_b64_tr_b16 a[170:171], v3 offset:5632\n"
"ds_read_b64_tr_b16 a[172:173], v4 offset:1536\n"
"ds_read_b64_tr_b16 a[174:175], v4 offset:5632\n"
"s_waitcnt lgkmcnt(0)\n"
"v_mfma_f32_16x16x32_bf16 v[16:19], a[80:83], a[0:3], v[40:43]\n"
"v_mfma_f32_16x16x32_bf16 v[16:19], a[84:87], a[4:7], v[16:19]\n"
"v_mfma_f32_16x16x32_bf16 v[16:19], a[88:91], a[8:11], v[16:19]\n"
"v_mfma_f32_16x16x32_bf16 v[16:19], a[92:95], a[12:15], v[16:19]\n"
"v_mfma_f32_16x16x32_bf16 v[20:23], a[108:111], a[12:15], v[40:43]\n"
"v_mfma_f32_16x16x32_bf16 v[20:23], a[104:107], a[8:11], v[20:23]\n"
"v_mfma_f32_16x16x32_bf16 v[20:23], a[100:103], a[4:7], v[20:23]\n"
"v_mfma_f32_16x16x32_bf16 v[20:23], a[96:99], a[0:3], v[20:23]\n"
"s_nop 7\n"
"v_exp_f32 v16, v16\n"
"v_exp_f32 v17, v17\n"
"v_exp_f32 v18, v18\n"
"v_exp_f32 v19, v19\n"
"v_exp_f32 v20, v20\n"
"v_exp_f32 v21, v21\n"
"v_exp_f32 v22, v22\n"
"v_exp_f32 v23, v23\n"
"s_nop 0\n"
"v_cvt_pk_bf16_f32 v32, v16, v17\n"
"v_cvt_pk_bf16_f32 v33, v18, v19\n"
"v_cvt_pk_bf16_f32 v34, v20, v21\n"
"v_cvt_pk_bf16_f32 v35, v22, v23\n"
"s_nop 1\n"
"v_mfma_f32_16x16x32_bf16 v[60:63], a[208:211], v[32:35], v[60:63]\n"
"v_mfma_f32_16x16x32_bf16 v[80:83], a[144:147], v[32:35], v[80:83]\n"
"v_mfma_f32_16x16x32_bf16 v[84:87], a[148:151], v[32:35], v[84:87]\n"
"v_mfma_f32_16x16x32_bf16 v[88:91], a[152:155], v[32:35], v[88:91]\n"
"v_mfma_f32_16x16x32_bf16 v[92:95], a[156:159], v[32:35], v[92:95]\n"
"v_mfma_f32_16x16x32_bf16 v[96:99], a[160:163], v[32:35], v[96:99]\n"
"v_mfma_f32_16x16x32_bf16 v[100:103], a[164:167], v[32:35], v[100:103]\n"
"v_mfma_f32_16x16x32_bf16 v[104:107], a[168:171], v[32:35], v[104:107]\n"
"v_mfma_f32_16x16x32_bf16 v[108:111], a[172:175], v[32:35], v[108:111]\n"
"v_mfma_f32_16x16x32_bf16 v[16:19], a[80:83], a[16:19], v[44:47]\n"
"v_mfma_f32_16x16x32_bf16 v[16:19], a[84:87], a[20:23], v[16:19]\n"
"v_mfma_f32_16x16x32_bf16 v[16:19], a[88:91], a[24:27], v[16:19]\n"
"v_mfma_f32_16x16x32_bf16 v[16:19], a[92:95], a[28:31], v[16:19]\n"
"v_mfma_f32_16x16x32_bf16 v[20:23], a[108:111], a[28:31], v[44:47]\n"
"v_mfma_f32_16x16x32_bf16 v[20:23], a[104:107], a[24:27], v[20:23]\n"
"v_mfma_f32_16x16x32_bf16 v[20:23], a[100:103], a[20:23], v[20:23]\n"
"v_mfma_f32_16x16x32_bf16 v[20:23], a[96:99], a[16:19], v[20:23]\n"
"s_nop 7\n"
"v_exp_f32 v16, v16\n"
"v_exp_f32 v17, v17\n"
"v_exp_f32 v18, v18\n"
"v_exp_f32 v19, v19\n"
"v_exp_f32 v20, v20\n"
"v_exp_f32 v21, v21\n"
"v_exp_f32 v22, v22\n"
"v_exp_f32 v23, v23\n"
"s_nop 0\n"
"v_cvt_pk_bf16_f32 v32, v16, v17\n"
"v_cvt_pk_bf16_f32 v33, v18, v19\n"
"v_cvt_pk_bf16_f32 v34, v20, v21\n"
"v_cvt_pk_bf16_f32 v35, v22, v23\n"
"s_nop 1\n"
"v_mfma_f32_16x16x32_bf16 v[64:67], a[208:211], v[32:35], v[64:67]\n"
"v_mfma_f32_16x16x32_bf16 v[112:115], a[144:147], v[32:35], v[112:115]\n"
"v_mfma_f32_16x16x32_bf16 v[116:119], a[148:151], v[32:35], v[116:119]\n"
"v_mfma_f32_16x16x32_bf16 v[120:123], a[152:155], v[32:35], v[120:123]\n"
"v_mfma_f32_16x16x32_bf16 v[124:127], a[156:159], v[32:35], v[124:127]\n"
"v_mfma_f32_16x16x32_bf16 v[128:131], a[160:163], v[32:35], v[128:131]\n"
"v_mfma_f32_16x16x32_bf16 v[132:135], a[164:167], v[32:35], v[132:135]\n"
"v_mfma_f32_16x16x32_bf16 v[136:139], a[168:171], v[32:35], v[136:139]\n"
"v_mfma_f32_16x16x32_bf16 v[140:143], a[172:175], v[32:35], v[140:143]\n"
"v_mfma_f32_16x16x32_bf16 v[16:19], a[80:83], a[32:35], v[48:51]\n"
"v_mfma_f32_16x16x32_bf16 v[16:19], a[84:87], a[36:39], v[16:19]\n"
"v_mfma_f32_16x16x32_bf16 v[16:19], a[88:91], a[40:43], v[16:19]\n"
"v_mfma_f32_16x16x32_bf16 v[16:19], a[92:95], a[44:47], v[16:19]\n"
"v_mfma_f32_16x16x32_bf16 v[20:23], a[108:111], a[44:47], v[48:51]\n"
"v_mfma_f32_16x16x32_bf16 v[20:23], a[104:107], a[40:43], v[20:23]\n"
"v_mfma_f32_16x16x32_bf16 v[20:23], a[100:103], a[36:39], v[20:23]\n"
"v_mfma_f32_16x16x32_bf16 v[20:23], a[96:99], a[32:35], v[20:23]\n"
"s_nop 7\n"
"v_exp_f32 v16, v16\n"
"v_exp_f32 v17, v17\n"
"v_exp_f32 v18, v18\n"
"v_exp_f32 v19, v19\n"
"v_exp_f32 v20, v20\n"
"v_exp_f32 v21, v21\n"
"v_exp_f32 v22, v22\n"
"v_exp_f32 v23, v23\n"
"s_nop 0\n"
"v_cvt_pk_bf16_f32 v32, v16, v17\n"
"v_cvt_pk_bf16_f32 v33, v18, v19\n"
"v_cvt_pk_bf16_f32 v34, v20, v21\n"
"v_cvt_pk_bf16_f32 v35, v22, v23\n"
"s_nop 1\n"
"v_mfma_f32_16x16x32_bf16 v[68:71], a[208:211], v[32:35], v[68:71]\n"
"v_mfma_f32_16x16x32_bf16 v[144:147], a[144:147], v[32:35], v[144:147]\n"
"v_mfma_f32_16x16x32_bf16 v[148:151], a[148:151], v[32:35], v[148:151]\n"
"v_mfma_f32_16x16x32_bf16 v[152:155], a[152:155], v[32:35], v[152:155]\n"
"v_mfma_f32_16x16x32_bf16 v[156:159], a[156:159], v[32:35], v[156:159]\n"
"v_mfma_f32_16x16x32_bf16 v[160:163], a[160:163], v[32:35], v[160:163]\n"
"v_mfma_f32_16x16x32_bf16 v[164:167], a[164:167], v[32:35], v[164:167]\n"
"v_mfma_f32_16x16x32_bf16 v[168:171], a[168:171], v[32:35], v[168:171]\n"
"v_mfma_f32_16x16x32_bf16 v[172:175], a[172:175], v[32:35], v[172:175]\n"
"v_mfma_f32_16x16x32_bf16 v[16:19], a[80:83], a[48:51], v[52:55]\n"
"v_mfma_f32_16x16x32_bf16 v[16:19], a[84:87], a[52:55], v[16:19]\n"
"v_mfma_f32_16x16x32_bf16 v[16:19], a[88:91], a[56:59], v[16:19]\n"
"v_mfma_f32_16x16x32_bf16 v[16:19], a[92:95], a[60:63], v[16:19]\n"
"v_mfma_f32_16x16x32_bf16 v[20:23], a[108:111], a[60:63], v[52:55]\n"
"v_mfma_f32_16x16x32_bf16 v[20:23], a[104:107], a[56:59], v[20:23]\n"
"v_mfma_f32_16x16x32_bf16 v[20:23], a[100:103], a[52:55], v[20:23]\n"
"v_mfma_f32_16x16x32_bf16 v[20:23], a[96:99], a[48:51], v[20:23]\n"
"s_nop 7\n"
"v_exp_f32 v16, v16\n"
"v_exp_f32 v17, v17\n"
"v_exp_f32 v18, v18\n"
"v_exp_f32 v19, v19\n"
"v_exp_f32 v20, v20\n"
"v_exp_f32 v21, v21\n"
"v_exp_f32 v22, v22\n"
"v_exp_f32 v23, v23\n"
"s_nop 0\n"
"v_cvt_pk_bf16_f32 v32, v16, v17\n"
"v_cvt_pk_bf16_f32 v33, v18, v19\n"
"v_cvt_pk_bf16_f32 v34, v20, v21\n"
"v_cvt_pk_bf16_f32 v35, v22, v23\n"
"s_nop 1\n"
"v_mfma_f32_16x16x32_bf16 v[72:75], a[208:211], v[32:35], v[72:75]\n"
"v_mfma_f32_16x16x32_bf16 v[176:179], a[144:147], v[32:35], v[176:179]\n"
"v_mfma_f32_16x16x32_bf16 v[180:183], a[148:151], v[32:35], v[180:183]\n"
"v_mfma_f32_16x16x32_bf16 v[184:187], a[152:155], v[32:35], v[184:187]\n"
"v_mfma_f32_16x16x32_bf16 v[188:191], a[156:159], v[32:35], v[188:191]\n"
"v_mfma_f32_16x16x32_bf16 v[192:195], a[160:163], v[32:35], v[192:195]\n"
"v_mfma_f32_16x16x32_bf16 v[196:199], a[164:167], v[32:35], v[196:199]\n"
"v_mfma_f32_16x16x32_bf16 v[200:203], a[168:171], v[32:35], v[200:203]\n"
"v_mfma_f32_16x16x32_bf16 v[204:207], a[172:175], v[32:35], v[204:207]\n"
"v_mfma_f32_16x16x32_bf16 v[16:19], a[80:83], a[64:67], v[56:59]\n"
"v_mfma_f32_16x16x32_bf16 v[16:19], a[84:87], a[68:71], v[16:19]\n"
"v_mfma_f32_16x16x32_bf16 v[16:19], a[88:91], a[72:75], v[16:19]\n"
"v_mfma_f32_16x16x32_bf16 v[16:19], a[92:95], a[76:79], v[16:19]\n"
"v_mfma_f32_16x16x32_bf16 v[20:23], a[108:111], a[76:79], v[56:59]\n"
"v_mfma_f32_16x16x32_bf16 v[20:23], a[104:107], a[72:75], v[20:23]\n"
"v_mfma_f32_16x16x32_bf16 v[20:23], a[100:103], a[68:71], v[20:23]\n"
"v_mfma_f32_16x16x32_bf16 v[20:23], a[96:99], a[64:67], v[20:23]\n"
"s_nop 7\n"
"v_exp_f32 v16, v16\n"
"v_exp_f32 v17, v17\n"
"v_exp_f32 v18, v18\n"
"v_exp_f32 v19, v19\n"
"v_exp_f32 v20, v20\n"
"v_exp_f32 v21, v21\n"
"v_exp_f32 v22, v22\n"
"v_exp_f32 v23, v23\n"
"s_nop 0\n"
"v_cvt_pk_bf16_f32 v32, v16, v17\n"
"v_cvt_pk_bf16_f32 v33, v18, v19\n"
"v_cvt_pk_bf16_f32 v34, v20, v21\n"
"v_cvt_pk_bf16_f32 v35, v22, v23\n"
"s_nop 1\n"
"v_mfma_f32_16x16x32_bf16 v[76:79], a[208:211], v[32:35], v[76:79]\n"
"v_mfma_f32_16x16x32_bf16 v[208:211], a[144:147], v[32:35], v[208:211]\n"
"v_mfma_f32_16x16x32_bf16 v[212:215], a[148:151], v[32:35], v[212:215]\n"
"v_mfma_f32_16x16x32_bf16 v[216:219], a[152:155], v[32:35], v[216:219]\n"
"v_mfma_f32_16x16x32_bf16 v[220:223], a[156:159], v[32:35], v[220:223]\n"
"v_mfma_f32_16x16x32_bf16 v[224:227], a[160:163], v[32:35], v[224:227]\n"
"v_mfma_f32_16x16x32_bf16 v[228:231], a[164:167], v[32:35], v[228:231]\n"
"v_mfma_f32_16x16x32_bf16 v[232:235], a[168:171], v[32:35], v[232:235]\n"
"v_mfma_f32_16x16x32_bf16 v[236:239], a[172:175], v[32:35], v[236:239]\n"
"s_add_u32 s85, s85, 8192\n"
"s_cmp_lg_u32 s85, 262144\n"
"s_cbranch_scc1 Lfb2_%=\n"
"s_nop 7\n"
"s_mov_b32 s96, 0\n"
"v_rcp_f32 v44, v60\n"
"v_cmp_nle_f32 vcc, v60, v11\n"
"v_mov_b32 v45, v44\n"
"s_or_b64 s[98:99], s[98:99], vcc\n"
"v_pk_mul_f32 v[80:81], v[80:81], v[44:45]\n"
"v_pk_mul_f32 v[82:83], v[82:83], v[44:45]\n"
"v_pk_mul_f32 v[84:85], v[84:85], v[44:45]\n"
"v_pk_mul_f32 v[86:87], v[86:87], v[44:45]\n"
"v_cvt_pk_bf16_f32 v240, v80, v81\n"
"v_cvt_pk_bf16_f32 v241, v82, v83\n"
"v_cvt_pk_bf16_f32 v242, v84, v85\n"
"v_cvt_pk_bf16_f32 v243, v86, v87\n"
"buffer_store_dwordx4 v[240:243], v9, s[52:55], s96 offen offset:0 sc1\n"
"v_pk_mul_f32 v[88:89], v[88:89], v[44:45]\n"
"v_pk_mul_f32 v[90:91], v[90:91], v[44:45]\n"
"v_pk_mul_f32 v[92:93], v[92:93], v[44:45]\n"
"v_pk_mul_f32 v[94:95], v[94:95], v[44:45]\n"
"v_cvt_pk_bf16_f32 v252, v88, v89\n"
"v_cvt_pk_bf16_f32 v253, v90, v91\n"
"v_cvt_pk_bf16_f32 v254, v92, v93\n"
"v_cvt_pk_bf16_f32 v255, v94, v95\n"
"buffer_store_dwordx4 v[252:255], v9, s[52:55], s96 offen offset:1024 sc1\n"
"v_pk_mul_f32 v[96:97], v[96:97], v[44:45]\n"
"v_pk_mul_f32 v[98:99], v[98:99], v[44:45]\n"
"v_pk_mul_f32 v[100:101], v[100:101], v[44:45]\n"
"v_pk_mul_f32 v[102:103], v[102:103], v[44:45]\n"
"v_cvt_pk_bf16_f32 v40, v96, v97\n"
"v_cvt_pk_bf16_f32 v41, v98, v99\n"
"v_cvt_pk_bf16_f32 v42, v100, v101\n"
"v_cvt_pk_bf16_f32 v43, v102, v103\n"
"buffer_store_dwordx4 v[40:43], v9, s[52:55], s96 offen offset:2048 sc1\n"
"v_pk_mul_f32 v[104:105], v[104:105], v[44:45]\n"
"v_pk_mul_f32 v[106:107], v[106:107], v[44:45]\n"
"v_pk_mul_f32 v[108:109], v[108:109], v[44:45]\n"
"v_pk_mul_f32 v[110:111], v[110:111], v[44:45]\n"
"v_cvt_pk_bf16_f32 v240, v104, v105\n"
"v_cvt_pk_bf16_f32 v241, v106, v107\n"
"v_cvt_pk_bf16_f32 v242, v108, v109\n"
"v_cvt_pk_bf16_f32 v243, v110, v111\n"
"buffer_store_dwordx4 v[240:243], v9, s[52:55], s96 offen offset:3072 sc1\n"
"s_mov_b32 s97, 131072\n"
"v_rcp_f32 v46, v64\n"
"v_cmp_nle_f32 vcc, v64, v11\n"
"v_mov_b32 v47, v46\n"
"s_or_b64 s[98:99], s[98:99], vcc\n"
"v_pk_mul_f32 v[112:113], v[112:113], v[46:47]\n"
"v_pk_mul_f32 v[114:115], v[114:115], v[46:47]\n"
"v_pk_mul_f32 v[116:117], v[116:117], v[46:47]\n"
"v_pk_mul_f32 v[118:119], v[118:119], v[46:47]\n"
"v_cvt_pk_bf16_f32 v252, v112, v113\n"
"v_cvt_pk_bf16_f32 v253, v114, v115\n"
"v_cvt_pk_bf16_f32 v254, v116, v117\n"
"v_cvt_pk_bf16_f32 v255, v118, v119\n"
"buffer_store_dwordx4 v[252:255], v9, s[52:55], s97 offen offset:0 sc1\n"
"v_pk_mul_f32 v[120:121], v[120:121], v[46:47]\n"
"v_pk_mul_f32 v[122:123], v[122:123], v[46:47]\n"
"v_pk_mul_f32 v[124:125], v[124:125], v[46:47]\n"
"v_pk_mul_f32 v[126:127], v[126:127], v[46:47]\n"
"v_cvt_pk_bf16_f32 v40, v120, v121\n"
"v_cvt_pk_bf16_f32 v41, v122, v123\n"
"v_cvt_pk_bf16_f32 v42, v124, v125\n"
"v_cvt_pk_bf16_f32 v43, v126, v127\n"
"buffer_store_dwordx4 v[40:43], v9, s[52:55], s97 offen offset:1024 sc1\n"
"v_pk_mul_f32 v[128:129], v[128:129], v[46:47]\n"
"v_pk_mul_f32 v[130:131], v[130:131], v[46:47]\n"
"v_pk_mul_f32 v[132:133], v[132:133], v[46:47]\n"
"v_pk_mul_f32 v[134:135], v[134:135], v[46:47]\n"
"v_cvt_pk_bf16_f32 v240, v128, v129\n"
"v_cvt_pk_bf16_f32 v241, v130, v131\n"
"v_cvt_pk_bf16_f32 v242, v132, v133\n"
"v_cvt_pk_bf16_f32 v243, v134, v135\n"
"buffer_store_dwordx4 v[240:243], v9, s[52:55], s97 offen offset:2048 sc1\n"
"v_pk_mul_f32 v[136:137], v[136:137], v[46:47]\n"
"v_pk_mul_f32 v[138:139], v[138:139], v[46:47]\n"
"v_pk_mul_f32 v[140:141], v[140:141], v[46:47]\n"
"v_pk_mul_f32 v[142:143], v[142:143], v[46:47]\n"
"v_cvt_pk_bf16_f32 v252, v136, v137\n"
"v_cvt_pk_bf16_f32 v253, v138, v139\n"
"v_cvt_pk_bf16_f32 v254, v140, v141\n"
"v_cvt_pk_bf16_f32 v255, v142, v143\n"
"buffer_store_dwordx4 v[252:255], v9, s[52:55], s97 offen offset:3072 sc1\n"
"s_mov_b32 s96, 262144\n"
"v_rcp_f32 v44, v68\n"
"v_cmp_nle_f32 vcc, v68, v11\n"
"v_mov_b32 v45, v44\n"
"s_or_b64 s[98:99], s[98:99], vcc\n"
"v_pk_mul_f32 v[144:145], v[144:145], v[44:45]\n"
"v_pk_mul_f32 v[146:147], v[146:147], v[44:45]\n"
"v_pk_mul_f32 v[148:149], v[148:149], v[44:45]\n"
"v_pk_mul_f32 v[150:151], v[150:151], v[44:45]\n"
"v_cvt_pk_bf16_f32 v40, v144, v145\n"
"v_cvt_pk_bf16_f32 v41, v146, v147\n"
"v_cvt_pk_bf16_f32 v42, v148, v149\n"
"v_cvt_pk_bf16_f32 v43, v150, v151\n"
"buffer_store_dwordx4 v[40:43], v9, s[52:55], s96 offen offset:0 sc1\n"
"v_pk_mul_f32 v[152:153], v[152:153], v[44:45]\n"
"v_pk_mul_f32 v[154:155], v[154:155], v[44:45]\n"
"v_pk_mul_f32 v[156:157], v[156:157], v[44:45]\n"
"v_pk_mul_f32 v[158:159], v[158:159], v[44:45]\n"
"v_cvt_pk_bf16_f32 v240, v152, v153\n"
"v_cvt_pk_bf16_f32 v241, v154, v155\n"
"v_cvt_pk_bf16_f32 v242, v156, v157\n"
"v_cvt_pk_bf16_f32 v243, v158, v159\n"
"buffer_store_dwordx4 v[240:243], v9, s[52:55], s96 offen offset:1024 sc1\n"
"v_pk_mul_f32 v[160:161], v[160:161], v[44:45]\n"
"v_pk_mul_f32 v[162:163], v[162:163], v[44:45]\n"
"v_pk_mul_f32 v[164:165], v[164:165], v[44:45]\n"
"v_pk_mul_f32 v[166:167], v[166:167], v[44:45]\n"
"v_cvt_pk_bf16_f32 v252, v160, v161\n"
"v_cvt_pk_bf16_f32 v253, v162, v163\n"
"v_cvt_pk_bf16_f32 v254, v164, v165\n"
"v_cvt_pk_bf16_f32 v255, v166, v167\n"
"buffer_store_dwordx4 v[252:255], v9, s[52:55], s96 offen offset:2048 sc1\n"
"v_pk_mul_f32 v[168:169], v[168:169], v[44:45]\n"
"v_pk_mul_f32 v[170:171], v[170:171], v[44:45]\n"
"v_pk_mul_f32 v[172:173], v[172:173], v[44:45]\n"
"v_pk_mul_f32 v[174:175], v[174:175], v[44:45]\n"
"v_cvt_pk_bf16_f32 v40, v168, v169\n"
"v_cvt_pk_bf16_f32 v41, v170, v171\n"
"v_cvt_pk_bf16_f32 v42, v172, v173\n"
"v_cvt_pk_bf16_f32 v43, v174, v175\n"
"buffer_store_dwordx4 v[40:43], v9, s[52:55], s96 offen offset:3072 sc1\n"
"s_mov_b32 s97, 393216\n"
"v_rcp_f32 v46, v72\n"
"v_cmp_nle_f32 vcc, v72, v11\n"
"v_mov_b32 v47, v46\n"
"s_or_b64 s[98:99], s[98:99], vcc\n"
"v_pk_mul_f32 v[176:177], v[176:177], v[46:47]\n"
"v_pk_mul_f32 v[178:179], v[178:179], v[46:47]\n"
"v_pk_mul_f32 v[180:181], v[180:181], v[46:47]\n"
"v_pk_mul_f32 v[182:183], v[182:183], v[46:47]\n"
"v_cvt_pk_bf16_f32 v240, v176, v177\n"
"v_cvt_pk_bf16_f32 v241, v178, v179\n"
"v_cvt_pk_bf16_f32 v242, v180, v181\n"
"v_cvt_pk_bf16_f32 v243, v182, v183\n"
"buffer_store_dwordx4 v[240:243], v9, s[52:55], s97 offen offset:0 sc1\n"
"v_pk_mul_f32 v[184:185], v[184:185], v[46:47]\n"
"v_pk_mul_f32 v[186:187], v[186:187], v[46:47]\n"
"v_pk_mul_f32 v[188:189], v[188:189], v[46:47]\n"
"v_pk_mul_f32 v[190:191], v[190:191], v[46:47]\n"
"v_cvt_pk_bf16_f32 v252, v184, v185\n"
"v_cvt_pk_bf16_f32 v253, v186, v187\n"
"v_cvt_pk_bf16_f32 v254, v188, v189\n"
"v_cvt_pk_bf16_f32 v255, v190, v191\n"
"buffer_store_dwordx4 v[252:255], v9, s[52:55], s97 offen offset:1024 sc1\n"
"v_pk_mul_f32 v[192:193], v[192:193], v[46:47]\n"
"v_pk_mul_f32 v[194:195], v[194:195], v[46:47]\n"
"v_pk_mul_f32 v[196:197], v[196:197], v[46:47]\n"
"v_pk_mul_f32 v[198:199], v[198:199], v[46:47]\n"
"v_cvt_pk_bf16_f32 v40, v192, v193\n"
"v_cvt_pk_bf16_f32 v41, v194, v195\n"
"v_cvt_pk_bf16_f32 v42, v196, v197\n"
"v_cvt_pk_bf16_f32 v43, v198, v199\n"
"buffer_store_dwordx4 v[40:43], v9, s[52:55], s97 offen offset:2048 sc1\n"
"v_pk_mul_f32 v[200:201], v[200:201], v[46:47]\n"
"v_pk_mul_f32 v[202:203], v[202:203], v[46:47]\n"
"v_pk_mul_f32 v[204:205], v[204:205], v[46:47]\n"
"v_pk_mul_f32 v[206:207], v[206:207], v[46:47]\n"
"v_cvt_pk_bf16_f32 v240, v200, v201\n"
"v_cvt_pk_bf16_f32 v241, v202, v203\n"
"v_cvt_pk_bf16_f32 v242, v204, v205\n"
"v_cvt_pk_bf16_f32 v243, v206, v207\n"
"buffer_store_dwordx4 v[240:243], v9, s[52:55], s97 offen offset:3072 sc1\n"
"s_mov_b32 s96, 524288\n"
"v_rcp_f32 v44, v76\n"
"v_cmp_nle_f32 vcc, v76, v11\n"
"v_mov_b32 v45, v44\n"
"s_or_b64 s[98:99], s[98:99], vcc\n"
"v_pk_mul_f32 v[208:209], v[208:209], v[44:45]\n"
"v_pk_mul_f32 v[210:211], v[210:211], v[44:45]\n"
"v_pk_mul_f32 v[212:213], v[212:213], v[44:45]\n"
"v_pk_mul_f32 v[214:215], v[214:215], v[44:45]\n"
"v_cvt_pk_bf16_f32 v252, v208, v209\n"
"v_cvt_pk_bf16_f32 v253, v210, v211\n"
"v_cvt_pk_bf16_f32 v254, v212, v213\n"
"v_cvt_pk_bf16_f32 v255, v214, v215\n"
"buffer_store_dwordx4 v[252:255], v9, s[52:55], s96 offen offset:0 sc1\n"
"v_pk_mul_f32 v[216:217], v[216:217], v[44:45]\n"
"v_pk_mul_f32 v[218:219], v[218:219], v[44:45]\n"
"v_pk_mul_f32 v[220:221], v[220:221], v[44:45]\n"
"v_pk_mul_f32 v[222:223], v[222:223], v[44:45]\n"
"v_cvt_pk_bf16_f32 v40, v216, v217\n"
"v_cvt_pk_bf16_f32 v41, v218, v219\n"
"v_cvt_pk_bf16_f32 v42, v220, v221\n"
"v_cvt_pk_bf16_f32 v43, v222, v223\n"
"buffer_store_dwordx4 v[40:43], v9, s[52:55], s96 offen offset:1024 sc1\n"
"v_pk_mul_f32 v[224:225], v[224:225], v[44:45]\n"
"v_pk_mul_f32 v[226:227], v[226:227], v[44:45]\n"
"v_pk_mul_f32 v[228:229], v[228:229], v[44:45]\n"
"v_pk_mul_f32 v[230:231], v[230:231], v[44:45]\n"
"v_cvt_pk_bf16_f32 v240, v224, v225\n"
"v_cvt_pk_bf16_f32 v241, v226, v227\n"
"v_cvt_pk_bf16_f32 v242, v228, v229\n"
"v_cvt_pk_bf16_f32 v243, v230, v231\n"
"buffer_store_dwordx4 v[240:243], v9, s[52:55], s96 offen offset:2048 sc1\n"
"v_pk_mul_f32 v[232:233], v[232:233], v[44:45]\n"
"v_pk_mul_f32 v[234:235], v[234:235], v[44:45]\n"
"v_pk_mul_f32 v[236:237], v[236:237], v[44:45]\n"
"v_pk_mul_f32 v[238:239], v[238:239], v[44:45]\n"
"v_cvt_pk_bf16_f32 v252, v232, v233\n"
"v_cvt_pk_bf16_f32 v253, v234, v235\n"
"v_cvt_pk_bf16_f32 v254, v236, v237\n"
"v_cvt_pk_bf16_f32 v255, v238, v239\n"
"buffer_store_dwordx4 v[252:255], v9, s[52:55], s96 offen offset:3072 sc1\n"
"s_mov_b32 s95, 11264\n"
"s_add_u32 m0, s94, 0\n"
"s_nop 0\n"
"buffer_load_dwordx4 v9, s[56:59], s95 offen offset:0 sc1 lds\n"
"s_mov_b32 s95, 11264\n"
"buffer_load_dwordx4 v9, s[56:59], s95 offen offset:1024 sc1 lds\n"
"s_mov_b32 s95, 11264\n"
"buffer_load_dwordx4 v9, s[56:59], s95 offen offset:2048 sc1 lds\n"
"s_mov_b32 s95, 11264\n"
"buffer_load_dwordx4 v9, s[56:59], s95 offen offset:3072 sc1 lds\n"
"s_mov_b32 s95, 15360\n"
"s_add_u32 m0, s94, 4096\n"
"s_nop 0\n"
"buffer_load_dwordx4 v9, s[56:59], s95 offen offset:0 sc1 lds\n"
"s_mov_b32 s95, 15360\n"
"buffer_load_dwordx4 v9, s[56:59], s95 offen offset:1024 sc1 lds\n"
"s_mov_b32 s95, 15360\n"
"buffer_load_dwordx4 v9, s[56:59], s95 offen offset:2048 sc1 lds\n"
"s_mov_b32 s95, 15360\n"
"buffer_load_dwordx4 v9, s[56:59], s95 offen offset:3072 sc1 lds\n"
"s_waitcnt vmcnt(0)\n"
"s_branch Lfbdone_%=\n"
"Lend_%=:\n"
    :: [ka] "s"(__builtin_amdgcn_kernarg_segment_ptr()), [bid] "s"(blockIdx.x), [wid] "s"(wid)
    : "memory", "scc", "vcc", "v0","v1","v2","v3","v4","v5","v6","v7","v8","v9","v10","v11","v12","v13","v14","v15","v16","v17","v18","v19","v20","v21","v22","v23","v24","v25","v26","v27","v28","v29","v30","v31","v32","v33","v34","v35","v36","v37","v38","v39","v40","v41","v42","v43","v44","v45","v46","v47","v48","v49","v50","v51","v52","v53","v54","v55","v56","v57","v58","v59","v60","v61","v62","v63","v64","v65","v66","v67","v68","v69","v70","v71","v72","v73","v74","v75","v76","v77","v78","v79","v80","v81","v82","v83","v84","v85","v86","v87","v88","v89","v90","v91","v92","v93","v94","v95","v96","v97","v98","v99","v100","v101","v102","v103","v104","v105","v106","v107","v108","v109","v110","v111","v112","v113","v114","v115","v116","v117","v118","v119","v120","v121","v122","v123","v124","v125","v126","v127","v128","v129","v130","v131","v132","v133","v134","v135","v136","v137","v138","v139","v140","v141","v142","v143","v144","v145","v146","v147","v148","v149","v150","v151","v152","v153","v154","v155","v156","v157","v158","v159","v160","v161","v162","v163","v164","v165","v166","v167","v168","v169","v170","v171","v172","v173","v174","v175","v176","v177","v178","v179","v180","v181","v182","v183","v184","v185","v186","v187","v188","v189","v190","v191","v192","v193","v194","v195","v196","v197","v198","v199","v200","v201","v202","v203","v204","v205","v206","v207","v208","v209","v210","v211","v212","v213","v214","v215","v216","v217","v218","v219","v220","v221","v222","v223","v224","v225","v226","v227","v228","v229","v230","v231","v232","v233","v234","v235","v236","v237","v238","v239","v240","v241","v242","v243","v244","v245","v246","v247","v248","v249","v250","v251","v252","v253","v254","v255","a0","a1","a2","a3","a4","a5","a6","a7","a8","a9","a10","a11","a12","a13","a14","a15","a16","a17","a18","a19","a20","a21","a22","a23","a24","a25","a26","a27","a28","a29","a30","a31","a32","a33","a34","a35","a36","a37","a38","a39","a40","a41","a42","a43","a44","a45","a46","a47","a48","a49","a50","a51","a52","a53","a54","a55","a56","a57","a58","a59","a60","a61","a62","a63","a64","a65","a66","a67","a68","a69","a70","a71","a72","a73","a74","a75","a76","a77","a78","a79","a80","a81","a82","a83","a84","a85","a86","a87","a88","a89","a90","a91","a92","a93","a94","a95","a96","a97","a98","a99","a100","a101","a102","a103","a104","a105","a106","a107","a108","a109","a110","a111","a112","a113","a114","a115","a116","a117","a118","a119","a120","a121","a122","a123","a124","a125","a126","a127","a128","a129","a130","a131","a132","a133","a134","a135","a136","a137","a138","a139","a140","a141","a142","a143","a144","a145","a146","a147","a148","a149","a150","a151","a152","a153","a154","a155","a156","a157","a158","a159","a160","a161","a162","a163","a164","a165","a166","a167","a168","a169","a170","a171","a172","a173","a174","a175","a176","a177","a178","a179","a180","a181","a182","a183","a184","a185","a186","a187","a188","a189","a190","a191","a192","a193","a194","a195","a196","a197","a198","a199","a200","a201","a202","a203","a204","a205","a206","a207","a208","a209","a210","a211","a212","a213","a214","a215","a216","a217","a218","a219","a220","a221","a222","a223","a224","a225","a226","a227","a228","a229","a230","a231","a232","a233","a234","a235","a236","a237","a238","a239","a240","a241","a242","a243","a244","a245","a246","a247","a248","a249","a250","a251","a252","a253","a254","a255","s4","s5","s6","s7","s8","s9","s10","s11","s12","s13","s14","s15","s16","s17","s18","s19","s20","s21","s22","s23","s24","s25","s26","s27","s28","s29","s30","s31","s34","s35","s36","s37","s38","s39","s40","s41","s42","s43","s44","s45","s46","s47","s48","s49","s50","s51","s52","s53","s54","s55","s56","s57","s58","s59","s60","s61","s62","s63","s64","s65","s66","s67","s68","s69","s70","s71","s72","s73","s74","s75","s76","s77","s78","s79","s80","s81","s82","s83","s84","s85","s86","s87","s88","s89","s90","s91","s92","s93","s94","s95","s96","s97","s98","s99");
}

extern "C" void kernel_launch(void* const* d_in, const int* in_sizes, int n_in, void* d_out, int out_size, void* d_ws, size_t ws_size, hipStream_t stream) {
  (void)hipFuncSetAttribute((const void*)gqa_fused, hipFuncAttributeMaxDynamicSharedMemorySize, 163840);
  gqa_fused<<<256, 256, 163840, stream>>>((const char*)d_in[0], (const char*)d_in[1], (const char*)d_in[2], (const char*)d_in[3], (const char*)d_in[4],
                                          (const char*)d_in[5], (const char*)d_in[6], (const char*)d_in[7], (const char*)d_in[8], (const char*)d_in[9],
                                          (char*)d_out, (char*)d_ws);
}
```
